```python
import torch

def _softmax(x, axis=-1):
    return torch.softmax(x, dim=axis)

def _gelu(x):
    return torch.nn.functional.gelu(x, approximate='tanh')

def _silu(x):
    return torch.nn.functional.silu(x)

def _sigmoid(x):
    return torch.sigmoid(x)

def _layernorm(x, axis=-1, eps=1e-05):
    m = x.mean(dim=axis, keepdim=True)
    v = x.var(dim=axis, keepdim=True, unbiased=False)
    return (x - m) / torch.sqrt(v + eps)

def _scatter_add(tgt, idx, src, axis=0):
    return tgt.clone().index_add_(axis, idx, src)

def _segment_sum(src, seg, n):
    out = torch.zeros((n,) + src.shape[1:], dtype=src.dtype)
    return out.index_add_(0, seg, src)

def _pad(x, pw):
    flat = tuple((v for pair in reversed(pw) for v in pair))
    return torch.nn.functional.pad(x, flat)
kernel_name = 'standalone__autoregressive_conv_wavenet__speech'

def setup_inputs():

    _w = lambda a: (lambda t: t.to(torch.bfloat16) if t.is_floating_point() else t)(torch.as_tensor(a))
    B, T, D, K = (2, 16000, 256, 5)
    return {'x': _w((lambda *s: torch.randn(*s, dtype=torch.bfloat16) * 0.1)(B, T, D)), 'w': _w((lambda *s: torch.randn(*s, dtype=torch.bfloat16) * 0.1)(K, D, 2 * D)), 'w_o': _w((lambda *s: torch.randn(*s, dtype=torch.bfloat16) * 0.05)(D, D))}

def reference(x, w, w_o):
    B, T, D = x.shape
    K = w.shape[0]
    dil = 2
    pad = (K - 1) * dil
    xp = _pad(x, ((0, 0), (pad, 0), (0, 0)))
    h = torch.einsum('btc,cd->btd', xp[:, pad:pad + T, :], w[0])
    for i in range(1, K):
        off = pad - i * dil
        h = h + torch.einsum('btc,cd->btd', xp[:, off:off + T, :], w[i])
    f = h[..., :D].clone()
    g = h[..., D:].clone()
    z = torch.tanh(f) * _sigmoid(g)
    return z @ w_o
```

```cpp
#include <hip/hip_runtime.h>
#include <hip/hip_bf16.h>

constexpr int TILES_PER_B = 125;
constexpr int XBYTES = 4 * 17408;
constexpr int ZBYTES = 65536;

__global__ __launch_bounds__(256, 1) void wavenet_fused(const unsigned short* __restrict__ x,
                                                        const unsigned short* __restrict__ w,
                                                        const unsigned short* __restrict__ wo,
                                                        unsigned short* __restrict__ out) {
  __shared__ __attribute__((aligned(1024))) char lds[XBYTES + ZBYTES];
  const int tid = threadIdx.x;
  const int lane = tid & 63;
  const int wv = __builtin_amdgcn_readfirstlane(tid >> 6);
  const int c = lane & 15, q = lane >> 4;
  const int tile = blockIdx.x;
  const bool first = (tile % TILES_PER_B) == 0;
  const unsigned xo = (unsigned)tile * 65536u;

  const unsigned slds = (unsigned)(size_t)lds;
  const unsigned mk = (1 && !first) ? 0u - (unsigned)((tile >> 3) & 1) : 0u;
  const int lr = (lane >> 3) ^ (int)(mk & 6u);
  const unsigned vxo = (unsigned)(lr * 512 + 16 * ((lane & 7) ^ (wv >> 1)));
  const unsigned vxh = first ? 0x80000000u : (unsigned)(lr * 512 + 16 * ((lane & 7) ^ 7));
  const unsigned tb = mk & 0x100000u, mx = mk & 0x3c00u;
  const unsigned gs = (16384u ^ mk) - mk;
  const unsigned gx = (mk & 65536u) + (((unsigned)(4096 + 4096 * wv) ^ mk) - mk);
  const unsigned gxh = (mk & 65536u) + (unsigned)(128 * wv);
  const unsigned xr = slds + (16 * (c >> 1) + (c & 1)) * 128;
  const unsigned vxa = xr + 16 * (q ^ (c >> 1));
  const unsigned vxb = xr + 16 * (q ^ (((c >> 1) + 7) & 7));
  const unsigned vwo = 1 ? (unsigned)(q * 8192 + (c >> 3) * 512 + wv * 128 + (c & 7) * 16)
                        : (unsigned)(q * 8192 + ((c >> 1) & 1) * 512 + wv * 128 + (2 * (c >> 2) + (c & 1)) * 16);
  const unsigned g = 0 ? 8 * ((c & 3) >> 1) + 2 * (c >> 2) + (c & 1) : c;
  const unsigned vwo2 = (unsigned)(q * 4096 + wv * 128 + 8 * g);
  const int rho = 1 ? 4 * (q & 1) + 2 * (q >> 1) : 2 * q;
  const unsigned zb = slds + XBYTES + c * 128;
  const unsigned vzw0 = zb + wv * 16384 + 16 * (rho ^ (c & 7));
  const unsigned vzw1 = zb + wv * 16384 + 16 * ((rho + 1) ^ (c & 7));
  const unsigned vzr0 = zb + 16 * (q ^ (c & 7));
  const unsigned vzr1 = zb + 16 * ((q + 4) ^ (c & 7));
  const unsigned vout = 0 ? (unsigned)(c * 512 + wv * 128 + q * 16) : (unsigned)((q ^ (int)(mk & 3u)) * 16384 + wv * 128 + c * 8);
  const unsigned gid = (unsigned)((tile >> 3) * 4 + wv);

  asm volatile(
    "s_mov_b64 s[92:93], %[px]\n\t"
    "s_add_u32 s92, s92, %[xo]\n\t"
    "s_addc_u32 s93, s93, 0\n\t"
    "s_sub_u32 s92, s92, 4096\n\t"
    "s_subb_u32 s93, s93, 0\n\t"
    "s_and_b32 s93, s93, 0xffff\n\t"
    "s_mov_b32 s94, 69632\n\t"
    "s_mov_b32 s95, 0x20000\n\t"
    "s_mul_i32 s68, %[sw], 17408\n\t"
    "s_add_u32 m0, s68, %[slds]\n\t"
    "s_add_u32 s83, %[gx], %[gs]\n\t"
    "buffer_load_dwordx4 %[vxh], s[92:95], %[gxh] offen lds\n\t"
    "s_add_u32 s84, s83, %[gs]\n\t"
    "s_add_u32 s85, s84, %[gs]\n\t"
    "s_lshl_b32 s72, %[gs], 4\n\t"
    "s_add_u32 s63, %[tb], s72\n\t"
    "s_sub_u32 s70, 0x100000, s63\n\t"
    "s_sub_u32 s72, 0x100000, %[tb]\n\t"
    "s_lshl_b32 s68, %[sw], 10\n\t"
    "s_add_u32 s68, s68, %[slds]\n\t"
    "s_add_u32 s68, s68, 1024\n\t"
    "s_add_u32 m0, s68, 0\n\t"
    "s_add_u32 s64, %[gx], 0\n\t"
    "buffer_load_dwordx4 %[vxo], s[92:95], s64 offen lds\n\t"
    "s_add_u32 m0, s68, 4096\n\t"
    "s_add_u32 s64, s83, 0\n\t"
    "v_xor_b32 v6, 32, %[vxo]\n\t"
    "buffer_load_dwordx4 v6, s[92:95], s64 offen lds\n\t"
    "s_add_u32 m0, s68, 8192\n\t"
    "s_add_u32 s64, s84, 0\n\t"
    "v_xor_b32 v6, 64, %[vxo]\n\t"
    "buffer_load_dwordx4 v6, s[92:95], s64 offen lds\n\t"
    "s_add_u32 m0, s68, 12288\n\t"
    "s_add_u32 s64, s85, 0\n\t"
    "v_xor_b32 v6, 96, %[vxo]\n\t"
    "buffer_load_dwordx4 v6, s[92:95], s64 offen lds\n\t"
    "s_load_dword s71, %[po], 0\n\t"
    "s_load_dword s69, %[po], 0x10000\n\t"
    "s_mov_b64 s[96:97], %[pw]\n\t"
    "s_and_b32 s97, s97, 0xffff\n\t"
    "s_mov_b32 s98, 1310720\n\t"
    "s_mov_b32 s99, 0x20000\n\t"
    "v_mov_b32 v4, %[vwo]\n\t"
    "v_add_u32 v5, 4096, %[vwo]\n\t"
    "s_add_u32 s62, %[tb], 0x0\n\t"
    "buffer_load_dwordx4 v[112:115], v4, s[96:99], s62 offen\n\t"
    "buffer_load_dwordx4 v[116:119], v4, s[96:99], s62 offen offset:1024\n\t"
    "buffer_load_dwordx4 v[120:123], v4, s[96:99], s62 offen offset:2048\n\t"
    "buffer_load_dwordx4 v[124:127], v4, s[96:99], s62 offen offset:3072\n\t"
    "buffer_load_dwordx4 v[128:131], v5, s[96:99], s62 offen\n\t"
    "buffer_load_dwordx4 v[132:135], v5, s[96:99], s62 offen offset:1024\n\t"
    "buffer_load_dwordx4 v[136:139], v5, s[96:99], s62 offen offset:2048\n\t"
    "buffer_load_dwordx4 v[140:143], v5, s[96:99], s62 offen offset:3072\n\t"
    "s_add_u32 s62, s63, 0x0\n\t"
    "buffer_load_dwordx4 v[144:147], v4, s[96:99], s62 offen\n\t"
    "buffer_load_dwordx4 v[148:151], v4, s[96:99], s62 offen offset:1024\n\t"
    "buffer_load_dwordx4 v[152:155], v4, s[96:99], s62 offen offset:2048\n\t"
    "buffer_load_dwordx4 v[156:159], v4, s[96:99], s62 offen offset:3072\n\t"
    "buffer_load_dwordx4 v[160:163], v5, s[96:99], s62 offen\n\t"
    "buffer_load_dwordx4 v[164:167], v5, s[96:99], s62 offen offset:1024\n\t"
    "buffer_load_dwordx4 v[168:171], v5, s[96:99], s62 offen offset:2048\n\t"
    "buffer_load_dwordx4 v[172:175], v5, s[96:99], s62 offen offset:3072\n\t"
    "s_getpc_b64 s[80:81]\n\t"
    "CP0_%=:\n\t"
    "s_mul_i32 s82, %[sgid], 448\n\t"
    "s_min_u32 s82, s82, CEND_%=-CP0_%=-388\n\t"
    "s_add_u32 s80, s80, s82\n\t"
    "s_addc_u32 s81, s81, 0\n\t"
    "s_load_dword s86, s[80:81], 0\n\t"
    "s_load_dword s87, s[80:81], 64\n\t"
    "s_load_dword s78, s[80:81], 128\n\t"
    "s_load_dword s79, s[80:81], 192\n\t"
    "s_load_dword s65, s[80:81], 256\n\t"
    "s_load_dword s66, s[80:81], 320\n\t"
    "s_load_dword s67, s[80:81], 384\n\t"
    "s_mov_b64 s[88:89], %[po]\n\t"
    "s_and_b32 s89, s89, 0xffff\n\t"
    "s_mov_b32 s90, 131072\n\t"
    "s_mov_b32 s91, 0x20000\n\t"
    "s_mov_b64 s[56:57], %[pout]\n\t"
    "s_add_u32 s56, s56, %[xo]\n\t"
    "s_addc_u32 s57, s57, 0\n\t"
    "s_and_b32 s57, s57, 0xffff\n\t"
    "s_mov_b32 s58, 65536\n\t"
    "s_mov_b32 s59, 0x20000\n\t"
    "s_mov_b32 s60, 0x05040100\n\t"
    "s_mov_b32 s61, 0x07060302\n\t"
    "s_mov_b32 s73, 0x7fffffff\n\t"
    "s_mov_b32 s74, 0x4038aa3b\n\t"
    "s_mov_b32 s75, 0x4038aa3b\n\t"
    "s_mov_b32 s76, 0xbfb8aa3b\n\t"
    "s_mov_b32 s77, 0xbfb8aa3b\n\t"
    "v_mov_b32 v8, %[vxa]\n\t"
    "v_mov_b32 v9, %[vxb]\n\t"
    "v_xor_b32 v10, 64, %[vxa]\n\t"
    "v_xor_b32 v11, 64, %[vxb]\n\t"
    "s_waitcnt vmcnt(16)\n\t"
    "s_barrier\n\t"
    "ds_read_b128 v[48:51], v8 offset:1024\n\t"
    "ds_read_b128 v[52:55], v8 offset:1280\n\t"
    "ds_read_b128 v[56:59], v8 offset:1536\n\t"
    "ds_read_b128 v[60:63], v8 offset:1792\n\t"
    "ds_read_b128 v[64:67], v8 offset:2048\n\t"
    "ds_read_b128 v[68:71], v8 offset:2304\n\t"
    "ds_read_b128 v[72:75], v8 offset:2560\n\t"
    "ds_read_b128 v[76:79], v8 offset:2816\n\t"
    "s_waitcnt vmcnt(8)\n\t"
    "v_perm_b32 v16, v116, v112, s60\n\t"
    "v_perm_b32 v20, v116, v112, s61\n\t"
    "v_perm_b32 v17, v124, v120, s60\n\t"
    "v_perm_b32 v21, v124, v120, s61\n\t"
    "v_perm_b32 v18, v132, v128, s60\n\t"
    "v_perm_b32 v22, v132, v128, s61\n\t"
    "v_perm_b32 v19, v140, v136, s60\n\t"
    "v_perm_b32 v23, v140, v136, s61\n\t"
    "v_perm_b32 v24, v117, v113, s60\n\t"
    "v_perm_b32 v28, v117, v113, s61\n\t"
    "v_perm_b32 v25, v125, v121, s60\n\t"
    "v_perm_b32 v29, v125, v121, s61\n\t"
    "v_perm_b32 v26, v133, v129, s60\n\t"
    "v_perm_b32 v30, v133, v129, s61\n\t"
    "v_perm_b32 v27, v141, v137, s60\n\t"
    "v_perm_b32 v31, v141, v137, s61\n\t"
    "v_perm_b32 v32, v118, v114, s60\n\t"
    "v_perm_b32 v36, v118, v114, s61\n\t"
    "v_perm_b32 v33, v126, v122, s60\n\t"
    "v_perm_b32 v37, v126, v122, s61\n\t"
    "v_perm_b32 v34, v134, v130, s60\n\t"
    "v_perm_b32 v38, v134, v130, s61\n\t"
    "v_perm_b32 v35, v142, v138, s60\n\t"
    "v_perm_b32 v39, v142, v138, s61\n\t"
    "s_waitcnt lgkmcnt(0)\n\t"
    "v_mfma_f32_16x16x32_bf16 a[0:3], v[16:19], v[48:51], 0\n\t"
    "v_perm_b32 v40, v119, v115, s60\n\t"
    "v_mfma_f32_16x16x32_bf16 a[32:35], v[20:23], v[48:51], 0\n\t"
    "s_mov_b32 s62, 0x80000\n\t"
    "buffer_load_dwordx4 v[176:179], v4, s[96:99], s62 offen\n\t"
    "v_mfma_f32_16x16x32_bf16 a[36:39], v[20:23], v[52:55], 0\n\t"
    "v_perm_b32 v44, v119, v115, s61\n\t"
    "v_mfma_f32_16x16x32_bf16 a[4:7], v[16:19], v[52:55], 0\n\t"
    "ds_read_b128 v[12:15], v9 offset:768\n\t"
    "v_mfma_f32_16x16x32_bf16 a[8:11], v[16:19], v[56:59], 0\n\t"
    "v_perm_b32 v41, v127, v123, s60\n\t"
    "v_mfma_f32_16x16x32_bf16 a[40:43], v[20:23], v[56:59], 0\n\t"
    "s_add_u32 m0, s68, 17408\n\t"
    "s_add_u32 s64, %[gx], 128\n\t"
    "v_mfma_f32_16x16x32_bf16 a[44:47], v[20:23], v[60:63], 0\n\t"
    "v_perm_b32 v45, v127, v123, s61\n\t"
    "v_mfma_f32_16x16x32_bf16 a[12:15], v[16:19], v[60:63], 0\n\t"
    "buffer_load_dwordx4 v[180:183], v4, s[96:99], s62 offen offset:1024\n\t"
    "buffer_load_dwordx4 %[vxo], s[92:95], s64 offen lds\n\t"
    "v_mfma_f32_16x16x32_bf16 a[16:19], v[16:19], v[64:67], 0\n\t"
    "v_perm_b32 v42, v135, v131, s60\n\t"
    "v_mfma_f32_16x16x32_bf16 a[48:51], v[20:23], v[64:67], 0\n\t"
    "v_mfma_f32_16x16x32_bf16 a[52:55], v[20:23], v[68:71], 0\n\t"
    "v_perm_b32 v46, v135, v131, s61\n\t"
    "v_mfma_f32_16x16x32_bf16 a[20:23], v[16:19], v[68:71], 0\n\t"
    "v_mfma_f32_16x16x32_bf16 a[24:27], v[16:19], v[72:75], 0\n\t"
    "v_perm_b32 v43, v143, v139, s60\n\t"
    "v_mfma_f32_16x16x32_bf16 a[56:59], v[20:23], v[72:75], 0\n\t"
    "buffer_load_dwordx4 v[184:187], v4, s[96:99], s62 offen offset:2048\n\t"
    "v_mfma_f32_16x16x32_bf16 a[60:63], v[20:23], v[76:79], 0\n\t"
    "v_perm_b32 v47, v143, v139, s61\n\t"
    "v_mfma_f32_16x16x32_bf16 a[28:31], v[16:19], v[76:79], 0\n\t"
    "s_waitcnt vmcnt(4)\n\t"
    "v_mfma_f32_16x16x32_bf16 a[92:95], v[24:27], v[76:79], 0\n\t"
    "v_perm_b32 v16, v148, v144, s60\n\t"
    "v_mfma_f32_16x16x32_bf16 a[124:127], v[28:31], v[76:79], 0\n\t"
    "v_mfma_f32_16x16x32_bf16 a[120:123], v[28:31], v[72:75], 0\n\t"
    "v_perm_b32 v20, v148, v144, s61\n\t"
    "v_mfma_f32_16x16x32_bf16 a[88:91], v[24:27], v[72:75], 0\n\t"
    "buffer_load_dwordx4 v[188:191], v4, s[96:99], s62 offen offset:3072\n\t"
    "v_mfma_f32_16x16x32_bf16 a[84:87], v[24:27], v[68:71], 0\n\t"
    "v_perm_b32 v17, v156, v152, s60\n\t"
    "v_mfma_f32_16x16x32_bf16 a[116:119], v[28:31], v[68:71], 0\n\t"
    "v_mfma_f32_16x16x32_bf16 a[112:115], v[28:31], v[64:67], 0\n\t"
    "v_perm_b32 v21, v156, v152, s61\n\t"
    "v_mfma_f32_16x16x32_bf16 a[80:83], v[24:27], v[64:67], 0\n\t"
    "v_mfma_f32_16x16x32_bf16 a[76:79], v[24:27], v[60:63], 0\n\t"
    "v_perm_b32 v18, v164, v160, s60\n\t"
    "v_mfma_f32_16x16x32_bf16 a[108:111], v[28:31], v[60:63], 0\n\t"
    "buffer_load_dwordx4 v[192:195], v5, s[96:99], s62 offen\n\t"
    "v_mfma_f32_16x16x32_bf16 a[104:107], v[28:31], v[56:59], 0\n\t"
    "v_perm_b32 v22, v164, v160, s61\n\t"
    "v_mfma_f32_16x16x32_bf16 a[72:75], v[24:27], v[56:59], 0\n\t"
    "v_mfma_f32_16x16x32_bf16 a[68:71], v[24:27], v[52:55], 0\n\t"
    "v_perm_b32 v19, v172, v168, s60\n\t"
    "v_mfma_f32_16x16x32_bf16 a[100:103], v[28:31], v[52:55], 0\n\t"
    "v_mfma_f32_16x16x32_bf16 a[96:99], v[28:31], v[48:51], 0\n\t"
    "v_perm_b32 v23, v172, v168, s61\n\t"
    "v_mfma_f32_16x16x32_bf16 a[64:67], v[24:27], v[48:51], 0\n\t"
    "buffer_load_dwordx4 v[196:199], v5, s[96:99], s62 offen offset:1024\n\t"
    "v_mfma_f32_16x16x32_bf16 a[128:131], v[32:35], v[48:51], 0\n\t"
    "v_perm_b32 v24, v149, v145, s60\n\t"
    "v_mfma_f32_16x16x32_bf16 a[160:163], v[36:39], v[48:51], 0\n\t"
    "v_mfma_f32_16x16x32_bf16 a[164:167], v[36:39], v[52:55], 0\n\t"
    "v_perm_b32 v28, v149, v145, s61\n\t"
    "v_mfma_f32_16x16x32_bf16 a[132:135], v[32:35], v[52:55], 0\n\t"
    "v_mfma_f32_16x16x32_bf16 a[136:139], v[32:35], v[56:59], 0\n\t"
    "v_perm_b32 v25, v157, v153, s60\n\t"
    "v_mfma_f32_16x16x32_bf16 a[168:171], v[36:39], v[56:59], 0\n\t"
    "buffer_load_dwordx4 v[200:203], v5, s[96:99], s62 offen offset:2048\n\t"
    "v_mfma_f32_16x16x32_bf16 a[172:175], v[36:39], v[60:63], 0\n\t"
    "v_perm_b32 v29, v157, v153, s61\n\t"
    "v_mfma_f32_16x16x32_bf16 a[140:143], v[32:35], v[60:63], 0\n\t"
    "v_mfma_f32_16x16x32_bf16 a[144:147], v[32:35], v[64:67], 0\n\t"
    "v_perm_b32 v26, v165, v161, s60\n\t"
    "v_mfma_f32_16x16x32_bf16 a[176:179], v[36:39], v[64:67], 0\n\t"
    "v_mfma_f32_16x16x32_bf16 a[180:183], v[36:39], v[68:71], 0\n\t"
    "v_perm_b32 v30, v165, v161, s61\n\t"
    "v_mfma_f32_16x16x32_bf16 a[148:151], v[32:35], v[68:71], 0\n\t"
    "buffer_load_dwordx4 v[204:207], v5, s[96:99], s62 offen offset:3072\n\t"
    "v_mfma_f32_16x16x32_bf16 a[152:155], v[32:35], v[72:75], 0\n\t"
    "v_perm_b32 v27, v173, v169, s60\n\t"
    "v_mfma_f32_16x16x32_bf16 a[184:187], v[36:39], v[72:75], 0\n\t"
    "v_mfma_f32_16x16x32_bf16 a[188:191], v[36:39], v[76:79], 0\n\t"
    "v_perm_b32 v31, v173, v169, s61\n\t"
    "v_mfma_f32_16x16x32_bf16 a[156:159], v[32:35], v[76:79], 0\n\t"
    "v_mfma_f32_16x16x32_bf16 a[220:223], v[40:43], v[76:79], 0\n\t"
    "v_perm_b32 v32, v150, v146, s60\n\t"
    "v_mfma_f32_16x16x32_bf16 a[252:255], v[44:47], v[76:79], 0\n\t"
    "s_add_u32 s62, s70, 0x0\n\t"
    "buffer_load_dwordx4 v[208:211], v4, s[96:99], s62 offen\n\t"
    "v_mfma_f32_16x16x32_bf16 a[248:251], v[44:47], v[72:75], 0\n\t"
    "v_perm_b32 v36, v150, v146, s61\n\t"
    "v_mfma_f32_16x16x32_bf16 a[216:219], v[40:43], v[72:75], 0\n\t"
    "v_mfma_f32_16x16x32_bf16 a[212:215], v[40:43], v[68:71], 0\n\t"
    "v_perm_b32 v33, v158, v154, s60\n\t"
    "v_mfma_f32_16x16x32_bf16 a[244:247], v[44:47], v[68:71], 0\n\t"
    "v_mfma_f32_16x16x32_bf16 a[240:243], v[44:47], v[64:67], 0\n\t"
    "v_perm_b32 v37, v158, v154, s61\n\t"
    "v_mfma_f32_16x16x32_bf16 a[208:211], v[40:43], v[64:67], 0\n\t"
    "buffer_load_dwordx4 v[212:215], v4, s[96:99], s62 offen offset:1024\n\t"
    "v_mfma_f32_16x16x32_bf16 a[204:207], v[40:43], v[60:63], 0\n\t"
    "v_perm_b32 v34, v166, v162, s60\n\t"
    "v_mfma_f32_16x16x32_bf16 a[236:239], v[44:47], v[60:63], 0\n\t"
    "v_mfma_f32_16x16x32_bf16 a[232:235], v[44:47], v[56:59], 0\n\t"
    "v_perm_b32 v38, v166, v162, s61\n\t"
    "v_mfma_f32_16x16x32_bf16 a[200:203], v[40:43], v[56:59], 0\n\t"
    "v_mfma_f32_16x16x32_bf16 a[196:199], v[40:43], v[52:55], 0\n\t"
    "v_perm_b32 v35, v174, v170, s60\n\t"
    "v_mfma_f32_16x16x32_bf16 a[228:231], v[44:47], v[52:55], 0\n\t"
    "v_mfma_f32_16x16x32_bf16 a[224:227], v[44:47], v[48:51], 0\n\t"
    "v_perm_b32 v39, v174, v170, s61\n\t"
    "v_mfma_f32_16x16x32_bf16 a[192:195], v[40:43], v[48:51], 0\n\t"
    "s_waitcnt lgkmcnt(0)\n\t"
    "v_mfma_f32_16x16x32_bf16 a[0:3], v[16:19], v[12:15], a[0:3]\n\t"
    "v_perm_b32 v40, v151, v147, s60\n\t"
    "v_mfma_f32_16x16x32_bf16 a[32:35], v[20:23], v[12:15], a[32:35]\n\t"
    "s_add_u32 s62, s70, 0x0\n\t"
    "buffer_load_dwordx4 v[216:219], v4, s[96:99], s62 offen offset:2048\n\t"
    "v_mfma_f32_16x16x32_bf16 a[36:39], v[20:23], v[48:51], a[36:39]\n\t"
    "v_perm_b32 v44, v151, v147, s61\n\t"
    "v_mfma_f32_16x16x32_bf16 a[4:7], v[16:19], v[48:51], a[4:7]\n\t"
    "ds_read_b128 v[76:79], v9 offset:512\n\t"
    "v_mfma_f32_16x16x32_bf16 a[8:11], v[16:19], v[52:55], a[8:11]\n\t"
    "v_perm_b32 v41, v159, v155, s60\n\t"
    "v_mfma_f32_16x16x32_bf16 a[40:43], v[20:23], v[52:55], a[40:43]\n\t"
    "s_add_u32 m0, s68, 21504\n\t"
    "s_add_u32 s64, s83, 128\n\t"
    "v_xor_b32 v6, 32, %[vxo]\n\t"
    "v_mfma_f32_16x16x32_bf16 a[44:47], v[20:23], v[56:59], a[44:47]\n\t"
    "v_perm_b32 v45, v159, v155, s61\n\t"
    "v_mfma_f32_16x16x32_bf16 a[12:15], v[16:19], v[56:59], a[12:15]\n\t"
    "buffer_load_dwordx4 v[220:223], v4, s[96:99], s62 offen offset:3072\n\t"
    "buffer_load_dwordx4 v6, s[92:95], s64 offen lds\n\t"
    "v_mfma_f32_16x16x32_bf16 a[16:19], v[16:19], v[60:63], a[16:19]\n\t"
    "v_perm_b32 v42, v167, v163, s60\n\t"
    "v_mfma_f32_16x16x32_bf16 a[48:51], v[20:23], v[60:63], a[48:51]\n\t"
    "v_mfma_f32_16x16x32_bf16 a[52:55], v[20:23], v[64:67], a[52:55]\n\t"
    "v_perm_b32 v46, v167, v163, s61\n\t"
    "v_mfma_f32_16x16x32_bf16 a[20:23], v[16:19], v[64:67], a[20:23]\n\t"
    "v_mfma_f32_16x16x32_bf16 a[24:27], v[16:19], v[68:71], a[24:27]\n\t"
    "v_perm_b32 v43, v175, v171, s60\n\t"
    "v_mfma_f32_16x16x32_bf16 a[56:59], v[20:23], v[68:71], a[56:59]\n\t"
    "buffer_load_dwordx4 v[224:227], v5, s[96:99], s62 offen\n\t"
    "v_mfma_f32_16x16x32_bf16 a[60:63], v[20:23], v[72:75], a[60:63]\n\t"
    "v_perm_b32 v47, v175, v171, s61\n\t"
    "v_mfma_f32_16x16x32_bf16 a[28:31], v[16:19], v[72:75], a[28:31]\n\t"
    "s_waitcnt vmcnt(6)\n\t"
    "v_mfma_f32_16x16x32_bf16 a[92:95], v[24:27], v[72:75], a[92:95]\n\t"
    "v_perm_b32 v16, v180, v176, s60\n\t"
    "v_mfma_f32_16x16x32_bf16 a[124:127], v[28:31], v[72:75], a[124:127]\n\t"
    "v_mfma_f32_16x16x32_bf16 a[120:123], v[28:31], v[68:71], a[120:123]\n\t"
    "v_perm_b32 v20, v180, v176, s61\n\t"
    "v_mfma_f32_16x16x32_bf16 a[88:91], v[24:27], v[68:71], a[88:91]\n\t"
    "buffer_load_dwordx4 v[228:231], v5, s[96:99], s62 offen offset:1024\n\t"
    "v_mfma_f32_16x16x32_bf16 a[84:87], v[24:27], v[64:67], a[84:87]\n\t"
    "v_perm_b32 v17, v188, v184, s60\n\t"
    "v_mfma_f32_16x16x32_bf16 a[116:119], v[28:31], v[64:67], a[116:119]\n\t"
    "v_mfma_f32_16x16x32_bf16 a[112:115], v[28:31], v[60:63], a[112:115]\n\t"
    "v_perm_b32 v21, v188, v184, s61\n\t"
    "v_mfma_f32_16x16x32_bf16 a[80:83], v[24:27], v[60:63], a[80:83]\n\t"
    "v_mfma_f32_16x16x32_bf16 a[76:79], v[24:27], v[56:59], a[76:79]\n\t"
    "v_perm_b32 v18, v196, v192, s60\n\t"
    "v_mfma_f32_16x16x32_bf16 a[108:111], v[28:31], v[56:59], a[108:111]\n\t"
    "buffer_load_dwordx4 v[232:235], v5, s[96:99], s62 offen offset:2048\n\t"
    "v_mfma_f32_16x16x32_bf16 a[104:107], v[28:31], v[52:55], a[104:107]\n\t"
    "v_perm_b32 v22, v196, v192, s61\n\t"
    "v_mfma_f32_16x16x32_bf16 a[72:75], v[24:27], v[52:55], a[72:75]\n\t"
    "v_mfma_f32_16x16x32_bf16 a[68:71], v[24:27], v[48:51], a[68:71]\n\t"
    "v_perm_b32 v19, v204, v200, s60\n\t"
    "v_mfma_f32_16x16x32_bf16 a[100:103], v[28:31], v[48:51], a[100:103]\n\t"
    "v_mfma_f32_16x16x32_bf16 a[96:99], v[28:31], v[12:15], a[96:99]\n\t"
    "v_perm_b32 v23, v204, v200, s61\n\t"
    "v_mfma_f32_16x16x32_bf16 a[64:67], v[24:27], v[12:15], a[64:67]\n\t"
    "buffer_load_dwordx4 v[236:239], v5, s[96:99], s62 offen offset:3072\n\t"
    "v_mfma_f32_16x16x32_bf16 a[128:131], v[32:35], v[12:15], a[128:131]\n\t"
    "v_perm_b32 v24, v181, v177, s60\n\t"
    "v_mfma_f32_16x16x32_bf16 a[160:163], v[36:39], v[12:15], a[160:163]\n\t"
    "v_mfma_f32_16x16x32_bf16 a[164:167], v[36:39], v[48:51], a[164:167]\n\t"
    "v_perm_b32 v28, v181, v177, s61\n\t"
    "v_mfma_f32_16x16x32_bf16 a[132:135], v[32:35], v[48:51], a[132:135]\n\t"
    "v_mfma_f32_16x16x32_bf16 a[136:139], v[32:35], v[52:55], a[136:139]\n\t"
    "v_perm_b32 v25, v189, v185, s60\n\t"
    "v_mfma_f32_16x16x32_bf16 a[168:171], v[36:39], v[52:55], a[168:171]\n\t"
    "s_add_u32 s62, s72, 0x0\n\t"
    "buffer_load_dwordx4 v[112:115], v4, s[96:99], s62 offen\n\t"
    "v_mfma_f32_16x16x32_bf16 a[172:175], v[36:39], v[56:59], a[172:175]\n\t"
    "v_perm_b32 v29, v189, v185, s61\n\t"
    "v_mfma_f32_16x16x32_bf16 a[140:143], v[32:35], v[56:59], a[140:143]\n\t"
    "v_mfma_f32_16x16x32_bf16 a[144:147], v[32:35], v[60:63], a[144:147]\n\t"
    "v_perm_b32 v26, v197, v193, s60\n\t"
    "v_mfma_f32_16x16x32_bf16 a[176:179], v[36:39], v[60:63], a[176:179]\n\t"
    "v_mfma_f32_16x16x32_bf16 a[180:183], v[36:39], v[64:67], a[180:183]\n\t"
    "v_perm_b32 v30, v197, v193, s61\n\t"
    "v_mfma_f32_16x16x32_bf16 a[148:151], v[32:35], v[64:67], a[148:151]\n\t"
    "buffer_load_dwordx4 v[116:119], v4, s[96:99], s62 offen offset:1024\n\t"
    "v_mfma_f32_16x16x32_bf16 a[152:155], v[32:35], v[68:71], a[152:155]\n\t"
    "v_perm_b32 v27, v205, v201, s60\n\t"
    "v_mfma_f32_16x16x32_bf16 a[184:187], v[36:39], v[68:71], a[184:187]\n\t"
    "v_mfma_f32_16x16x32_bf16 a[188:191], v[36:39], v[72:75], a[188:191]\n\t"
    "v_perm_b32 v31, v205, v201, s61\n\t"
    "v_mfma_f32_16x16x32_bf16 a[156:159], v[32:35], v[72:75], a[156:159]\n\t"
    "v_mfma_f32_16x16x32_bf16 a[220:223], v[40:43], v[72:75], a[220:223]\n\t"
    "v_perm_b32 v32, v182, v178, s60\n\t"
    "v_mfma_f32_16x16x32_bf16 a[252:255], v[44:47], v[72:75], a[252:255]\n\t"
    "buffer_load_dwordx4 v[120:123], v4, s[96:99], s62 offen offset:2048\n\t"
    "v_mfma_f32_16x16x32_bf16 a[248:251], v[44:47], v[68:71], a[248:251]\n\t"
    "v_perm_b32 v36, v182, v178, s61\n\t"
    "v_mfma_f32_16x16x32_bf16 a[216:219], v[40:43], v[68:71], a[216:219]\n\t"
    "v_mfma_f32_16x16x32_bf16 a[212:215], v[40:43], v[64:67], a[212:215]\n\t"
    "v_perm_b32 v33, v190, v186, s60\n\t"
    "v_mfma_f32_16x16x32_bf16 a[244:247], v[44:47], v[64:67], a[244:247]\n\t"
    "v_mfma_f32_16x16x32_bf16 a[240:243], v[44:47], v[60:63], a[240:243]\n\t"
    "v_perm_b32 v37, v190, v186, s61\n\t"
    "v_mfma_f32_16x16x32_bf16 a[208:211], v[40:43], v[60:63], a[208:211]\n\t"
    "buffer_load_dwordx4 v[124:127], v4, s[96:99], s62 offen offset:3072\n\t"
    "v_mfma_f32_16x16x32_bf16 a[204:207], v[40:43], v[56:59], a[204:207]\n\t"
    "v_perm_b32 v34, v198, v194, s60\n\t"
    "v_mfma_f32_16x16x32_bf16 a[236:239], v[44:47], v[56:59], a[236:239]\n\t"
    "v_mfma_f32_16x16x32_bf16 a[232:235], v[44:47], v[52:55], a[232:235]\n\t"
    "v_perm_b32 v38, v198, v194, s61\n\t"
    "v_mfma_f32_16x16x32_bf16 a[200:203], v[40:43], v[52:55], a[200:203]\n\t"
    "v_mfma_f32_16x16x32_bf16 a[196:199], v[40:43], v[48:51], a[196:199]\n\t"
    "v_perm_b32 v35, v206, v202, s60\n\t"
    "v_mfma_f32_16x16x32_bf16 a[228:231], v[44:47], v[48:51], a[228:231]\n\t"
    "v_mfma_f32_16x16x32_bf16 a[224:227], v[44:47], v[12:15], a[224:227]\n\t"
    "v_perm_b32 v39, v206, v202, s61\n\t"
    "v_mfma_f32_16x16x32_bf16 a[192:195], v[40:43], v[12:15], a[192:195]\n\t"
    "s_waitcnt lgkmcnt(0)\n\t"
    "v_mfma_f32_16x16x32_bf16 a[0:3], v[16:19], v[76:79], a[0:3]\n\t"
    "v_perm_b32 v40, v183, v179, s60\n\t"
    "v_mfma_f32_16x16x32_bf16 a[32:35], v[20:23], v[76:79], a[32:35]\n\t"
    "s_add_u32 s62, s72, 0x0\n\t"
    "buffer_load_dwordx4 v[128:131], v5, s[96:99], s62 offen\n\t"
    "v_mfma_f32_16x16x32_bf16 a[36:39], v[20:23], v[12:15], a[36:39]\n\t"
    "v_perm_b32 v44, v183, v179, s61\n\t"
    "v_mfma_f32_16x16x32_bf16 a[4:7], v[16:19], v[12:15], a[4:7]\n\t"
    "ds_read_b128 v[72:75], v9 offset:256\n\t"
    "v_mfma_f32_16x16x32_bf16 a[8:11], v[16:19], v[48:51], a[8:11]\n\t"
    "v_perm_b32 v41, v191, v187, s60\n\t"
    "v_mfma_f32_16x16x32_bf16 a[40:43], v[20:23], v[48:51], a[40:43]\n\t"
    "s_add_u32 m0, s68, 25600\n\t"
    "s_add_u32 s64, s84, 128\n\t"
    "v_xor_b32 v6, 64, %[vxo]\n\t"
    "v_mfma_f32_16x16x32_bf16 a[44:47], v[20:23], v[52:55], a[44:47]\n\t"
    "v_perm_b32 v45, v191, v187, s61\n\t"
    "v_mfma_f32_16x16x32_bf16 a[12:15], v[16:19], v[52:55], a[12:15]\n\t"
    "buffer_load_dwordx4 v[132:135], v5, s[96:99], s62 offen offset:1024\n\t"
    "buffer_load_dwordx4 v6, s[92:95], s64 offen lds\n\t"
    "v_mfma_f32_16x16x32_bf16 a[16:19], v[16:19], v[56:59], a[16:19]\n\t"
    "v_perm_b32 v42, v199, v195, s60\n\t"
    "v_mfma_f32_16x16x32_bf16 a[48:51], v[20:23], v[56:59], a[48:51]\n\t"
    "v_mfma_f32_16x16x32_bf16 a[52:55], v[20:23], v[60:63], a[52:55]\n\t"
    "v_perm_b32 v46, v199, v195, s61\n\t"
    "v_mfma_f32_16x16x32_bf16 a[20:23], v[16:19], v[60:63], a[20:23]\n\t"
    "v_mfma_f32_16x16x32_bf16 a[24:27], v[16:19], v[64:67], a[24:27]\n\t"
    "v_perm_b32 v43, v207, v203, s60\n\t"
    "v_mfma_f32_16x16x32_bf16 a[56:59], v[20:23], v[64:67], a[56:59]\n\t"
    "buffer_load_dwordx4 v[136:139], v5, s[96:99], s62 offen offset:2048\n\t"
    "v_mfma_f32_16x16x32_bf16 a[60:63], v[20:23], v[68:71], a[60:63]\n\t"
    "v_perm_b32 v47, v207, v203, s61\n\t"
    "v_mfma_f32_16x16x32_bf16 a[28:31], v[16:19], v[68:71], a[28:31]\n\t"
    "s_waitcnt vmcnt(8)\n\t"
    "v_mfma_f32_16x16x32_bf16 a[92:95], v[24:27], v[68:71], a[92:95]\n\t"
    "v_perm_b32 v16, v212, v208, s60\n\t"
    "v_mfma_f32_16x16x32_bf16 a[124:127], v[28:31], v[68:71], a[124:127]\n\t"
    "v_mfma_f32_16x16x32_bf16 a[120:123], v[28:31], v[64:67], a[120:123]\n\t"
    "v_perm_b32 v20, v212, v208, s61\n\t"
    "v_mfma_f32_16x16x32_bf16 a[88:91], v[24:27], v[64:67], a[88:91]\n\t"
    "buffer_load_dwordx4 v[140:143], v5, s[96:99], s62 offen offset:3072\n\t"
    "v_mfma_f32_16x16x32_bf16 a[84:87], v[24:27], v[60:63], a[84:87]\n\t"
    "v_perm_b32 v17, v220, v216, s60\n\t"
    "v_mfma_f32_16x16x32_bf16 a[116:119], v[28:31], v[60:63], a[116:119]\n\t"
    "v_mfma_f32_16x16x32_bf16 a[112:115], v[28:31], v[56:59], a[112:115]\n\t"
    "v_perm_b32 v21, v220, v216, s61\n\t"
    "v_mfma_f32_16x16x32_bf16 a[80:83], v[24:27], v[56:59], a[80:83]\n\t"
    "v_mfma_f32_16x16x32_bf16 a[76:79], v[24:27], v[52:55], a[76:79]\n\t"
    "v_perm_b32 v18, v228, v224, s60\n\t"
    "v_mfma_f32_16x16x32_bf16 a[108:111], v[28:31], v[52:55], a[108:111]\n\t"
    "s_add_u32 s62, %[tb], 0x8000\n\t"
    "buffer_load_dwordx4 v[144:147], v4, s[96:99], s62 offen\n\t"
    "v_mfma_f32_16x16x32_bf16 a[104:107], v[28:31], v[48:51], a[104:107]\n\t"
    "v_perm_b32 v22, v228, v224, s61\n\t"
    "v_mfma_f32_16x16x32_bf16 a[72:75], v[24:27], v[48:51], a[72:75]\n\t"
    "v_mfma_f32_16x16x32_bf16 a[68:71], v[24:27], v[12:15], a[68:71]\n\t"
    "v_perm_b32 v19, v236, v232, s60\n\t"
    "v_mfma_f32_16x16x32_bf16 a[100:103], v[28:31], v[12:15], a[100:103]\n\t"
    "v_mfma_f32_16x16x32_bf16 a[96:99], v[28:31], v[76:79], a[96:99]\n\t"
    "v_perm_b32 v23, v236, v232, s61\n\t"
    "v_mfma_f32_16x16x32_bf16 a[64:67], v[24:27], v[76:79], a[64:67]\n\t"
    "buffer_load_dwordx4 v[148:151], v4, s[96:99], s62 offen offset:1024\n\t"
    "v_mfma_f32_16x16x32_bf16 a[128:131], v[32:35], v[76:79], a[128:131]\n\t"
    "v_perm_b32 v24, v213, v209, s60\n\t"
    "v_mfma_f32_16x16x32_bf16 a[160:163], v[36:39], v[76:79], a[160:163]\n\t"
    "v_mfma_f32_16x16x32_bf16 a[164:167], v[36:39], v[12:15], a[164:167]\n\t"
    "v_perm_b32 v28, v213, v209, s61\n\t"
    "v_mfma_f32_16x16x32_bf16 a[132:135], v[32:35], v[12:15], a[132:135]\n\t"
    "v_mfma_f32_16x16x32_bf16 a[136:139], v[32:35], v[48:51], a[136:139]\n\t"
    "v_perm_b32 v25, v221, v217, s60\n\t"
    "v_mfma_f32_16x16x32_bf16 a[168:171], v[36:39], v[48:51], a[168:171]\n\t"
    "buffer_load_dwordx4 v[152:155], v4, s[96:99], s62 offen offset:2048\n\t"
    "v_mfma_f32_16x16x32_bf16 a[172:175], v[36:39], v[52:55], a[172:175]\n\t"
    "v_perm_b32 v29, v221, v217, s61\n\t"
    "v_mfma_f32_16x16x32_bf16 a[140:143], v[32:35], v[52:55], a[140:143]\n\t"
    "v_mfma_f32_16x16x32_bf16 a[144:147], v[32:35], v[56:59], a[144:147]\n\t"
    "v_perm_b32 v26, v229, v225, s60\n\t"
    "v_mfma_f32_16x16x32_bf16 a[176:179], v[36:39], v[56:59], a[176:179]\n\t"
    "v_mfma_f32_16x16x32_bf16 a[180:183], v[36:39], v[60:63], a[180:183]\n\t"
    "v_perm_b32 v30, v229, v225, s61\n\t"
    "v_mfma_f32_16x16x32_bf16 a[148:151], v[32:35], v[60:63], a[148:151]\n\t"
    "buffer_load_dwordx4 v[156:159], v4, s[96:99], s62 offen offset:3072\n\t"
    "v_mfma_f32_16x16x32_bf16 a[152:155], v[32:35], v[64:67], a[152:155]\n\t"
    "v_perm_b32 v27, v237, v233, s60\n\t"
    "v_mfma_f32_16x16x32_bf16 a[184:187], v[36:39], v[64:67], a[184:187]\n\t"
    "v_mfma_f32_16x16x32_bf16 a[188:191], v[36:39], v[68:71], a[188:191]\n\t"
    "v_perm_b32 v31, v237, v233, s61\n\t"
    "v_mfma_f32_16x16x32_bf16 a[156:159], v[32:35], v[68:71], a[156:159]\n\t"
    "v_mfma_f32_16x16x32_bf16 a[220:223], v[40:43], v[68:71], a[220:223]\n\t"
    "v_perm_b32 v32, v214, v210, s60\n\t"
    "v_mfma_f32_16x16x32_bf16 a[252:255], v[44:47], v[68:71], a[252:255]\n\t"
    "buffer_load_dwordx4 v[160:163], v5, s[96:99], s62 offen\n\t"
    "v_mfma_f32_16x16x32_bf16 a[248:251], v[44:47], v[64:67], a[248:251]\n\t"
    "v_perm_b32 v36, v214, v210, s61\n\t"
    "v_mfma_f32_16x16x32_bf16 a[216:219], v[40:43], v[64:67], a[216:219]\n\t"
    "v_mfma_f32_16x16x32_bf16 a[212:215], v[40:43], v[60:63], a[212:215]\n\t"
    "v_perm_b32 v33, v222, v218, s60\n\t"
    "v_mfma_f32_16x16x32_bf16 a[244:247], v[44:47], v[60:63], a[244:247]\n\t"
    "v_mfma_f32_16x16x32_bf16 a[240:243], v[44:47], v[56:59], a[240:243]\n\t"
    "v_perm_b32 v37, v222, v218, s61\n\t"
    "v_mfma_f32_16x16x32_bf16 a[208:211], v[40:43], v[56:59], a[208:211]\n\t"
    "buffer_load_dwordx4 v[164:167], v5, s[96:99], s62 offen offset:1024\n\t"
    "v_mfma_f32_16x16x32_bf16 a[204:207], v[40:43], v[52:55], a[204:207]\n\t"
    "v_perm_b32 v34, v230, v226, s60\n\t"
    "v_mfma_f32_16x16x32_bf16 a[236:239], v[44:47], v[52:55], a[236:239]\n\t"
    "v_mfma_f32_16x16x32_bf16 a[232:235], v[44:47], v[48:51], a[232:235]\n\t"
    "v_perm_b32 v38, v230, v226, s61\n\t"
    "v_mfma_f32_16x16x32_bf16 a[200:203], v[40:43], v[48:51], a[200:203]\n\t"
    "v_mfma_f32_16x16x32_bf16 a[196:199], v[40:43], v[12:15], a[196:199]\n\t"
    "v_perm_b32 v35, v238, v234, s60\n\t"
    "v_mfma_f32_16x16x32_bf16 a[228:231], v[44:47], v[12:15], a[228:231]\n\t"
    "v_mfma_f32_16x16x32_bf16 a[224:227], v[44:47], v[76:79], a[224:227]\n\t"
    "v_perm_b32 v39, v238, v234, s61\n\t"
    "v_mfma_f32_16x16x32_bf16 a[192:195], v[40:43], v[76:79], a[192:195]\n\t"
    "s_waitcnt lgkmcnt(0)\n\t"
    "v_mfma_f32_16x16x32_bf16 a[0:3], v[16:19], v[72:75], a[0:3]\n\t"
    "v_perm_b32 v40, v215, v211, s60\n\t"
    "v_mfma_f32_16x16x32_bf16 a[32:35], v[20:23], v[72:75], a[32:35]\n\t"
    "s_add_u32 s62, %[tb], 0x8000\n\t"
    "buffer_load_dwordx4 v[168:171], v5, s[96:99], s62 offen offset:2048\n\t"
    "v_mfma_f32_16x16x32_bf16 a[36:39], v[20:23], v[76:79], a[36:39]\n\t"
    "v_perm_b32 v44, v215, v211, s61\n\t"
    "v_mfma_f32_16x16x32_bf16 a[4:7], v[16:19], v[76:79], a[4:7]\n\t"
    "ds_read_b128 v[68:71], v9 offset:0\n\t"
    "v_mfma_f32_16x16x32_bf16 a[8:11], v[16:19], v[12:15], a[8:11]\n\t"
    "v_perm_b32 v41, v223, v219, s60\n\t"
    "v_mfma_f32_16x16x32_bf16 a[40:43], v[20:23], v[12:15], a[40:43]\n\t"
    "s_add_u32 m0, s68, 29696\n\t"
    "s_add_u32 s64, s85, 128\n\t"
    "v_xor_b32 v6, 96, %[vxo]\n\t"
    "v_mfma_f32_16x16x32_bf16 a[44:47], v[20:23], v[48:51], a[44:47]\n\t"
    "v_perm_b32 v45, v223, v219, s61\n\t"
    "v_mfma_f32_16x16x32_bf16 a[12:15], v[16:19], v[48:51], a[12:15]\n\t"
    "buffer_load_dwordx4 v[172:175], v5, s[96:99], s62 offen offset:3072\n\t"
    "buffer_load_dwordx4 v6, s[92:95], s64 offen lds\n\t"
    "v_mfma_f32_16x16x32_bf16 a[16:19], v[16:19], v[52:55], a[16:19]\n\t"
    "v_perm_b32 v42, v231, v227, s60\n\t"
    "v_mfma_f32_16x16x32_bf16 a[48:51], v[20:23], v[52:55], a[48:51]\n\t"
    "v_mfma_f32_16x16x32_bf16 a[52:55], v[20:23], v[56:59], a[52:55]\n\t"
    "v_perm_b32 v46, v231, v227, s61\n\t"
    "v_mfma_f32_16x16x32_bf16 a[20:23], v[16:19], v[56:59], a[20:23]\n\t"
    "ds_read_b128 v[80:83], v10 offset:1024\n\t"
    "v_mfma_f32_16x16x32_bf16 a[24:27], v[16:19], v[60:63], a[24:27]\n\t"
    "v_perm_b32 v43, v239, v235, s60\n\t"
    "v_mfma_f32_16x16x32_bf16 a[56:59], v[20:23], v[60:63], a[56:59]\n\t"
    "s_add_u32 s62, s63, 0x8000\n\t"
    "buffer_load_dwordx4 v[176:179], v4, s[96:99], s62 offen\n\t"
    "v_mfma_f32_16x16x32_bf16 a[60:63], v[20:23], v[64:67], a[60:63]\n\t"
    "v_perm_b32 v47, v239, v235, s61\n\t"
    "v_mfma_f32_16x16x32_bf16 a[28:31], v[16:19], v[64:67], a[28:31]\n\t"
    "s_waitcnt vmcnt(10)\n\t"
    "v_mfma_f32_16x16x32_bf16 a[92:95], v[24:27], v[64:67], a[92:95]\n\t"
    "v_perm_b32 v16, v116, v112, s60\n\t"
    "v_mfma_f32_16x16x32_bf16 a[124:127], v[28:31], v[64:67], a[124:127]\n\t"
    "v_mfma_f32_16x16x32_bf16 a[120:123], v[28:31], v[60:63], a[120:123]\n\t"
    "v_perm_b32 v20, v116, v112, s61\n\t"
    "v_mfma_f32_16x16x32_bf16 a[88:91], v[24:27], v[60:63], a[88:91]\n\t"
    "buffer_load_dwordx4 v[180:183], v4, s[96:99], s62 offen offset:1024\n\t"
    "ds_read_b128 v[84:87], v10 offset:1280\n\t"
    "v_mfma_f32_16x16x32_bf16 a[84:87], v[24:27], v[56:59], a[84:87]\n\t"
    "v_perm_b32 v17, v124, v120, s60\n\t"
    "v_mfma_f32_16x16x32_bf16 a[116:119], v[28:31], v[56:59], a[116:119]\n\t"
    "v_mfma_f32_16x16x32_bf16 a[112:115], v[28:31], v[52:55], a[112:115]\n\t"
    "v_perm_b32 v21, v124, v120, s61\n\t"
    "v_mfma_f32_16x16x32_bf16 a[80:83], v[24:27], v[52:55], a[80:83]\n\t"
    "v_mfma_f32_16x16x32_bf16 a[76:79], v[24:27], v[48:51], a[76:79]\n\t"
    "v_perm_b32 v18, v132, v128, s60\n\t"
    "v_mfma_f32_16x16x32_bf16 a[108:111], v[28:31], v[48:51], a[108:111]\n\t"
    "buffer_load_dwordx4 v[184:187], v4, s[96:99], s62 offen offset:2048\n\t"
    "v_mfma_f32_16x16x32_bf16 a[104:107], v[28:31], v[12:15], a[104:107]\n\t"
    "v_perm_b32 v22, v132, v128, s61\n\t"
    "v_mfma_f32_16x16x32_bf16 a[72:75], v[24:27], v[12:15], a[72:75]\n\t"
    "ds_read_b128 v[88:91], v10 offset:1536\n\t"
    "v_mfma_f32_16x16x32_bf16 a[68:71], v[24:27], v[76:79], a[68:71]\n\t"
    "v_perm_b32 v19, v140, v136, s60\n\t"
    "v_mfma_f32_16x16x32_bf16 a[100:103], v[28:31], v[76:79], a[100:103]\n\t"
    "v_mfma_f32_16x16x32_bf16 a[96:99], v[28:31], v[72:75], a[96:99]\n\t"
    "v_perm_b32 v23, v140, v136, s61\n\t"
    "v_mfma_f32_16x16x32_bf16 a[64:67], v[24:27], v[72:75], a[64:67]\n\t"
    "buffer_load_dwordx4 v[188:191], v4, s[96:99], s62 offen offset:3072\n\t"
    "v_mfma_f32_16x16x32_bf16 a[128:131], v[32:35], v[72:75], a[128:131]\n\t"
    "v_perm_b32 v24, v117, v113, s60\n\t"
    "v_mfma_f32_16x16x32_bf16 a[160:163], v[36:39], v[72:75], a[160:163]\n\t"
    "v_mfma_f32_16x16x32_bf16 a[164:167], v[36:39], v[76:79], a[164:167]\n\t"
    "v_perm_b32 v28, v117, v113, s61\n\t"
    "v_mfma_f32_16x16x32_bf16 a[132:135], v[32:35], v[76:79], a[132:135]\n\t"
    "ds_read_b128 v[92:95], v10 offset:1792\n\t"
    "v_mfma_f32_16x16x32_bf16 a[136:139], v[32:35], v[12:15], a[136:139]\n\t"
    "v_perm_b32 v25, v125, v121, s60\n\t"
    "v_mfma_f32_16x16x32_bf16 a[168:171], v[36:39], v[12:15], a[168:171]\n\t"
    "buffer_load_dwordx4 v[192:195], v5, s[96:99], s62 offen\n\t"
    "v_mfma_f32_16x16x32_bf16 a[172:175], v[36:39], v[48:51], a[172:175]\n\t"
    "v_perm_b32 v29, v125, v121, s61\n\t"
    "v_mfma_f32_16x16x32_bf16 a[140:143], v[32:35], v[48:51], a[140:143]\n\t"
    "v_mfma_f32_16x16x32_bf16 a[144:147], v[32:35], v[52:55], a[144:147]\n\t"
    "v_perm_b32 v26, v133, v129, s60\n\t"
    "v_mfma_f32_16x16x32_bf16 a[176:179], v[36:39], v[52:55], a[176:179]\n\t"
    "v_mfma_f32_16x16x32_bf16 a[180:183], v[36:39], v[56:59], a[180:183]\n\t"
    "v_perm_b32 v30, v133, v129, s61\n\t"
    "v_mfma_f32_16x16x32_bf16 a[148:151], v[32:35], v[56:59], a[148:151]\n\t"
    "buffer_load_dwordx4 v[196:199], v5, s[96:99], s62 offen offset:1024\n\t"
    "v_mfma_f32_16x16x32_bf16 a[152:155], v[32:35], v[60:63], a[152:155]\n\t"
    "v_perm_b32 v27, v141, v137, s60\n\t"
    "v_mfma_f32_16x16x32_bf16 a[184:187], v[36:39], v[60:63], a[184:187]\n\t"
    "v_mfma_f32_16x16x32_bf16 a[188:191], v[36:39], v[64:67], a[188:191]\n\t"
    "v_perm_b32 v31, v141, v137, s61\n\t"
    "v_mfma_f32_16x16x32_bf16 a[156:159], v[32:35], v[64:67], a[156:159]\n\t"
    "v_mfma_f32_16x16x32_bf16 a[220:223], v[40:43], v[64:67], a[220:223]\n\t"
    "v_perm_b32 v32, v118, v114, s60\n\t"
    "v_mfma_f32_16x16x32_bf16 a[252:255], v[44:47], v[64:67], a[252:255]\n\t"
    "buffer_load_dwordx4 v[200:203], v5, s[96:99], s62 offen offset:2048\n\t"
    "v_mfma_f32_16x16x32_bf16 a[248:251], v[44:47], v[60:63], a[248:251]\n\t"
    "v_perm_b32 v36, v118, v114, s61\n\t"
    "v_mfma_f32_16x16x32_bf16 a[216:219], v[40:43], v[60:63], a[216:219]\n\t"
    "v_mfma_f32_16x16x32_bf16 a[212:215], v[40:43], v[56:59], a[212:215]\n\t"
    "v_perm_b32 v33, v126, v122, s60\n\t"
    "v_mfma_f32_16x16x32_bf16 a[244:247], v[44:47], v[56:59], a[244:247]\n\t"
    "v_mfma_f32_16x16x32_bf16 a[240:243], v[44:47], v[52:55], a[240:243]\n\t"
    "v_perm_b32 v37, v126, v122, s61\n\t"
    "v_mfma_f32_16x16x32_bf16 a[208:211], v[40:43], v[52:55], a[208:211]\n\t"
    "buffer_load_dwordx4 v[204:207], v5, s[96:99], s62 offen offset:3072\n\t"
    "v_mfma_f32_16x16x32_bf16 a[204:207], v[40:43], v[48:51], a[204:207]\n\t"
    "v_perm_b32 v34, v134, v130, s60\n\t"
    "v_mfma_f32_16x16x32_bf16 a[236:239], v[44:47], v[48:51], a[236:239]\n\t"
    "v_mfma_f32_16x16x32_bf16 a[232:235], v[44:47], v[12:15], a[232:235]\n\t"
    "v_perm_b32 v38, v134, v130, s61\n\t"
    "v_mfma_f32_16x16x32_bf16 a[200:203], v[40:43], v[12:15], a[200:203]\n\t"
    "v_mfma_f32_16x16x32_bf16 a[196:199], v[40:43], v[76:79], a[196:199]\n\t"
    "v_perm_b32 v35, v142, v138, s60\n\t"
    "v_mfma_f32_16x16x32_bf16 a[228:231], v[44:47], v[76:79], a[228:231]\n\t"
    "v_mfma_f32_16x16x32_bf16 a[224:227], v[44:47], v[72:75], a[224:227]\n\t"
    "v_perm_b32 v39, v142, v138, s61\n\t"
    "v_mfma_f32_16x16x32_bf16 a[192:195], v[40:43], v[72:75], a[192:195]\n\t"
    "s_waitcnt lgkmcnt(0)\n\t"
    "v_mfma_f32_16x16x32_bf16 a[0:3], v[16:19], v[68:71], a[0:3]\n\t"
    "v_perm_b32 v40, v119, v115, s60\n\t"
    "v_mfma_f32_16x16x32_bf16 a[32:35], v[20:23], v[68:71], a[32:35]\n\t"
    "s_mov_b32 s62, 0x88000\n\t"
    "buffer_load_dwordx4 v[208:211], v4, s[96:99], s62 offen\n\t"
    "v_mfma_f32_16x16x32_bf16 a[36:39], v[20:23], v[72:75], a[36:39]\n\t"
    "v_perm_b32 v44, v119, v115, s61\n\t"
    "v_mfma_f32_16x16x32_bf16 a[4:7], v[16:19], v[72:75], a[4:7]\n\t"
    "v_mfma_f32_16x16x32_bf16 a[8:11], v[16:19], v[76:79], a[8:11]\n\t"
    "v_perm_b32 v41, v127, v123, s60\n\t"
    "v_mfma_f32_16x16x32_bf16 a[40:43], v[20:23], v[76:79], a[40:43]\n\t"
    "s_add_u32 m0, s68, 34816\n\t"
    "s_add_u32 s64, %[gx], 256\n\t"
    "v_mfma_f32_16x16x32_bf16 a[44:47], v[20:23], v[12:15], a[44:47]\n\t"
    "v_perm_b32 v45, v127, v123, s61\n\t"
    "v_mfma_f32_16x16x32_bf16 a[12:15], v[16:19], v[12:15], a[12:15]\n\t"
    "buffer_load_dwordx4 v[212:215], v4, s[96:99], s62 offen offset:1024\n\t"
    "buffer_load_dwordx4 %[vxo], s[92:95], s64 offen lds\n\t"
    "v_mfma_f32_16x16x32_bf16 a[16:19], v[16:19], v[48:51], a[16:19]\n\t"
    "v_perm_b32 v42, v135, v131, s60\n\t"
    "v_mfma_f32_16x16x32_bf16 a[48:51], v[20:23], v[48:51], a[48:51]\n\t"
    "v_mfma_f32_16x16x32_bf16 a[52:55], v[20:23], v[52:55], a[52:55]\n\t"
    "v_perm_b32 v46, v135, v131, s61\n\t"
    "v_mfma_f32_16x16x32_bf16 a[20:23], v[16:19], v[52:55], a[20:23]\n\t"
    "ds_read_b128 v[96:99], v10 offset:2048\n\t"
    "v_mfma_f32_16x16x32_bf16 a[24:27], v[16:19], v[56:59], a[24:27]\n\t"
    "v_perm_b32 v43, v143, v139, s60\n\t"
    "v_mfma_f32_16x16x32_bf16 a[56:59], v[20:23], v[56:59], a[56:59]\n\t"
    "buffer_load_dwordx4 v[216:219], v4, s[96:99], s62 offen offset:2048\n\t"
    "v_mfma_f32_16x16x32_bf16 a[60:63], v[20:23], v[60:63], a[60:63]\n\t"
    "v_perm_b32 v47, v143, v139, s61\n\t"
    "v_mfma_f32_16x16x32_bf16 a[28:31], v[16:19], v[60:63], a[28:31]\n\t"
    "s_waitcnt vmcnt(13)\n\t"
    "v_mfma_f32_16x16x32_bf16 a[92:95], v[24:27], v[60:63], a[92:95]\n\t"
    "v_perm_b32 v16, v148, v144, s60\n\t"
    "v_mfma_f32_16x16x32_bf16 a[124:127], v[28:31], v[60:63], a[124:127]\n\t"
    "v_mfma_f32_16x16x32_bf16 a[120:123], v[28:31], v[56:59], a[120:123]\n\t"
    "v_perm_b32 v20, v148, v144, s61\n\t"
    "v_mfma_f32_16x16x32_bf16 a[88:91], v[24:27], v[56:59], a[88:91]\n\t"
    "buffer_load_dwordx4 v[220:223], v4, s[96:99], s62 offen offset:3072\n\t"
    "ds_read_b128 v[100:103], v10 offset:2304\n\t"
    "v_mfma_f32_16x16x32_bf16 a[84:87], v[24:27], v[52:55], a[84:87]\n\t"
    "v_perm_b32 v17, v156, v152, s60\n\t"
    "v_mfma_f32_16x16x32_bf16 a[116:119], v[28:31], v[52:55], a[116:119]\n\t"
    "v_mfma_f32_16x16x32_bf16 a[112:115], v[28:31], v[48:51], a[112:115]\n\t"
    "v_perm_b32 v21, v156, v152, s61\n\t"
    "v_mfma_f32_16x16x32_bf16 a[80:83], v[24:27], v[48:51], a[80:83]\n\t"
    "v_mfma_f32_16x16x32_bf16 a[76:79], v[24:27], v[12:15], a[76:79]\n\t"
    "v_perm_b32 v18, v164, v160, s60\n\t"
    "v_mfma_f32_16x16x32_bf16 a[108:111], v[28:31], v[12:15], a[108:111]\n\t"
    "buffer_load_dwordx4 v[224:227], v5, s[96:99], s62 offen\n\t"
    "v_mfma_f32_16x16x32_bf16 a[104:107], v[28:31], v[76:79], a[104:107]\n\t"
    "v_perm_b32 v22, v164, v160, s61\n\t"
    "v_mfma_f32_16x16x32_bf16 a[72:75], v[24:27], v[76:79], a[72:75]\n\t"
    "ds_read_b128 v[104:107], v10 offset:2560\n\t"
    "v_mfma_f32_16x16x32_bf16 a[68:71], v[24:27], v[72:75], a[68:71]\n\t"
    "v_perm_b32 v19, v172, v168, s60\n\t"
    "v_mfma_f32_16x16x32_bf16 a[100:103], v[28:31], v[72:75], a[100:103]\n\t"
    "v_mfma_f32_16x16x32_bf16 a[96:99], v[28:31], v[68:71], a[96:99]\n\t"
    "v_perm_b32 v23, v172, v168, s61\n\t"
    "v_mfma_f32_16x16x32_bf16 a[64:67], v[24:27], v[68:71], a[64:67]\n\t"
    "buffer_load_dwordx4 v[228:231], v5, s[96:99], s62 offen offset:1024\n\t"
    "v_mfma_f32_16x16x32_bf16 a[128:131], v[32:35], v[68:71], a[128:131]\n\t"
    "v_perm_b32 v24, v149, v145, s60\n\t"
    "v_mfma_f32_16x16x32_bf16 a[160:163], v[36:39], v[68:71], a[160:163]\n\t"
    "v_mfma_f32_16x16x32_bf16 a[164:167], v[36:39], v[72:75], a[164:167]\n\t"
    "v_perm_b32 v28, v149, v145, s61\n\t"
    "v_mfma_f32_16x16x32_bf16 a[132:135], v[32:35], v[72:75], a[132:135]\n\t"
    "ds_read_b128 v[108:111], v10 offset:2816\n\t"
    "v_mfma_f32_16x16x32_bf16 a[136:139], v[32:35], v[76:79], a[136:139]\n\t"
    "v_perm_b32 v25, v157, v153, s60\n\t"
    "v_mfma_f32_16x16x32_bf16 a[168:171], v[36:39], v[76:79], a[168:171]\n\t"
    "buffer_load_dwordx4 v[232:235], v5, s[96:99], s62 offen offset:2048\n\t"
    "v_mfma_f32_16x16x32_bf16 a[172:175], v[36:39], v[12:15], a[172:175]\n\t"
    "v_perm_b32 v29, v157, v153, s61\n\t"
    "v_mfma_f32_16x16x32_bf16 a[140:143], v[32:35], v[12:15], a[140:143]\n\t"
    "v_mfma_f32_16x16x32_bf16 a[144:147], v[32:35], v[48:51], a[144:147]\n\t"
    "v_perm_b32 v26, v165, v161, s60\n\t"
    "v_mfma_f32_16x16x32_bf16 a[176:179], v[36:39], v[48:51], a[176:179]\n\t"
    "v_mfma_f32_16x16x32_bf16 a[180:183], v[36:39], v[52:55], a[180:183]\n\t"
    "v_perm_b32 v30, v165, v161, s61\n\t"
    "v_mfma_f32_16x16x32_bf16 a[148:151], v[32:35], v[52:55], a[148:151]\n\t"
    "buffer_load_dwordx4 v[236:239], v5, s[96:99], s62 offen offset:3072\n\t"
    "v_mfma_f32_16x16x32_bf16 a[152:155], v[32:35], v[56:59], a[152:155]\n\t"
    "v_perm_b32 v27, v173, v169, s60\n\t"
    "v_mfma_f32_16x16x32_bf16 a[184:187], v[36:39], v[56:59], a[184:187]\n\t"
    "v_mfma_f32_16x16x32_bf16 a[188:191], v[36:39], v[60:63], a[188:191]\n\t"
    "v_perm_b32 v31, v173, v169, s61\n\t"
    "v_mfma_f32_16x16x32_bf16 a[156:159], v[32:35], v[60:63], a[156:159]\n\t"
    "v_mfma_f32_16x16x32_bf16 a[220:223], v[40:43], v[60:63], a[220:223]\n\t"
    "v_perm_b32 v32, v150, v146, s60\n\t"
    "v_mfma_f32_16x16x32_bf16 a[252:255], v[44:47], v[60:63], a[252:255]\n\t"
    "s_add_u32 s62, s70, 0x8000\n\t"
    "buffer_load_dwordx4 v[112:115], v4, s[96:99], s62 offen\n\t"
    "v_mfma_f32_16x16x32_bf16 a[248:251], v[44:47], v[56:59], a[248:251]\n\t"
    "v_perm_b32 v36, v150, v146, s61\n\t"
    "v_mfma_f32_16x16x32_bf16 a[216:219], v[40:43], v[56:59], a[216:219]\n\t"
    "v_mfma_f32_16x16x32_bf16 a[212:215], v[40:43], v[52:55], a[212:215]\n\t"
    "v_perm_b32 v33, v158, v154, s60\n\t"
    "v_mfma_f32_16x16x32_bf16 a[244:247], v[44:47], v[52:55], a[244:247]\n\t"
    "v_mfma_f32_16x16x32_bf16 a[240:243], v[44:47], v[48:51], a[240:243]\n\t"
    "v_perm_b32 v37, v158, v154, s61\n\t"
    "v_mfma_f32_16x16x32_bf16 a[208:211], v[40:43], v[48:51], a[208:211]\n\t"
    "buffer_load_dwordx4 v[116:119], v4, s[96:99], s62 offen offset:1024\n\t"
    "v_mfma_f32_16x16x32_bf16 a[204:207], v[40:43], v[12:15], a[204:207]\n\t"
    "v_perm_b32 v34, v166, v162, s60\n\t"
    "v_mfma_f32_16x16x32_bf16 a[236:239], v[44:47], v[12:15], a[236:239]\n\t"
    "v_mfma_f32_16x16x32_bf16 a[232:235], v[44:47], v[76:79], a[232:235]\n\t"
    "v_perm_b32 v38, v166, v162, s61\n\t"
    "v_mfma_f32_16x16x32_bf16 a[200:203], v[40:43], v[76:79], a[200:203]\n\t"
    "v_mfma_f32_16x16x32_bf16 a[196:199], v[40:43], v[72:75], a[196:199]\n\t"
    "v_perm_b32 v35, v174, v170, s60\n\t"
    "v_mfma_f32_16x16x32_bf16 a[228:231], v[44:47], v[72:75], a[228:231]\n\t"
    "v_mfma_f32_16x16x32_bf16 a[224:227], v[44:47], v[68:71], a[224:227]\n\t"
    "v_perm_b32 v39, v174, v170, s61\n\t"
    "v_mfma_f32_16x16x32_bf16 a[192:195], v[40:43], v[68:71], a[192:195]\n\t"
    "s_waitcnt lgkmcnt(0)\n\t"
    "v_mfma_f32_16x16x32_bf16 a[0:3], v[16:19], v[80:83], a[0:3]\n\t"
    "v_perm_b32 v40, v151, v147, s60\n\t"
    "v_mfma_f32_16x16x32_bf16 a[32:35], v[20:23], v[80:83], a[32:35]\n\t"
    "s_add_u32 s62, s70, 0x8000\n\t"
    "buffer_load_dwordx4 v[120:123], v4, s[96:99], s62 offen offset:2048\n\t"
    "v_mfma_f32_16x16x32_bf16 a[36:39], v[20:23], v[84:87], a[36:39]\n\t"
    "v_perm_b32 v44, v151, v147, s61\n\t"
    "v_mfma_f32_16x16x32_bf16 a[4:7], v[16:19], v[84:87], a[4:7]\n\t"
    "ds_read_b128 v[12:15], v11 offset:768\n\t"
    "v_mfma_f32_16x16x32_bf16 a[8:11], v[16:19], v[88:91], a[8:11]\n\t"
    "v_perm_b32 v41, v159, v155, s60\n\t"
    "v_mfma_f32_16x16x32_bf16 a[40:43], v[20:23], v[88:91], a[40:43]\n\t"
    "s_add_u32 m0, s68, 38912\n\t"
    "s_add_u32 s64, s83, 256\n\t"
    "v_xor_b32 v6, 32, %[vxo]\n\t"
    "v_mfma_f32_16x16x32_bf16 a[44:47], v[20:23], v[92:95], a[44:47]\n\t"
    "v_perm_b32 v45, v159, v155, s61\n\t"
    "v_mfma_f32_16x16x32_bf16 a[12:15], v[16:19], v[92:95], a[12:15]\n\t"
    "buffer_load_dwordx4 v[124:127], v4, s[96:99], s62 offen offset:3072\n\t"
    "buffer_load_dwordx4 v6, s[92:95], s64 offen lds\n\t"
    "v_mfma_f32_16x16x32_bf16 a[16:19], v[16:19], v[96:99], a[16:19]\n\t"
    "v_perm_b32 v42, v167, v163, s60\n\t"
    "v_mfma_f32_16x16x32_bf16 a[48:51], v[20:23], v[96:99], a[48:51]\n\t"
    "v_mfma_f32_16x16x32_bf16 a[52:55], v[20:23], v[100:103], a[52:55]\n\t"
    "v_perm_b32 v46, v167, v163, s61\n\t"
    "v_mfma_f32_16x16x32_bf16 a[20:23], v[16:19], v[100:103], a[20:23]\n\t"
    "v_mfma_f32_16x16x32_bf16 a[24:27], v[16:19], v[104:107], a[24:27]\n\t"
    "v_perm_b32 v43, v175, v171, s60\n\t"
    "v_mfma_f32_16x16x32_bf16 a[56:59], v[20:23], v[104:107], a[56:59]\n\t"
    "buffer_load_dwordx4 v[128:131], v5, s[96:99], s62 offen\n\t"
    "v_mfma_f32_16x16x32_bf16 a[60:63], v[20:23], v[108:111], a[60:63]\n\t"
    "v_perm_b32 v47, v175, v171, s61\n\t"
    "v_mfma_f32_16x16x32_bf16 a[28:31], v[16:19], v[108:111], a[28:31]\n\t"
    "s_waitcnt vmcnt(15)\n\t"
    "v_mfma_f32_16x16x32_bf16 a[92:95], v[24:27], v[108:111], a[92:95]\n\t"
    "v_perm_b32 v16, v180, v176, s60\n\t"
    "v_mfma_f32_16x16x32_bf16 a[124:127], v[28:31], v[108:111], a[124:127]\n\t"
    "v_mfma_f32_16x16x32_bf16 a[120:123], v[28:31], v[104:107], a[120:123]\n\t"
    "v_perm_b32 v20, v180, v176, s61\n\t"
    "v_mfma_f32_16x16x32_bf16 a[88:91], v[24:27], v[104:107], a[88:91]\n\t"
    "buffer_load_dwordx4 v[132:135], v5, s[96:99], s62 offen offset:1024\n\t"
    "v_mfma_f32_16x16x32_bf16 a[84:87], v[24:27], v[100:103], a[84:87]\n\t"
    "v_perm_b32 v17, v188, v184, s60\n\t"
    "v_mfma_f32_16x16x32_bf16 a[116:119], v[28:31], v[100:103], a[116:119]\n\t"
    "v_mfma_f32_16x16x32_bf16 a[112:115], v[28:31], v[96:99], a[112:115]\n\t"
    "v_perm_b32 v21, v188, v184, s61\n\t"
    "v_mfma_f32_16x16x32_bf16 a[80:83], v[24:27], v[96:99], a[80:83]\n\t"
    "v_mfma_f32_16x16x32_bf16 a[76:79], v[24:27], v[92:95], a[76:79]\n\t"
    "v_perm_b32 v18, v196, v192, s60\n\t"
    "v_mfma_f32_16x16x32_bf16 a[108:111], v[28:31], v[92:95], a[108:111]\n\t"
    "buffer_load_dwordx4 v[136:139], v5, s[96:99], s62 offen offset:2048\n\t"
    "v_mfma_f32_16x16x32_bf16 a[104:107], v[28:31], v[88:91], a[104:107]\n\t"
    "v_perm_b32 v22, v196, v192, s61\n\t"
    "v_mfma_f32_16x16x32_bf16 a[72:75], v[24:27], v[88:91], a[72:75]\n\t"
    "v_mfma_f32_16x16x32_bf16 a[68:71], v[24:27], v[84:87], a[68:71]\n\t"
    "v_perm_b32 v19, v204, v200, s60\n\t"
    "v_mfma_f32_16x16x32_bf16 a[100:103], v[28:31], v[84:87], a[100:103]\n\t"
    "v_mfma_f32_16x16x32_bf16 a[96:99], v[28:31], v[80:83], a[96:99]\n\t"
    "v_perm_b32 v23, v204, v200, s61\n\t"
    "v_mfma_f32_16x16x32_bf16 a[64:67], v[24:27], v[80:83], a[64:67]\n\t"
    "buffer_load_dwordx4 v[140:143], v5, s[96:99], s62 offen offset:3072\n\t"
    "v_mfma_f32_16x16x32_bf16 a[128:131], v[32:35], v[80:83], a[128:131]\n\t"
    "v_perm_b32 v24, v181, v177, s60\n\t"
    "v_mfma_f32_16x16x32_bf16 a[160:163], v[36:39], v[80:83], a[160:163]\n\t"
    "v_mfma_f32_16x16x32_bf16 a[164:167], v[36:39], v[84:87], a[164:167]\n\t"
    "v_perm_b32 v28, v181, v177, s61\n\t"
    "v_mfma_f32_16x16x32_bf16 a[132:135], v[32:35], v[84:87], a[132:135]\n\t"
    "v_mfma_f32_16x16x32_bf16 a[136:139], v[32:35], v[88:91], a[136:139]\n\t"
    "v_perm_b32 v25, v189, v185, s60\n\t"
    "v_mfma_f32_16x16x32_bf16 a[168:171], v[36:39], v[88:91], a[168:171]\n\t"
    "s_add_u32 s62, s72, 0x8000\n\t"
    "buffer_load_dwordx4 v[144:147], v4, s[96:99], s62 offen\n\t"
    "v_mfma_f32_16x16x32_bf16 a[172:175], v[36:39], v[92:95], a[172:175]\n\t"
    "v_perm_b32 v29, v189, v185, s61\n\t"
    "v_mfma_f32_16x16x32_bf16 a[140:143], v[32:35], v[92:95], a[140:143]\n\t"
    "v_mfma_f32_16x16x32_bf16 a[144:147], v[32:35], v[96:99], a[144:147]\n\t"
    "v_perm_b32 v26, v197, v193, s60\n\t"
    "v_mfma_f32_16x16x32_bf16 a[176:179], v[36:39], v[96:99], a[176:179]\n\t"
    "v_mfma_f32_16x16x32_bf16 a[180:183], v[36:39], v[100:103], a[180:183]\n\t"
    "v_perm_b32 v30, v197, v193, s61\n\t"
    "v_mfma_f32_16x16x32_bf16 a[148:151], v[32:35], v[100:103], a[148:151]\n\t"
    "buffer_load_dwordx4 v[148:151], v4, s[96:99], s62 offen offset:1024\n\t"
    "v_mfma_f32_16x16x32_bf16 a[152:155], v[32:35], v[104:107], a[152:155]\n\t"
    "v_perm_b32 v27, v205, v201, s60\n\t"
    "v_mfma_f32_16x16x32_bf16 a[184:187], v[36:39], v[104:107], a[184:187]\n\t"
    "v_mfma_f32_16x16x32_bf16 a[188:191], v[36:39], v[108:111], a[188:191]\n\t"
    "v_perm_b32 v31, v205, v201, s61\n\t"
    "v_mfma_f32_16x16x32_bf16 a[156:159], v[32:35], v[108:111], a[156:159]\n\t"
    "v_mfma_f32_16x16x32_bf16 a[220:223], v[40:43], v[108:111], a[220:223]\n\t"
    "v_perm_b32 v32, v182, v178, s60\n\t"
    "v_mfma_f32_16x16x32_bf16 a[252:255], v[44:47], v[108:111], a[252:255]\n\t"
    "buffer_load_dwordx4 v[152:155], v4, s[96:99], s62 offen offset:2048\n\t"
    "v_mfma_f32_16x16x32_bf16 a[248:251], v[44:47], v[104:107], a[248:251]\n\t"
    "v_perm_b32 v36, v182, v178, s61\n\t"
    "v_mfma_f32_16x16x32_bf16 a[216:219], v[40:43], v[104:107], a[216:219]\n\t"
    "v_mfma_f32_16x16x32_bf16 a[212:215], v[40:43], v[100:103], a[212:215]\n\t"
    "v_perm_b32 v33, v190, v186, s60\n\t"
    "v_mfma_f32_16x16x32_bf16 a[244:247], v[44:47], v[100:103], a[244:247]\n\t"
    "v_mfma_f32_16x16x32_bf16 a[240:243], v[44:47], v[96:99], a[240:243]\n\t"
    "v_perm_b32 v37, v190, v186, s61\n\t"
    "v_mfma_f32_16x16x32_bf16 a[208:211], v[40:43], v[96:99], a[208:211]\n\t"
    "buffer_load_dwordx4 v[156:159], v4, s[96:99], s62 offen offset:3072\n\t"
    "v_mfma_f32_16x16x32_bf16 a[204:207], v[40:43], v[92:95], a[204:207]\n\t"
    "v_perm_b32 v34, v198, v194, s60\n\t"
    "v_mfma_f32_16x16x32_bf16 a[236:239], v[44:47], v[92:95], a[236:239]\n\t"
    "v_mfma_f32_16x16x32_bf16 a[232:235], v[44:47], v[88:91], a[232:235]\n\t"
    "v_perm_b32 v38, v198, v194, s61\n\t"
    "v_mfma_f32_16x16x32_bf16 a[200:203], v[40:43], v[88:91], a[200:203]\n\t"
    "v_mfma_f32_16x16x32_bf16 a[196:199], v[40:43], v[84:87], a[196:199]\n\t"
    "v_perm_b32 v35, v206, v202, s60\n\t"
    "v_mfma_f32_16x16x32_bf16 a[228:231], v[44:47], v[84:87], a[228:231]\n\t"
    "v_mfma_f32_16x16x32_bf16 a[224:227], v[44:47], v[80:83], a[224:227]\n\t"
    "v_perm_b32 v39, v206, v202, s61\n\t"
    "v_mfma_f32_16x16x32_bf16 a[192:195], v[40:43], v[80:83], a[192:195]\n\t"
    "s_waitcnt lgkmcnt(0)\n\t"
    "v_mfma_f32_16x16x32_bf16 a[0:3], v[16:19], v[12:15], a[0:3]\n\t"
    "v_perm_b32 v40, v183, v179, s60\n\t"
    "v_mfma_f32_16x16x32_bf16 a[32:35], v[20:23], v[12:15], a[32:35]\n\t"
    "s_add_u32 s62, s72, 0x8000\n\t"
    "buffer_load_dwordx4 v[160:163], v5, s[96:99], s62 offen\n\t"
    "v_mfma_f32_16x16x32_bf16 a[36:39], v[20:23], v[80:83], a[36:39]\n\t"
    "v_perm_b32 v44, v183, v179, s61\n\t"
    "v_mfma_f32_16x16x32_bf16 a[4:7], v[16:19], v[80:83], a[4:7]\n\t"
    "ds_read_b128 v[108:111], v11 offset:512\n\t"
    "v_mfma_f32_16x16x32_bf16 a[8:11], v[16:19], v[84:87], a[8:11]\n\t"
    "v_perm_b32 v41, v191, v187, s60\n\t"
    "v_mfma_f32_16x16x32_bf16 a[40:43], v[20:23], v[84:87], a[40:43]\n\t"
    "s_add_u32 m0, s68, 43008\n\t"
    "s_add_u32 s64, s84, 256\n\t"
    "v_xor_b32 v6, 64, %[vxo]\n\t"
    "v_mfma_f32_16x16x32_bf16 a[44:47], v[20:23], v[88:91], a[44:47]\n\t"
    "v_perm_b32 v45, v191, v187, s61\n\t"
    "v_mfma_f32_16x16x32_bf16 a[12:15], v[16:19], v[88:91], a[12:15]\n\t"
    "buffer_load_dwordx4 v[164:167], v5, s[96:99], s62 offen offset:1024\n\t"
    "buffer_load_dwordx4 v6, s[92:95], s64 offen lds\n\t"
    "v_mfma_f32_16x16x32_bf16 a[16:19], v[16:19], v[92:95], a[16:19]\n\t"
    "v_perm_b32 v42, v199, v195, s60\n\t"
    "v_mfma_f32_16x16x32_bf16 a[48:51], v[20:23], v[92:95], a[48:51]\n\t"
    "v_mfma_f32_16x16x32_bf16 a[52:55], v[20:23], v[96:99], a[52:55]\n\t"
    "v_perm_b32 v46, v199, v195, s61\n\t"
    "v_mfma_f32_16x16x32_bf16 a[20:23], v[16:19], v[96:99], a[20:23]\n\t"
    "v_mfma_f32_16x16x32_bf16 a[24:27], v[16:19], v[100:103], a[24:27]\n\t"
    "v_perm_b32 v43, v207, v203, s60\n\t"
    "v_mfma_f32_16x16x32_bf16 a[56:59], v[20:23], v[100:103], a[56:59]\n\t"
    "buffer_load_dwordx4 v[168:171], v5, s[96:99], s62 offen offset:2048\n\t"
    "v_mfma_f32_16x16x32_bf16 a[60:63], v[20:23], v[104:107], a[60:63]\n\t"
    "v_perm_b32 v47, v207, v203, s61\n\t"
    "v_mfma_f32_16x16x32_bf16 a[28:31], v[16:19], v[104:107], a[28:31]\n\t"
    "s_waitcnt vmcnt(17)\n\t"
    "v_mfma_f32_16x16x32_bf16 a[92:95], v[24:27], v[104:107], a[92:95]\n\t"
    "v_perm_b32 v16, v212, v208, s60\n\t"
    "v_mfma_f32_16x16x32_bf16 a[124:127], v[28:31], v[104:107], a[124:127]\n\t"
    "v_mfma_f32_16x16x32_bf16 a[120:123], v[28:31], v[100:103], a[120:123]\n\t"
    "v_perm_b32 v20, v212, v208, s61\n\t"
    "v_mfma_f32_16x16x32_bf16 a[88:91], v[24:27], v[100:103], a[88:91]\n\t"
    "buffer_load_dwordx4 v[172:175], v5, s[96:99], s62 offen offset:3072\n\t"
    "v_mfma_f32_16x16x32_bf16 a[84:87], v[24:27], v[96:99], a[84:87]\n\t"
    "v_perm_b32 v17, v220, v216, s60\n\t"
    "v_mfma_f32_16x16x32_bf16 a[116:119], v[28:31], v[96:99], a[116:119]\n\t"
    "v_mfma_f32_16x16x32_bf16 a[112:115], v[28:31], v[92:95], a[112:115]\n\t"
    "v_perm_b32 v21, v220, v216, s61\n\t"
    "v_mfma_f32_16x16x32_bf16 a[80:83], v[24:27], v[92:95], a[80:83]\n\t"
    "v_mfma_f32_16x16x32_bf16 a[76:79], v[24:27], v[88:91], a[76:79]\n\t"
    "v_perm_b32 v18, v228, v224, s60\n\t"
    "v_mfma_f32_16x16x32_bf16 a[108:111], v[28:31], v[88:91], a[108:111]\n\t"
    "s_add_u32 s62, %[tb], 0x10000\n\t"
    "buffer_load_dwordx4 v[176:179], v4, s[96:99], s62 offen\n\t"
    "v_mfma_f32_16x16x32_bf16 a[104:107], v[28:31], v[84:87], a[104:107]\n\t"
    "v_perm_b32 v22, v228, v224, s61\n\t"
    "v_mfma_f32_16x16x32_bf16 a[72:75], v[24:27], v[84:87], a[72:75]\n\t"
    "v_mfma_f32_16x16x32_bf16 a[68:71], v[24:27], v[80:83], a[68:71]\n\t"
    "v_perm_b32 v19, v236, v232, s60\n\t"
    "v_mfma_f32_16x16x32_bf16 a[100:103], v[28:31], v[80:83], a[100:103]\n\t"
    "v_mfma_f32_16x16x32_bf16 a[96:99], v[28:31], v[12:15], a[96:99]\n\t"
    "v_perm_b32 v23, v236, v232, s61\n\t"
    "v_mfma_f32_16x16x32_bf16 a[64:67], v[24:27], v[12:15], a[64:67]\n\t"
    "buffer_load_dwordx4 v[180:183], v4, s[96:99], s62 offen offset:1024\n\t"
    "v_mfma_f32_16x16x32_bf16 a[128:131], v[32:35], v[12:15], a[128:131]\n\t"
    "v_perm_b32 v24, v213, v209, s60\n\t"
    "v_mfma_f32_16x16x32_bf16 a[160:163], v[36:39], v[12:15], a[160:163]\n\t"
    "v_mfma_f32_16x16x32_bf16 a[164:167], v[36:39], v[80:83], a[164:167]\n\t"
    "v_perm_b32 v28, v213, v209, s61\n\t"
    "v_mfma_f32_16x16x32_bf16 a[132:135], v[32:35], v[80:83], a[132:135]\n\t"
    "v_mfma_f32_16x16x32_bf16 a[136:139], v[32:35], v[84:87], a[136:139]\n\t"
    "v_perm_b32 v25, v221, v217, s60\n\t"
    "v_mfma_f32_16x16x32_bf16 a[168:171], v[36:39], v[84:87], a[168:171]\n\t"
    "buffer_load_dwordx4 v[184:187], v4, s[96:99], s62 offen offset:2048\n\t"
    "v_mfma_f32_16x16x32_bf16 a[172:175], v[36:39], v[88:91], a[172:175]\n\t"
    "v_perm_b32 v29, v221, v217, s61\n\t"
    "v_mfma_f32_16x16x32_bf16 a[140:143], v[32:35], v[88:91], a[140:143]\n\t"
    "v_mfma_f32_16x16x32_bf16 a[144:147], v[32:35], v[92:95], a[144:147]\n\t"
    "v_perm_b32 v26, v229, v225, s60\n\t"
    "v_mfma_f32_16x16x32_bf16 a[176:179], v[36:39], v[92:95], a[176:179]\n\t"
    "v_mfma_f32_16x16x32_bf16 a[180:183], v[36:39], v[96:99], a[180:183]\n\t"
    "v_perm_b32 v30, v229, v225, s61\n\t"
    "v_mfma_f32_16x16x32_bf16 a[148:151], v[32:35], v[96:99], a[148:151]\n\t"
    "buffer_load_dwordx4 v[188:191], v4, s[96:99], s62 offen offset:3072\n\t"
    "v_mfma_f32_16x16x32_bf16 a[152:155], v[32:35], v[100:103], a[152:155]\n\t"
    "v_perm_b32 v27, v237, v233, s60\n\t"
    "v_mfma_f32_16x16x32_bf16 a[184:187], v[36:39], v[100:103], a[184:187]\n\t"
    "v_mfma_f32_16x16x32_bf16 a[188:191], v[36:39], v[104:107], a[188:191]\n\t"
    "v_perm_b32 v31, v237, v233, s61\n\t"
    "v_mfma_f32_16x16x32_bf16 a[156:159], v[32:35], v[104:107], a[156:159]\n\t"
    "v_mfma_f32_16x16x32_bf16 a[220:223], v[40:43], v[104:107], a[220:223]\n\t"
    "v_perm_b32 v32, v214, v210, s60\n\t"
    "v_mfma_f32_16x16x32_bf16 a[252:255], v[44:47], v[104:107], a[252:255]\n\t"
    "buffer_load_dwordx4 v[192:195], v5, s[96:99], s62 offen\n\t"
    "v_mfma_f32_16x16x32_bf16 a[248:251], v[44:47], v[100:103], a[248:251]\n\t"
    "v_perm_b32 v36, v214, v210, s61\n\t"
    "v_mfma_f32_16x16x32_bf16 a[216:219], v[40:43], v[100:103], a[216:219]\n\t"
    "v_mfma_f32_16x16x32_bf16 a[212:215], v[40:43], v[96:99], a[212:215]\n\t"
    "v_perm_b32 v33, v222, v218, s60\n\t"
    "v_mfma_f32_16x16x32_bf16 a[244:247], v[44:47], v[96:99], a[244:247]\n\t"
    "v_mfma_f32_16x16x32_bf16 a[240:243], v[44:47], v[92:95], a[240:243]\n\t"
    "v_perm_b32 v37, v222, v218, s61\n\t"
    "v_mfma_f32_16x16x32_bf16 a[208:211], v[40:43], v[92:95], a[208:211]\n\t"
    "buffer_load_dwordx4 v[196:199], v5, s[96:99], s62 offen offset:1024\n\t"
    "v_mfma_f32_16x16x32_bf16 a[204:207], v[40:43], v[88:91], a[204:207]\n\t"
    "v_perm_b32 v34, v230, v226, s60\n\t"
    "v_mfma_f32_16x16x32_bf16 a[236:239], v[44:47], v[88:91], a[236:239]\n\t"
    "v_mfma_f32_16x16x32_bf16 a[232:235], v[44:47], v[84:87], a[232:235]\n\t"
    "v_perm_b32 v38, v230, v226, s61\n\t"
    "v_mfma_f32_16x16x32_bf16 a[200:203], v[40:43], v[84:87], a[200:203]\n\t"
    "v_mfma_f32_16x16x32_bf16 a[196:199], v[40:43], v[80:83], a[196:199]\n\t"
    "v_perm_b32 v35, v238, v234, s60\n\t"
    "v_mfma_f32_16x16x32_bf16 a[228:231], v[44:47], v[80:83], a[228:231]\n\t"
    "v_mfma_f32_16x16x32_bf16 a[224:227], v[44:47], v[12:15], a[224:227]\n\t"
    "v_perm_b32 v39, v238, v234, s61\n\t"
    "v_mfma_f32_16x16x32_bf16 a[192:195], v[40:43], v[12:15], a[192:195]\n\t"
    "s_waitcnt lgkmcnt(0)\n\t"
    "v_mfma_f32_16x16x32_bf16 a[0:3], v[16:19], v[108:111], a[0:3]\n\t"
    "v_perm_b32 v40, v215, v211, s60\n\t"
    "v_mfma_f32_16x16x32_bf16 a[32:35], v[20:23], v[108:111], a[32:35]\n\t"
    "s_add_u32 s62, %[tb], 0x10000\n\t"
    "buffer_load_dwordx4 v[200:203], v5, s[96:99], s62 offen offset:2048\n\t"
    "v_mfma_f32_16x16x32_bf16 a[36:39], v[20:23], v[12:15], a[36:39]\n\t"
    "v_perm_b32 v44, v215, v211, s61\n\t"
    "v_mfma_f32_16x16x32_bf16 a[4:7], v[16:19], v[12:15], a[4:7]\n\t"
    "ds_read_b128 v[104:107], v11 offset:256\n\t"
    "v_mfma_f32_16x16x32_bf16 a[8:11], v[16:19], v[80:83], a[8:11]\n\t"
    "v_perm_b32 v41, v223, v219, s60\n\t"
    "v_mfma_f32_16x16x32_bf16 a[40:43], v[20:23], v[80:83], a[40:43]\n\t"
    "s_add_u32 m0, s68, 47104\n\t"
    "s_add_u32 s64, s85, 256\n\t"
    "v_xor_b32 v6, 96, %[vxo]\n\t"
    "v_mfma_f32_16x16x32_bf16 a[44:47], v[20:23], v[84:87], a[44:47]\n\t"
    "v_perm_b32 v45, v223, v219, s61\n\t"
    "v_mfma_f32_16x16x32_bf16 a[12:15], v[16:19], v[84:87], a[12:15]\n\t"
    "buffer_load_dwordx4 v[204:207], v5, s[96:99], s62 offen offset:3072\n\t"
    "buffer_load_dwordx4 v6, s[92:95], s64 offen lds\n\t"
    "v_mfma_f32_16x16x32_bf16 a[16:19], v[16:19], v[88:91], a[16:19]\n\t"
    "v_perm_b32 v42, v231, v227, s60\n\t"
    "v_mfma_f32_16x16x32_bf16 a[48:51], v[20:23], v[88:91], a[48:51]\n\t"
    "v_mfma_f32_16x16x32_bf16 a[52:55], v[20:23], v[92:95], a[52:55]\n\t"
    "v_perm_b32 v46, v231, v227, s61\n\t"
    "v_mfma_f32_16x16x32_bf16 a[20:23], v[16:19], v[92:95], a[20:23]\n\t"
    "v_mfma_f32_16x16x32_bf16 a[24:27], v[16:19], v[96:99], a[24:27]\n\t"
    "v_perm_b32 v43, v239, v235, s60\n\t"
    "v_mfma_f32_16x16x32_bf16 a[56:59], v[20:23], v[96:99], a[56:59]\n\t"
    "v_mfma_f32_16x16x32_bf16 a[60:63], v[20:23], v[100:103], a[60:63]\n\t"
    "v_perm_b32 v47, v239, v235, s61\n\t"
    "v_mfma_f32_16x16x32_bf16 a[28:31], v[16:19], v[100:103], a[28:31]\n\t"
    "s_waitcnt vmcnt(18)\n\t"
    "v_mfma_f32_16x16x32_bf16 a[92:95], v[24:27], v[100:103], a[92:95]\n\t"
    "v_perm_b32 v16, v116, v112, s60\n\t"
    "v_mfma_f32_16x16x32_bf16 a[124:127], v[28:31], v[100:103], a[124:127]\n\t"
    "s_add_u32 s62, s63, 0x10000\n\t"
    "buffer_load_dwordx4 v[208:211], v4, s[96:99], s62 offen\n\t"
    "v_mfma_f32_16x16x32_bf16 a[120:123], v[28:31], v[96:99], a[120:123]\n\t"
    "v_perm_b32 v20, v116, v112, s61\n\t"
    "v_mfma_f32_16x16x32_bf16 a[88:91], v[24:27], v[96:99], a[88:91]\n\t"
    "v_mfma_f32_16x16x32_bf16 a[84:87], v[24:27], v[92:95], a[84:87]\n\t"
    "v_perm_b32 v17, v124, v120, s60\n\t"
    "v_mfma_f32_16x16x32_bf16 a[116:119], v[28:31], v[92:95], a[116:119]\n\t"
    "buffer_load_dwordx4 v[212:215], v4, s[96:99], s62 offen offset:1024\n\t"
    "v_mfma_f32_16x16x32_bf16 a[112:115], v[28:31], v[88:91], a[112:115]\n\t"
    "v_perm_b32 v21, v124, v120, s61\n\t"
    "v_mfma_f32_16x16x32_bf16 a[80:83], v[24:27], v[88:91], a[80:83]\n\t"
    "v_mfma_f32_16x16x32_bf16 a[76:79], v[24:27], v[84:87], a[76:79]\n\t"
    "v_perm_b32 v18, v132, v128, s60\n\t"
    "v_mfma_f32_16x16x32_bf16 a[108:111], v[28:31], v[84:87], a[108:111]\n\t"
    "buffer_load_dwordx4 v[216:219], v4, s[96:99], s62 offen offset:2048\n\t"
    "v_mfma_f32_16x16x32_bf16 a[104:107], v[28:31], v[80:83], a[104:107]\n\t"
    "v_perm_b32 v22, v132, v128, s61\n\t"
    "v_mfma_f32_16x16x32_bf16 a[72:75], v[24:27], v[80:83], a[72:75]\n\t"
    "v_mfma_f32_16x16x32_bf16 a[68:71], v[24:27], v[12:15], a[68:71]\n\t"
    "v_perm_b32 v19, v140, v136, s60\n\t"
    "v_mfma_f32_16x16x32_bf16 a[100:103], v[28:31], v[12:15], a[100:103]\n\t"
    "v_mfma_f32_16x16x32_bf16 a[96:99], v[28:31], v[108:111], a[96:99]\n\t"
    "v_perm_b32 v23, v140, v136, s61\n\t"
    "v_mfma_f32_16x16x32_bf16 a[64:67], v[24:27], v[108:111], a[64:67]\n\t"
    "buffer_load_dwordx4 v[220:223], v4, s[96:99], s62 offen offset:3072\n\t"
    "v_mfma_f32_16x16x32_bf16 a[128:131], v[32:35], v[108:111], a[128:131]\n\t"
    "v_perm_b32 v24, v117, v113, s60\n\t"
    "v_mfma_f32_16x16x32_bf16 a[160:163], v[36:39], v[108:111], a[160:163]\n\t"
    "v_mfma_f32_16x16x32_bf16 a[164:167], v[36:39], v[12:15], a[164:167]\n\t"
    "v_perm_b32 v28, v117, v113, s61\n\t"
    "v_mfma_f32_16x16x32_bf16 a[132:135], v[32:35], v[12:15], a[132:135]\n\t"
    "v_mfma_f32_16x16x32_bf16 a[136:139], v[32:35], v[80:83], a[136:139]\n\t"
    "v_perm_b32 v25, v125, v121, s60\n\t"
    "v_mfma_f32_16x16x32_bf16 a[168:171], v[36:39], v[80:83], a[168:171]\n\t"
    "buffer_load_dwordx4 v[224:227], v5, s[96:99], s62 offen\n\t"
    "v_mfma_f32_16x16x32_bf16 a[172:175], v[36:39], v[84:87], a[172:175]\n\t"
    "v_perm_b32 v29, v125, v121, s61\n\t"
    "v_mfma_f32_16x16x32_bf16 a[140:143], v[32:35], v[84:87], a[140:143]\n\t"
    "v_mfma_f32_16x16x32_bf16 a[144:147], v[32:35], v[88:91], a[144:147]\n\t"
    "v_perm_b32 v26, v133, v129, s60\n\t"
    "v_mfma_f32_16x16x32_bf16 a[176:179], v[36:39], v[88:91], a[176:179]\n\t"
    "v_mfma_f32_16x16x32_bf16 a[180:183], v[36:39], v[92:95], a[180:183]\n\t"
    "v_perm_b32 v30, v133, v129, s61\n\t"
    "v_mfma_f32_16x16x32_bf16 a[148:151], v[32:35], v[92:95], a[148:151]\n\t"
    "buffer_load_dwordx4 v[228:231], v5, s[96:99], s62 offen offset:1024\n\t"
    "v_mfma_f32_16x16x32_bf16 a[152:155], v[32:35], v[96:99], a[152:155]\n\t"
    "v_perm_b32 v27, v141, v137, s60\n\t"
    "v_mfma_f32_16x16x32_bf16 a[184:187], v[36:39], v[96:99], a[184:187]\n\t"
    "v_mfma_f32_16x16x32_bf16 a[188:191], v[36:39], v[100:103], a[188:191]\n\t"
    "v_perm_b32 v31, v141, v137, s61\n\t"
    "v_mfma_f32_16x16x32_bf16 a[156:159], v[32:35], v[100:103], a[156:159]\n\t"
    "v_mfma_f32_16x16x32_bf16 a[220:223], v[40:43], v[100:103], a[220:223]\n\t"
    "v_perm_b32 v32, v118, v114, s60\n\t"
    "v_mfma_f32_16x16x32_bf16 a[252:255], v[44:47], v[100:103], a[252:255]\n\t"
    "buffer_load_dwordx4 v[232:235], v5, s[96:99], s62 offen offset:2048\n\t"
    "v_mfma_f32_16x16x32_bf16 a[248:251], v[44:47], v[96:99], a[248:251]\n\t"
    "v_perm_b32 v36, v118, v114, s61\n\t"
    "v_mfma_f32_16x16x32_bf16 a[216:219], v[40:43], v[96:99], a[216:219]\n\t"
    "v_mfma_f32_16x16x32_bf16 a[212:215], v[40:43], v[92:95], a[212:215]\n\t"
    "v_perm_b32 v33, v126, v122, s60\n\t"
    "v_mfma_f32_16x16x32_bf16 a[244:247], v[44:47], v[92:95], a[244:247]\n\t"
    "v_mfma_f32_16x16x32_bf16 a[240:243], v[44:47], v[88:91], a[240:243]\n\t"
    "v_perm_b32 v37, v126, v122, s61\n\t"
    "v_mfma_f32_16x16x32_bf16 a[208:211], v[40:43], v[88:91], a[208:211]\n\t"
    "buffer_load_dwordx4 v[236:239], v5, s[96:99], s62 offen offset:3072\n\t"
    "v_mfma_f32_16x16x32_bf16 a[204:207], v[40:43], v[84:87], a[204:207]\n\t"
    "v_perm_b32 v34, v134, v130, s60\n\t"
    "v_mfma_f32_16x16x32_bf16 a[236:239], v[44:47], v[84:87], a[236:239]\n\t"
    "v_mfma_f32_16x16x32_bf16 a[232:235], v[44:47], v[80:83], a[232:235]\n\t"
    "v_perm_b32 v38, v134, v130, s61\n\t"
    "v_mfma_f32_16x16x32_bf16 a[200:203], v[40:43], v[80:83], a[200:203]\n\t"
    "v_mfma_f32_16x16x32_bf16 a[196:199], v[40:43], v[12:15], a[196:199]\n\t"
    "v_perm_b32 v35, v142, v138, s60\n\t"
    "v_mfma_f32_16x16x32_bf16 a[228:231], v[44:47], v[12:15], a[228:231]\n\t"
    "v_mfma_f32_16x16x32_bf16 a[224:227], v[44:47], v[108:111], a[224:227]\n\t"
    "v_perm_b32 v39, v142, v138, s61\n\t"
    "v_mfma_f32_16x16x32_bf16 a[192:195], v[40:43], v[108:111], a[192:195]\n\t"
    "s_waitcnt lgkmcnt(0)\n\t"
    "s_waitcnt vmcnt(52)\n\t"
    "s_barrier\n\t"
    "v_mfma_f32_16x16x32_bf16 a[0:3], v[16:19], v[104:107], a[0:3]\n\t"
    "v_perm_b32 v40, v119, v115, s60\n\t"
    "v_mfma_f32_16x16x32_bf16 a[32:35], v[20:23], v[104:107], a[32:35]\n\t"
    "v_mfma_f32_16x16x32_bf16 a[36:39], v[20:23], v[108:111], a[36:39]\n\t"
    "v_perm_b32 v44, v119, v115, s61\n\t"
    "v_mfma_f32_16x16x32_bf16 a[4:7], v[16:19], v[108:111], a[4:7]\n\t"
    "ds_read_b128 v[100:103], v11 offset:0\n\t"
    "v_mfma_f32_16x16x32_bf16 a[8:11], v[16:19], v[12:15], a[8:11]\n\t"
    "v_perm_b32 v41, v127, v123, s60\n\t"
    "v_mfma_f32_16x16x32_bf16 a[40:43], v[20:23], v[12:15], a[40:43]\n\t"
    "s_add_u32 m0, s68, 52224\n\t"
    "s_add_u32 s64, %[gx], 384\n\t"
    "v_mfma_f32_16x16x32_bf16 a[44:47], v[20:23], v[80:83], a[44:47]\n\t"
    "v_perm_b32 v45, v127, v123, s61\n\t"
    "v_mfma_f32_16x16x32_bf16 a[12:15], v[16:19], v[80:83], a[12:15]\n\t"
    "buffer_load_dwordx4 %[vxo], s[92:95], s64 offen lds\n\t"
    "v_mfma_f32_16x16x32_bf16 a[16:19], v[16:19], v[84:87], a[16:19]\n\t"
    "v_perm_b32 v42, v135, v131, s60\n\t"
    "v_mfma_f32_16x16x32_bf16 a[48:51], v[20:23], v[84:87], a[48:51]\n\t"
    "v_mfma_f32_16x16x32_bf16 a[52:55], v[20:23], v[88:91], a[52:55]\n\t"
    "v_perm_b32 v46, v135, v131, s61\n\t"
    "v_mfma_f32_16x16x32_bf16 a[20:23], v[16:19], v[88:91], a[20:23]\n\t"
    "ds_read_b128 v[48:51], v8 offset:18432\n\t"
    "v_mfma_f32_16x16x32_bf16 a[24:27], v[16:19], v[92:95], a[24:27]\n\t"
    "v_perm_b32 v43, v143, v139, s60\n\t"
    "v_mfma_f32_16x16x32_bf16 a[56:59], v[20:23], v[92:95], a[56:59]\n\t"
    "v_mfma_f32_16x16x32_bf16 a[60:63], v[20:23], v[96:99], a[60:63]\n\t"
    "v_perm_b32 v47, v143, v139, s61\n\t"
    "v_mfma_f32_16x16x32_bf16 a[28:31], v[16:19], v[96:99], a[28:31]\n\t"
    "s_waitcnt vmcnt(18)\n\t"
    "s_mov_b32 s62, 0x90000\n\t"
    "v_mfma_f32_16x16x32_bf16 a[92:95], v[24:27], v[96:99], a[92:95]\n\t"
    "v_perm_b32 v16, v148, v144, s60\n\t"
    "v_mfma_f32_16x16x32_bf16 a[124:127], v[28:31], v[96:99], a[124:127]\n\t"
    "buffer_load_dwordx4 v[112:115], v4, s[96:99], s62 offen\n\t"
    "v_mfma_f32_16x16x32_bf16 a[120:123], v[28:31], v[92:95], a[120:123]\n\t"
    "v_perm_b32 v20, v148, v144, s61\n\t"
    "v_mfma_f32_16x16x32_bf16 a[88:91], v[24:27], v[92:95], a[88:91]\n\t"
    "ds_read_b128 v[52:55], v8 offset:18688\n\t"
    "v_mfma_f32_16x16x32_bf16 a[84:87], v[24:27], v[88:91], a[84:87]\n\t"
    "v_perm_b32 v17, v156, v152, s60\n\t"
    "v_mfma_f32_16x16x32_bf16 a[116:119], v[28:31], v[88:91], a[116:119]\n\t"
    "v_mfma_f32_16x16x32_bf16 a[112:115], v[28:31], v[84:87], a[112:115]\n\t"
    "v_perm_b32 v21, v156, v152, s61\n\t"
    "v_mfma_f32_16x16x32_bf16 a[80:83], v[24:27], v[84:87], a[80:83]\n\t"
    "buffer_load_dwordx4 v[116:119], v4, s[96:99], s62 offen offset:1024\n\t"
    "v_mfma_f32_16x16x32_bf16 a[76:79], v[24:27], v[80:83], a[76:79]\n\t"
    "v_perm_b32 v18, v164, v160, s60\n\t"
    "v_mfma_f32_16x16x32_bf16 a[108:111], v[28:31], v[80:83], a[108:111]\n\t"
    "v_mfma_f32_16x16x32_bf16 a[104:107], v[28:31], v[12:15], a[104:107]\n\t"
    "v_perm_b32 v22, v164, v160, s61\n\t"
    "v_mfma_f32_16x16x32_bf16 a[72:75], v[24:27], v[12:15], a[72:75]\n\t"
    "ds_read_b128 v[56:59], v8 offset:18944\n\t"
    "v_mfma_f32_16x16x32_bf16 a[68:71], v[24:27], v[108:111], a[68:71]\n\t"
    "v_perm_b32 v19, v172, v168, s60\n\t"
    "v_mfma_f32_16x16x32_bf16 a[100:103], v[28:31], v[108:111], a[100:103]\n\t"
    "buffer_load_dwordx4 v[120:123], v4, s[96:99], s62 offen offset:2048\n\t"
    "v_mfma_f32_16x16x32_bf16 a[96:99], v[28:31], v[104:107], a[96:99]\n\t"
    "v_perm_b32 v23, v172, v168, s61\n\t"
    "v_mfma_f32_16x16x32_bf16 a[64:67], v[24:27], v[104:107], a[64:67]\n\t"
    "v_mfma_f32_16x16x32_bf16 a[128:131], v[32:35], v[104:107], a[128:131]\n\t"
    "v_perm_b32 v24, v149, v145, s60\n\t"
    "v_mfma_f32_16x16x32_bf16 a[160:163], v[36:39], v[104:107], a[160:163]\n\t"
    "v_mfma_f32_16x16x32_bf16 a[164:167], v[36:39], v[108:111], a[164:167]\n\t"
    "v_perm_b32 v28, v149, v145, s61\n\t"
    "v_mfma_f32_16x16x32_bf16 a[132:135], v[32:35], v[108:111], a[132:135]\n\t"
    "buffer_load_dwordx4 v[124:127], v4, s[96:99], s62 offen offset:3072\n\t"
    "ds_read_b128 v[60:63], v8 offset:19200\n\t"
    "v_mfma_f32_16x16x32_bf16 a[136:139], v[32:35], v[12:15], a[136:139]\n\t"
    "v_perm_b32 v25, v157, v153, s60\n\t"
    "v_mfma_f32_16x16x32_bf16 a[168:171], v[36:39], v[12:15], a[168:171]\n\t"
    "v_mfma_f32_16x16x32_bf16 a[172:175], v[36:39], v[80:83], a[172:175]\n\t"
    "v_perm_b32 v29, v157, v153, s61\n\t"
    "v_mfma_f32_16x16x32_bf16 a[140:143], v[32:35], v[80:83], a[140:143]\n\t"
    "v_mfma_f32_16x16x32_bf16 a[144:147], v[32:35], v[84:87], a[144:147]\n\t"
    "v_perm_b32 v26, v165, v161, s60\n\t"
    "v_mfma_f32_16x16x32_bf16 a[176:179], v[36:39], v[84:87], a[176:179]\n\t"
    "buffer_load_dwordx4 v[128:131], v5, s[96:99], s62 offen\n\t"
    "v_mfma_f32_16x16x32_bf16 a[180:183], v[36:39], v[88:91], a[180:183]\n\t"
    "v_perm_b32 v30, v165, v161, s61\n\t"
    "v_mfma_f32_16x16x32_bf16 a[148:151], v[32:35], v[88:91], a[148:151]\n\t"
    "v_mfma_f32_16x16x32_bf16 a[152:155], v[32:35], v[92:95], a[152:155]\n\t"
    "v_perm_b32 v27, v173, v169, s60\n\t"
    "v_mfma_f32_16x16x32_bf16 a[184:187], v[36:39], v[92:95], a[184:187]\n\t"
    "v_mfma_f32_16x16x32_bf16 a[188:191], v[36:39], v[96:99], a[188:191]\n\t"
    "v_perm_b32 v31, v173, v169, s61\n\t"
    "v_mfma_f32_16x16x32_bf16 a[156:159], v[32:35], v[96:99], a[156:159]\n\t"
    "buffer_load_dwordx4 v[132:135], v5, s[96:99], s62 offen offset:1024\n\t"
    "v_mfma_f32_16x16x32_bf16 a[220:223], v[40:43], v[96:99], a[220:223]\n\t"
    "v_perm_b32 v32, v150, v146, s60\n\t"
    "v_mfma_f32_16x16x32_bf16 a[252:255], v[44:47], v[96:99], a[252:255]\n\t"
    "v_mfma_f32_16x16x32_bf16 a[248:251], v[44:47], v[92:95], a[248:251]\n\t"
    "v_perm_b32 v36, v150, v146, s61\n\t"
    "v_mfma_f32_16x16x32_bf16 a[216:219], v[40:43], v[92:95], a[216:219]\n\t"
    "v_mfma_f32_16x16x32_bf16 a[212:215], v[40:43], v[88:91], a[212:215]\n\t"
    "v_perm_b32 v33, v158, v154, s60\n\t"
    "v_mfma_f32_16x16x32_bf16 a[244:247], v[44:47], v[88:91], a[244:247]\n\t"
    "buffer_load_dwordx4 v[136:139], v5, s[96:99], s62 offen offset:2048\n\t"
    "v_mfma_f32_16x16x32_bf16 a[240:243], v[44:47], v[84:87], a[240:243]\n\t"
    "v_perm_b32 v37, v158, v154, s61\n\t"
    "v_mfma_f32_16x16x32_bf16 a[208:211], v[40:43], v[84:87], a[208:211]\n\t"
    "v_mfma_f32_16x16x32_bf16 a[204:207], v[40:43], v[80:83], a[204:207]\n\t"
    "v_perm_b32 v34, v166, v162, s60\n\t"
    "v_mfma_f32_16x16x32_bf16 a[236:239], v[44:47], v[80:83], a[236:239]\n\t"
    "v_mfma_f32_16x16x32_bf16 a[232:235], v[44:47], v[12:15], a[232:235]\n\t"
    "v_perm_b32 v38, v166, v162, s61\n\t"
    "v_mfma_f32_16x16x32_bf16 a[200:203], v[40:43], v[12:15], a[200:203]\n\t"
    "buffer_load_dwordx4 v[140:143], v5, s[96:99], s62 offen offset:3072\n\t"
    "v_mfma_f32_16x16x32_bf16 a[196:199], v[40:43], v[108:111], a[196:199]\n\t"
    "v_perm_b32 v35, v174, v170, s60\n\t"
    "v_mfma_f32_16x16x32_bf16 a[228:231], v[44:47], v[108:111], a[228:231]\n\t"
    "v_mfma_f32_16x16x32_bf16 a[224:227], v[44:47], v[104:107], a[224:227]\n\t"
    "v_perm_b32 v39, v174, v170, s61\n\t"
    "v_mfma_f32_16x16x32_bf16 a[192:195], v[40:43], v[104:107], a[192:195]\n\t"
    "s_waitcnt lgkmcnt(0)\n\t"
    "v_mfma_f32_16x16x32_bf16 a[0:3], v[16:19], v[100:103], a[0:3]\n\t"
    "v_perm_b32 v40, v151, v147, s60\n\t"
    "v_mfma_f32_16x16x32_bf16 a[32:35], v[20:23], v[100:103], a[32:35]\n\t"
    "v_mfma_f32_16x16x32_bf16 a[36:39], v[20:23], v[104:107], a[36:39]\n\t"
    "v_perm_b32 v44, v151, v147, s61\n\t"
    "v_mfma_f32_16x16x32_bf16 a[4:7], v[16:19], v[104:107], a[4:7]\n\t"
    "v_mfma_f32_16x16x32_bf16 a[8:11], v[16:19], v[108:111], a[8:11]\n\t"
    "v_perm_b32 v41, v159, v155, s60\n\t"
    "v_mfma_f32_16x16x32_bf16 a[40:43], v[20:23], v[108:111], a[40:43]\n\t"
    "s_add_u32 m0, s68, 56320\n\t"
    "s_add_u32 s64, s83, 384\n\t"
    "v_xor_b32 v6, 32, %[vxo]\n\t"
    "v_mfma_f32_16x16x32_bf16 a[44:47], v[20:23], v[12:15], a[44:47]\n\t"
    "v_perm_b32 v45, v159, v155, s61\n\t"
    "v_mfma_f32_16x16x32_bf16 a[12:15], v[16:19], v[12:15], a[12:15]\n\t"
    "buffer_load_dwordx4 v6, s[92:95], s64 offen lds\n\t"
    "v_mfma_f32_16x16x32_bf16 a[16:19], v[16:19], v[80:83], a[16:19]\n\t"
    "v_perm_b32 v42, v167, v163, s60\n\t"
    "v_mfma_f32_16x16x32_bf16 a[48:51], v[20:23], v[80:83], a[48:51]\n\t"
    "v_mfma_f32_16x16x32_bf16 a[52:55], v[20:23], v[84:87], a[52:55]\n\t"
    "v_perm_b32 v46, v167, v163, s61\n\t"
    "v_mfma_f32_16x16x32_bf16 a[20:23], v[16:19], v[84:87], a[20:23]\n\t"
    "ds_read_b128 v[64:67], v8 offset:19456\n\t"
    "v_mfma_f32_16x16x32_bf16 a[24:27], v[16:19], v[88:91], a[24:27]\n\t"
    "v_perm_b32 v43, v175, v171, s60\n\t"
    "v_mfma_f32_16x16x32_bf16 a[56:59], v[20:23], v[88:91], a[56:59]\n\t"
    "v_mfma_f32_16x16x32_bf16 a[60:63], v[20:23], v[92:95], a[60:63]\n\t"
    "v_perm_b32 v47, v175, v171, s61\n\t"
    "v_mfma_f32_16x16x32_bf16 a[28:31], v[16:19], v[92:95], a[28:31]\n\t"
    "s_waitcnt vmcnt(19)\n\t"
    "s_add_u32 s62, s70, 0x10000\n\t"
    "v_mfma_f32_16x16x32_bf16 a[92:95], v[24:27], v[92:95], a[92:95]\n\t"
    "v_perm_b32 v16, v180, v176, s60\n\t"
    "v_mfma_f32_16x16x32_bf16 a[124:127], v[28:31], v[92:95], a[124:127]\n\t"
    "buffer_load_dwordx4 v[144:147], v4, s[96:99], s62 offen\n\t"
    "v_mfma_f32_16x16x32_bf16 a[120:123], v[28:31], v[88:91], a[120:123]\n\t"
    "v_perm_b32 v20, v180, v176, s61\n\t"
    "v_mfma_f32_16x16x32_bf16 a[88:91], v[24:27], v[88:91], a[88:91]\n\t"
    "ds_read_b128 v[68:71], v8 offset:19712\n\t"
    "v_mfma_f32_16x16x32_bf16 a[84:87], v[24:27], v[84:87], a[84:87]\n\t"
    "v_perm_b32 v17, v188, v184, s60\n\t"
    "v_mfma_f32_16x16x32_bf16 a[116:119], v[28:31], v[84:87], a[116:119]\n\t"
    "v_mfma_f32_16x16x32_bf16 a[112:115], v[28:31], v[80:83], a[112:115]\n\t"
    "v_perm_b32 v21, v188, v184, s61\n\t"
    "v_mfma_f32_16x16x32_bf16 a[80:83], v[24:27], v[80:83], a[80:83]\n\t"
    "buffer_load_dwordx4 v[148:151], v4, s[96:99], s62 offen offset:1024\n\t"
    "v_mfma_f32_16x16x32_bf16 a[76:79], v[24:27], v[12:15], a[76:79]\n\t"
    "v_perm_b32 v18, v196, v192, s60\n\t"
    "v_mfma_f32_16x16x32_bf16 a[108:111], v[28:31], v[12:15], a[108:111]\n\t"
    "v_mfma_f32_16x16x32_bf16 a[104:107], v[28:31], v[108:111], a[104:107]\n\t"
    "v_perm_b32 v22, v196, v192, s61\n\t"
    "v_mfma_f32_16x16x32_bf16 a[72:75], v[24:27], v[108:111], a[72:75]\n\t"
    "ds_read_b128 v[72:75], v8 offset:19968\n\t"
    "v_mfma_f32_16x16x32_bf16 a[68:71], v[24:27], v[104:107], a[68:71]\n\t"
    "v_perm_b32 v19, v204, v200, s60\n\t"
    "v_mfma_f32_16x16x32_bf16 a[100:103], v[28:31], v[104:107], a[100:103]\n\t"
    "buffer_load_dwordx4 v[152:155], v4, s[96:99], s62 offen offset:2048\n\t"
    "v_mfma_f32_16x16x32_bf16 a[96:99], v[28:31], v[100:103], a[96:99]\n\t"
    "v_perm_b32 v23, v204, v200, s61\n\t"
    "v_mfma_f32_16x16x32_bf16 a[64:67], v[24:27], v[100:103], a[64:67]\n\t"
    "v_mfma_f32_16x16x32_bf16 a[128:131], v[32:35], v[100:103], a[128:131]\n\t"
    "v_perm_b32 v24, v181, v177, s60\n\t"
    "v_mfma_f32_16x16x32_bf16 a[160:163], v[36:39], v[100:103], a[160:163]\n\t"
    "v_mfma_f32_16x16x32_bf16 a[164:167], v[36:39], v[104:107], a[164:167]\n\t"
    "v_perm_b32 v28, v181, v177, s61\n\t"
    "v_mfma_f32_16x16x32_bf16 a[132:135], v[32:35], v[104:107], a[132:135]\n\t"
    "buffer_load_dwordx4 v[156:159], v4, s[96:99], s62 offen offset:3072\n\t"
    "ds_read_b128 v[76:79], v8 offset:20224\n\t"
    "v_mfma_f32_16x16x32_bf16 a[136:139], v[32:35], v[108:111], a[136:139]\n\t"
    "v_perm_b32 v25, v189, v185, s60\n\t"
    "v_mfma_f32_16x16x32_bf16 a[168:171], v[36:39], v[108:111], a[168:171]\n\t"
    "v_mfma_f32_16x16x32_bf16 a[172:175], v[36:39], v[12:15], a[172:175]\n\t"
    "v_perm_b32 v29, v189, v185, s61\n\t"
    "v_mfma_f32_16x16x32_bf16 a[140:143], v[32:35], v[12:15], a[140:143]\n\t"
    "v_mfma_f32_16x16x32_bf16 a[144:147], v[32:35], v[80:83], a[144:147]\n\t"
    "v_perm_b32 v26, v197, v193, s60\n\t"
    "v_mfma_f32_16x16x32_bf16 a[176:179], v[36:39], v[80:83], a[176:179]\n\t"
    "buffer_load_dwordx4 v[160:163], v5, s[96:99], s62 offen\n\t"
    "v_mfma_f32_16x16x32_bf16 a[180:183], v[36:39], v[84:87], a[180:183]\n\t"
    "v_perm_b32 v30, v197, v193, s61\n\t"
    "v_mfma_f32_16x16x32_bf16 a[148:151], v[32:35], v[84:87], a[148:151]\n\t"
    "v_mfma_f32_16x16x32_bf16 a[152:155], v[32:35], v[88:91], a[152:155]\n\t"
    "v_perm_b32 v27, v205, v201, s60\n\t"
    "v_mfma_f32_16x16x32_bf16 a[184:187], v[36:39], v[88:91], a[184:187]\n\t"
    "v_mfma_f32_16x16x32_bf16 a[188:191], v[36:39], v[92:95], a[188:191]\n\t"
    "v_perm_b32 v31, v205, v201, s61\n\t"
    "v_mfma_f32_16x16x32_bf16 a[156:159], v[32:35], v[92:95], a[156:159]\n\t"
    "buffer_load_dwordx4 v[164:167], v5, s[96:99], s62 offen offset:1024\n\t"
    "v_mfma_f32_16x16x32_bf16 a[220:223], v[40:43], v[92:95], a[220:223]\n\t"
    "v_perm_b32 v32, v182, v178, s60\n\t"
    "v_mfma_f32_16x16x32_bf16 a[252:255], v[44:47], v[92:95], a[252:255]\n\t"
    "v_mfma_f32_16x16x32_bf16 a[248:251], v[44:47], v[88:91], a[248:251]\n\t"
    "v_perm_b32 v36, v182, v178, s61\n\t"
    "v_mfma_f32_16x16x32_bf16 a[216:219], v[40:43], v[88:91], a[216:219]\n\t"
    "v_mfma_f32_16x16x32_bf16 a[212:215], v[40:43], v[84:87], a[212:215]\n\t"
    "v_perm_b32 v33, v190, v186, s60\n\t"
    "v_mfma_f32_16x16x32_bf16 a[244:247], v[44:47], v[84:87], a[244:247]\n\t"
    "buffer_load_dwordx4 v[168:171], v5, s[96:99], s62 offen offset:2048\n\t"
    "v_mfma_f32_16x16x32_bf16 a[240:243], v[44:47], v[80:83], a[240:243]\n\t"
    "v_perm_b32 v37, v190, v186, s61\n\t"
    "v_mfma_f32_16x16x32_bf16 a[208:211], v[40:43], v[80:83], a[208:211]\n\t"
    "v_mfma_f32_16x16x32_bf16 a[204:207], v[40:43], v[12:15], a[204:207]\n\t"
    "v_perm_b32 v34, v198, v194, s60\n\t"
    "v_mfma_f32_16x16x32_bf16 a[236:239], v[44:47], v[12:15], a[236:239]\n\t"
    "v_mfma_f32_16x16x32_bf16 a[232:235], v[44:47], v[108:111], a[232:235]\n\t"
    "v_perm_b32 v38, v198, v194, s61\n\t"
    "v_mfma_f32_16x16x32_bf16 a[200:203], v[40:43], v[108:111], a[200:203]\n\t"
    "buffer_load_dwordx4 v[172:175], v5, s[96:99], s62 offen offset:3072\n\t"
    "v_mfma_f32_16x16x32_bf16 a[196:199], v[40:43], v[104:107], a[196:199]\n\t"
    "v_perm_b32 v35, v206, v202, s60\n\t"
    "v_mfma_f32_16x16x32_bf16 a[228:231], v[44:47], v[104:107], a[228:231]\n\t"
    "v_mfma_f32_16x16x32_bf16 a[224:227], v[44:47], v[100:103], a[224:227]\n\t"
    "v_perm_b32 v39, v206, v202, s61\n\t"
    "v_mfma_f32_16x16x32_bf16 a[192:195], v[40:43], v[100:103], a[192:195]\n\t"
    "s_waitcnt lgkmcnt(0)\n\t"
    "v_mfma_f32_16x16x32_bf16 a[0:3], v[16:19], v[48:51], a[0:3]\n\t"
    "v_perm_b32 v40, v183, v179, s60\n\t"
    "v_mfma_f32_16x16x32_bf16 a[32:35], v[20:23], v[48:51], a[32:35]\n\t"
    "v_mfma_f32_16x16x32_bf16 a[36:39], v[20:23], v[52:55], a[36:39]\n\t"
    "v_perm_b32 v44, v183, v179, s61\n\t"
    "v_mfma_f32_16x16x32_bf16 a[4:7], v[16:19], v[52:55], a[4:7]\n\t"
    "ds_read_b128 v[12:15], v9 offset:18176\n\t"
    "v_mfma_f32_16x16x32_bf16 a[8:11], v[16:19], v[56:59], a[8:11]\n\t"
    "v_perm_b32 v41, v191, v187, s60\n\t"
    "v_mfma_f32_16x16x32_bf16 a[40:43], v[20:23], v[56:59], a[40:43]\n\t"
    "s_add_u32 m0, s68, 60416\n\t"
    "s_add_u32 s64, s84, 384\n\t"
    "v_xor_b32 v6, 64, %[vxo]\n\t"
    "v_mfma_f32_16x16x32_bf16 a[44:47], v[20:23], v[60:63], a[44:47]\n\t"
    "v_perm_b32 v45, v191, v187, s61\n\t"
    "v_mfma_f32_16x16x32_bf16 a[12:15], v[16:19], v[60:63], a[12:15]\n\t"
    "buffer_load_dwordx4 v6, s[92:95], s64 offen lds\n\t"
    "v_mfma_f32_16x16x32_bf16 a[16:19], v[16:19], v[64:67], a[16:19]\n\t"
    "v_perm_b32 v42, v199, v195, s60\n\t"
    "v_mfma_f32_16x16x32_bf16 a[48:51], v[20:23], v[64:67], a[48:51]\n\t"
    "v_mfma_f32_16x16x32_bf16 a[52:55], v[20:23], v[68:71], a[52:55]\n\t"
    "v_perm_b32 v46, v199, v195, s61\n\t"
    "v_mfma_f32_16x16x32_bf16 a[20:23], v[16:19], v[68:71], a[20:23]\n\t"
    "v_mfma_f32_16x16x32_bf16 a[24:27], v[16:19], v[72:75], a[24:27]\n\t"
    "v_perm_b32 v43, v207, v203, s60\n\t"
    "v_mfma_f32_16x16x32_bf16 a[56:59], v[20:23], v[72:75], a[56:59]\n\t"
    "v_mfma_f32_16x16x32_bf16 a[60:63], v[20:23], v[76:79], a[60:63]\n\t"
    "v_perm_b32 v47, v207, v203, s61\n\t"
    "v_mfma_f32_16x16x32_bf16 a[28:31], v[16:19], v[76:79], a[28:31]\n\t"
    "s_waitcnt vmcnt(19)\n\t"
    "s_add_u32 s62, s72, 0x10000\n\t"
    "v_mfma_f32_16x16x32_bf16 a[92:95], v[24:27], v[76:79], a[92:95]\n\t"
    "v_perm_b32 v16, v212, v208, s60\n\t"
    "v_mfma_f32_16x16x32_bf16 a[124:127], v[28:31], v[76:79], a[124:127]\n\t"
    "buffer_load_dwordx4 v[176:179], v4, s[96:99], s62 offen\n\t"
    "v_mfma_f32_16x16x32_bf16 a[120:123], v[28:31], v[72:75], a[120:123]\n\t"
    "v_perm_b32 v20, v212, v208, s61\n\t"
    "v_mfma_f32_16x16x32_bf16 a[88:91], v[24:27], v[72:75], a[88:91]\n\t"
    "v_mfma_f32_16x16x32_bf16 a[84:87], v[24:27], v[68:71], a[84:87]\n\t"
    "v_perm_b32 v17, v220, v216, s60\n\t"
    "v_mfma_f32_16x16x32_bf16 a[116:119], v[28:31], v[68:71], a[116:119]\n\t"
    "v_mfma_f32_16x16x32_bf16 a[112:115], v[28:31], v[64:67], a[112:115]\n\t"
    "v_perm_b32 v21, v220, v216, s61\n\t"
    "v_mfma_f32_16x16x32_bf16 a[80:83], v[24:27], v[64:67], a[80:83]\n\t"
    "buffer_load_dwordx4 v[180:183], v4, s[96:99], s62 offen offset:1024\n\t"
    "v_mfma_f32_16x16x32_bf16 a[76:79], v[24:27], v[60:63], a[76:79]\n\t"
    "v_perm_b32 v18, v228, v224, s60\n\t"
    "v_mfma_f32_16x16x32_bf16 a[108:111], v[28:31], v[60:63], a[108:111]\n\t"
    "v_mfma_f32_16x16x32_bf16 a[104:107], v[28:31], v[56:59], a[104:107]\n\t"
    "v_perm_b32 v22, v228, v224, s61\n\t"
    "v_mfma_f32_16x16x32_bf16 a[72:75], v[24:27], v[56:59], a[72:75]\n\t"
    "v_mfma_f32_16x16x32_bf16 a[68:71], v[24:27], v[52:55], a[68:71]\n\t"
    "v_perm_b32 v19, v236, v232, s60\n\t"
    "v_mfma_f32_16x16x32_bf16 a[100:103], v[28:31], v[52:55], a[100:103]\n\t"
    "buffer_load_dwordx4 v[184:187], v4, s[96:99], s62 offen offset:2048\n\t"
    "v_mfma_f32_16x16x32_bf16 a[96:99], v[28:31], v[48:51], a[96:99]\n\t"
    "v_perm_b32 v23, v236, v232, s61\n\t"
    "v_mfma_f32_16x16x32_bf16 a[64:67], v[24:27], v[48:51], a[64:67]\n\t"
    "v_mfma_f32_16x16x32_bf16 a[128:131], v[32:35], v[48:51], a[128:131]\n\t"
    "v_perm_b32 v24, v213, v209, s60\n\t"
    "v_mfma_f32_16x16x32_bf16 a[160:163], v[36:39], v[48:51], a[160:163]\n\t"
    "v_mfma_f32_16x16x32_bf16 a[164:167], v[36:39], v[52:55], a[164:167]\n\t"
    "v_perm_b32 v28, v213, v209, s61\n\t"
    "v_mfma_f32_16x16x32_bf16 a[132:135], v[32:35], v[52:55], a[132:135]\n\t"
    "buffer_load_dwordx4 v[188:191], v4, s[96:99], s62 offen offset:3072\n\t"
    "v_mfma_f32_16x16x32_bf16 a[136:139], v[32:35], v[56:59], a[136:139]\n\t"
    "v_perm_b32 v25, v221, v217, s60\n\t"
    "v_mfma_f32_16x16x32_bf16 a[168:171], v[36:39], v[56:59], a[168:171]\n\t"
    "v_mfma_f32_16x16x32_bf16 a[172:175], v[36:39], v[60:63], a[172:175]\n\t"
    "v_perm_b32 v29, v221, v217, s61\n\t"
    "v_mfma_f32_16x16x32_bf16 a[140:143], v[32:35], v[60:63], a[140:143]\n\t"
    "v_mfma_f32_16x16x32_bf16 a[144:147], v[32:35], v[64:67], a[144:147]\n\t"
    "v_perm_b32 v26, v229, v225, s60\n\t"
    "v_mfma_f32_16x16x32_bf16 a[176:179], v[36:39], v[64:67], a[176:179]\n\t"
    "buffer_load_dwordx4 v[192:195], v5, s[96:99], s62 offen\n\t"
    "v_mfma_f32_16x16x32_bf16 a[180:183], v[36:39], v[68:71], a[180:183]\n\t"
    "v_perm_b32 v30, v229, v225, s61\n\t"
    "v_mfma_f32_16x16x32_bf16 a[148:151], v[32:35], v[68:71], a[148:151]\n\t"
    "v_mfma_f32_16x16x32_bf16 a[152:155], v[32:35], v[72:75], a[152:155]\n\t"
    "v_perm_b32 v27, v237, v233, s60\n\t"
    "v_mfma_f32_16x16x32_bf16 a[184:187], v[36:39], v[72:75], a[184:187]\n\t"
    "v_mfma_f32_16x16x32_bf16 a[188:191], v[36:39], v[76:79], a[188:191]\n\t"
    "v_perm_b32 v31, v237, v233, s61\n\t"
    "v_mfma_f32_16x16x32_bf16 a[156:159], v[32:35], v[76:79], a[156:159]\n\t"
    "buffer_load_dwordx4 v[196:199], v5, s[96:99], s62 offen offset:1024\n\t"
    "v_mfma_f32_16x16x32_bf16 a[220:223], v[40:43], v[76:79], a[220:223]\n\t"
    "v_perm_b32 v32, v214, v210, s60\n\t"
    "v_mfma_f32_16x16x32_bf16 a[252:255], v[44:47], v[76:79], a[252:255]\n\t"
    "v_mfma_f32_16x16x32_bf16 a[248:251], v[44:47], v[72:75], a[248:251]\n\t"
    "v_perm_b32 v36, v214, v210, s61\n\t"
    "v_mfma_f32_16x16x32_bf16 a[216:219], v[40:43], v[72:75], a[216:219]\n\t"
    "v_mfma_f32_16x16x32_bf16 a[212:215], v[40:43], v[68:71], a[212:215]\n\t"
    "v_perm_b32 v33, v222, v218, s60\n\t"
    "v_mfma_f32_16x16x32_bf16 a[244:247], v[44:47], v[68:71], a[244:247]\n\t"
    "buffer_load_dwordx4 v[200:203], v5, s[96:99], s62 offen offset:2048\n\t"
    "v_mfma_f32_16x16x32_bf16 a[240:243], v[44:47], v[64:67], a[240:243]\n\t"
    "v_perm_b32 v37, v222, v218, s61\n\t"
    "v_mfma_f32_16x16x32_bf16 a[208:211], v[40:43], v[64:67], a[208:211]\n\t"
    "v_mfma_f32_16x16x32_bf16 a[204:207], v[40:43], v[60:63], a[204:207]\n\t"
    "v_perm_b32 v34, v230, v226, s60\n\t"
    "v_mfma_f32_16x16x32_bf16 a[236:239], v[44:47], v[60:63], a[236:239]\n\t"
    "v_mfma_f32_16x16x32_bf16 a[232:235], v[44:47], v[56:59], a[232:235]\n\t"
    "v_perm_b32 v38, v230, v226, s61\n\t"
    "v_mfma_f32_16x16x32_bf16 a[200:203], v[40:43], v[56:59], a[200:203]\n\t"
    "buffer_load_dwordx4 v[204:207], v5, s[96:99], s62 offen offset:3072\n\t"
    "v_mfma_f32_16x16x32_bf16 a[196:199], v[40:43], v[52:55], a[196:199]\n\t"
    "v_perm_b32 v35, v238, v234, s60\n\t"
    "v_mfma_f32_16x16x32_bf16 a[228:231], v[44:47], v[52:55], a[228:231]\n\t"
    "v_mfma_f32_16x16x32_bf16 a[224:227], v[44:47], v[48:51], a[224:227]\n\t"
    "v_perm_b32 v39, v238, v234, s61\n\t"
    "v_mfma_f32_16x16x32_bf16 a[192:195], v[40:43], v[48:51], a[192:195]\n\t"
    "s_waitcnt lgkmcnt(0)\n\t"
    "v_mfma_f32_16x16x32_bf16 a[0:3], v[16:19], v[12:15], a[0:3]\n\t"
    "v_perm_b32 v40, v215, v211, s60\n\t"
    "v_mfma_f32_16x16x32_bf16 a[32:35], v[20:23], v[12:15], a[32:35]\n\t"
    "v_mfma_f32_16x16x32_bf16 a[36:39], v[20:23], v[48:51], a[36:39]\n\t"
    "v_perm_b32 v44, v215, v211, s61\n\t"
    "v_mfma_f32_16x16x32_bf16 a[4:7], v[16:19], v[48:51], a[4:7]\n\t"
    "ds_read_b128 v[76:79], v9 offset:17920\n\t"
    "v_mfma_f32_16x16x32_bf16 a[8:11], v[16:19], v[52:55], a[8:11]\n\t"
    "v_perm_b32 v41, v223, v219, s60\n\t"
    "v_mfma_f32_16x16x32_bf16 a[40:43], v[20:23], v[52:55], a[40:43]\n\t"
    "s_add_u32 m0, s68, 64512\n\t"
    "s_add_u32 s64, s85, 384\n\t"
    "v_xor_b32 v6, 96, %[vxo]\n\t"
    "v_mfma_f32_16x16x32_bf16 a[44:47], v[20:23], v[56:59], a[44:47]\n\t"
    "v_perm_b32 v45, v223, v219, s61\n\t"
    "v_mfma_f32_16x16x32_bf16 a[12:15], v[16:19], v[56:59], a[12:15]\n\t"
    "buffer_load_dwordx4 v6, s[92:95], s64 offen lds\n\t"
    "v_mfma_f32_16x16x32_bf16 a[16:19], v[16:19], v[60:63], a[16:19]\n\t"
    "v_perm_b32 v42, v231, v227, s60\n\t"
    "v_mfma_f32_16x16x32_bf16 a[48:51], v[20:23], v[60:63], a[48:51]\n\t"
    "v_mfma_f32_16x16x32_bf16 a[52:55], v[20:23], v[64:67], a[52:55]\n\t"
    "v_perm_b32 v46, v231, v227, s61\n\t"
    "v_mfma_f32_16x16x32_bf16 a[20:23], v[16:19], v[64:67], a[20:23]\n\t"
    "v_mfma_f32_16x16x32_bf16 a[24:27], v[16:19], v[68:71], a[24:27]\n\t"
    "v_perm_b32 v43, v239, v235, s60\n\t"
    "v_mfma_f32_16x16x32_bf16 a[56:59], v[20:23], v[68:71], a[56:59]\n\t"
    "v_mfma_f32_16x16x32_bf16 a[60:63], v[20:23], v[72:75], a[60:63]\n\t"
    "v_perm_b32 v47, v239, v235, s61\n\t"
    "v_mfma_f32_16x16x32_bf16 a[28:31], v[16:19], v[72:75], a[28:31]\n\t"
    "s_waitcnt vmcnt(19)\n\t"
    "s_add_u32 s62, %[tb], 0x18000\n\t"
    "v_mfma_f32_16x16x32_bf16 a[92:95], v[24:27], v[72:75], a[92:95]\n\t"
    "v_perm_b32 v16, v116, v112, s60\n\t"
    "v_mfma_f32_16x16x32_bf16 a[124:127], v[28:31], v[72:75], a[124:127]\n\t"
    "buffer_load_dwordx4 v[208:211], v4, s[96:99], s62 offen\n\t"
    "v_mfma_f32_16x16x32_bf16 a[120:123], v[28:31], v[68:71], a[120:123]\n\t"
    "v_perm_b32 v20, v116, v112, s61\n\t"
    "v_mfma_f32_16x16x32_bf16 a[88:91], v[24:27], v[68:71], a[88:91]\n\t"
    "v_mfma_f32_16x16x32_bf16 a[84:87], v[24:27], v[64:67], a[84:87]\n\t"
    "v_perm_b32 v17, v124, v120, s60\n\t"
    "v_mfma_f32_16x16x32_bf16 a[116:119], v[28:31], v[64:67], a[116:119]\n\t"
    "v_mfma_f32_16x16x32_bf16 a[112:115], v[28:31], v[60:63], a[112:115]\n\t"
    "v_perm_b32 v21, v124, v120, s61\n\t"
    "v_mfma_f32_16x16x32_bf16 a[80:83], v[24:27], v[60:63], a[80:83]\n\t"
    "buffer_load_dwordx4 v[212:215], v4, s[96:99], s62 offen offset:1024\n\t"
    "v_mfma_f32_16x16x32_bf16 a[76:79], v[24:27], v[56:59], a[76:79]\n\t"
    "v_perm_b32 v18, v132, v128, s60\n\t"
    "v_mfma_f32_16x16x32_bf16 a[108:111], v[28:31], v[56:59], a[108:111]\n\t"
    "v_mfma_f32_16x16x32_bf16 a[104:107], v[28:31], v[52:55], a[104:107]\n\t"
    "v_perm_b32 v22, v132, v128, s61\n\t"
    "v_mfma_f32_16x16x32_bf16 a[72:75], v[24:27], v[52:55], a[72:75]\n\t"
    "v_mfma_f32_16x16x32_bf16 a[68:71], v[24:27], v[48:51], a[68:71]\n\t"
    "v_perm_b32 v19, v140, v136, s60\n\t"
    "v_mfma_f32_16x16x32_bf16 a[100:103], v[28:31], v[48:51], a[100:103]\n\t"
    "buffer_load_dwordx4 v[216:219], v4, s[96:99], s62 offen offset:2048\n\t"
    "v_mfma_f32_16x16x32_bf16 a[96:99], v[28:31], v[12:15], a[96:99]\n\t"
    "v_perm_b32 v23, v140, v136, s61\n\t"
    "v_mfma_f32_16x16x32_bf16 a[64:67], v[24:27], v[12:15], a[64:67]\n\t"
    "v_mfma_f32_16x16x32_bf16 a[128:131], v[32:35], v[12:15], a[128:131]\n\t"
    "v_perm_b32 v24, v117, v113, s60\n\t"
    "v_mfma_f32_16x16x32_bf16 a[160:163], v[36:39], v[12:15], a[160:163]\n\t"
    "v_mfma_f32_16x16x32_bf16 a[164:167], v[36:39], v[48:51], a[164:167]\n\t"
    "v_perm_b32 v28, v117, v113, s61\n\t"
    "v_mfma_f32_16x16x32_bf16 a[132:135], v[32:35], v[48:51], a[132:135]\n\t"
    "buffer_load_dwordx4 v[220:223], v4, s[96:99], s62 offen offset:3072\n\t"
    "v_mfma_f32_16x16x32_bf16 a[136:139], v[32:35], v[52:55], a[136:139]\n\t"
    "v_perm_b32 v25, v125, v121, s60\n\t"
    "v_mfma_f32_16x16x32_bf16 a[168:171], v[36:39], v[52:55], a[168:171]\n\t"
    "v_mfma_f32_16x16x32_bf16 a[172:175], v[36:39], v[56:59], a[172:175]\n\t"
    "v_perm_b32 v29, v125, v121, s61\n\t"
    "v_mfma_f32_16x16x32_bf16 a[140:143], v[32:35], v[56:59], a[140:143]\n\t"
    "v_mfma_f32_16x16x32_bf16 a[144:147], v[32:35], v[60:63], a[144:147]\n\t"
    "v_perm_b32 v26, v133, v129, s60\n\t"
    "v_mfma_f32_16x16x32_bf16 a[176:179], v[36:39], v[60:63], a[176:179]\n\t"
    "buffer_load_dwordx4 v[224:227], v5, s[96:99], s62 offen\n\t"
    "v_mfma_f32_16x16x32_bf16 a[180:183], v[36:39], v[64:67], a[180:183]\n\t"
    "v_perm_b32 v30, v133, v129, s61\n\t"
    "v_mfma_f32_16x16x32_bf16 a[148:151], v[32:35], v[64:67], a[148:151]\n\t"
    "v_mfma_f32_16x16x32_bf16 a[152:155], v[32:35], v[68:71], a[152:155]\n\t"
    "v_perm_b32 v27, v141, v137, s60\n\t"
    "v_mfma_f32_16x16x32_bf16 a[184:187], v[36:39], v[68:71], a[184:187]\n\t"
    "v_mfma_f32_16x16x32_bf16 a[188:191], v[36:39], v[72:75], a[188:191]\n\t"
    "v_perm_b32 v31, v141, v137, s61\n\t"
    "v_mfma_f32_16x16x32_bf16 a[156:159], v[32:35], v[72:75], a[156:159]\n\t"
    "buffer_load_dwordx4 v[228:231], v5, s[96:99], s62 offen offset:1024\n\t"
    "v_mfma_f32_16x16x32_bf16 a[220:223], v[40:43], v[72:75], a[220:223]\n\t"
    "v_perm_b32 v32, v118, v114, s60\n\t"
    "v_mfma_f32_16x16x32_bf16 a[252:255], v[44:47], v[72:75], a[252:255]\n\t"
    "v_mfma_f32_16x16x32_bf16 a[248:251], v[44:47], v[68:71], a[248:251]\n\t"
    "v_perm_b32 v36, v118, v114, s61\n\t"
    "v_mfma_f32_16x16x32_bf16 a[216:219], v[40:43], v[68:71], a[216:219]\n\t"
    "v_mfma_f32_16x16x32_bf16 a[212:215], v[40:43], v[64:67], a[212:215]\n\t"
    "v_perm_b32 v33, v126, v122, s60\n\t"
    "v_mfma_f32_16x16x32_bf16 a[244:247], v[44:47], v[64:67], a[244:247]\n\t"
    "buffer_load_dwordx4 v[232:235], v5, s[96:99], s62 offen offset:2048\n\t"
    "v_mfma_f32_16x16x32_bf16 a[240:243], v[44:47], v[60:63], a[240:243]\n\t"
    "v_perm_b32 v37, v126, v122, s61\n\t"
    "v_mfma_f32_16x16x32_bf16 a[208:211], v[40:43], v[60:63], a[208:211]\n\t"
    "v_mfma_f32_16x16x32_bf16 a[204:207], v[40:43], v[56:59], a[204:207]\n\t"
    "v_perm_b32 v34, v134, v130, s60\n\t"
    "v_mfma_f32_16x16x32_bf16 a[236:239], v[44:47], v[56:59], a[236:239]\n\t"
    "v_mfma_f32_16x16x32_bf16 a[232:235], v[44:47], v[52:55], a[232:235]\n\t"
    "v_perm_b32 v38, v134, v130, s61\n\t"
    "v_mfma_f32_16x16x32_bf16 a[200:203], v[40:43], v[52:55], a[200:203]\n\t"
    "buffer_load_dwordx4 v[236:239], v5, s[96:99], s62 offen offset:3072\n\t"
    "v_mfma_f32_16x16x32_bf16 a[196:199], v[40:43], v[48:51], a[196:199]\n\t"
    "v_perm_b32 v35, v142, v138, s60\n\t"
    "v_mfma_f32_16x16x32_bf16 a[228:231], v[44:47], v[48:51], a[228:231]\n\t"
    "v_mfma_f32_16x16x32_bf16 a[224:227], v[44:47], v[12:15], a[224:227]\n\t"
    "v_perm_b32 v39, v142, v138, s61\n\t"
    "v_mfma_f32_16x16x32_bf16 a[192:195], v[40:43], v[12:15], a[192:195]\n\t"
    "s_waitcnt lgkmcnt(0)\n\t"
    "v_mfma_f32_16x16x32_bf16 a[0:3], v[16:19], v[76:79], a[0:3]\n\t"
    "v_perm_b32 v40, v119, v115, s60\n\t"
    "v_mfma_f32_16x16x32_bf16 a[32:35], v[20:23], v[76:79], a[32:35]\n\t"
    "v_mfma_f32_16x16x32_bf16 a[36:39], v[20:23], v[12:15], a[36:39]\n\t"
    "v_perm_b32 v44, v119, v115, s61\n\t"
    "v_mfma_f32_16x16x32_bf16 a[4:7], v[16:19], v[12:15], a[4:7]\n\t"
    "ds_read_b128 v[72:75], v9 offset:17664\n\t"
    "v_mfma_f32_16x16x32_bf16 a[8:11], v[16:19], v[48:51], a[8:11]\n\t"
    "v_perm_b32 v41, v127, v123, s60\n\t"
    "v_mfma_f32_16x16x32_bf16 a[40:43], v[20:23], v[48:51], a[40:43]\n\t"
    "v_mfma_f32_16x16x32_bf16 a[44:47], v[20:23], v[52:55], a[44:47]\n\t"
    "v_perm_b32 v45, v127, v123, s61\n\t"
    "v_mfma_f32_16x16x32_bf16 a[12:15], v[16:19], v[52:55], a[12:15]\n\t"
    "v_mfma_f32_16x16x32_bf16 a[16:19], v[16:19], v[56:59], a[16:19]\n\t"
    "v_perm_b32 v42, v135, v131, s60\n\t"
    "v_mfma_f32_16x16x32_bf16 a[48:51], v[20:23], v[56:59], a[48:51]\n\t"
    "v_mfma_f32_16x16x32_bf16 a[52:55], v[20:23], v[60:63], a[52:55]\n\t"
    "v_perm_b32 v46, v135, v131, s61\n\t"
    "v_mfma_f32_16x16x32_bf16 a[20:23], v[16:19], v[60:63], a[20:23]\n\t"
    "v_mfma_f32_16x16x32_bf16 a[24:27], v[16:19], v[64:67], a[24:27]\n\t"
    "v_perm_b32 v43, v143, v139, s60\n\t"
    "v_mfma_f32_16x16x32_bf16 a[56:59], v[20:23], v[64:67], a[56:59]\n\t"
    "v_mfma_f32_16x16x32_bf16 a[60:63], v[20:23], v[68:71], a[60:63]\n\t"
    "v_perm_b32 v47, v143, v139, s61\n\t"
    "v_mfma_f32_16x16x32_bf16 a[28:31], v[16:19], v[68:71], a[28:31]\n\t"
    "s_waitcnt vmcnt(18)\n\t"
    "s_add_u32 s62, s63, 0x18000\n\t"
    "v_mfma_f32_16x16x32_bf16 a[92:95], v[24:27], v[68:71], a[92:95]\n\t"
    "v_perm_b32 v16, v148, v144, s60\n\t"
    "v_mfma_f32_16x16x32_bf16 a[124:127], v[28:31], v[68:71], a[124:127]\n\t"
    "buffer_load_dwordx4 v[112:115], v4, s[96:99], s62 offen\n\t"
    "v_mfma_f32_16x16x32_bf16 a[120:123], v[28:31], v[64:67], a[120:123]\n\t"
    "v_perm_b32 v20, v148, v144, s61\n\t"
    "v_mfma_f32_16x16x32_bf16 a[88:91], v[24:27], v[64:67], a[88:91]\n\t"
    "v_mfma_f32_16x16x32_bf16 a[84:87], v[24:27], v[60:63], a[84:87]\n\t"
    "v_perm_b32 v17, v156, v152, s60\n\t"
    "v_mfma_f32_16x16x32_bf16 a[116:119], v[28:31], v[60:63], a[116:119]\n\t"
    "v_mfma_f32_16x16x32_bf16 a[112:115], v[28:31], v[56:59], a[112:115]\n\t"
    "v_perm_b32 v21, v156, v152, s61\n\t"
    "v_mfma_f32_16x16x32_bf16 a[80:83], v[24:27], v[56:59], a[80:83]\n\t"
    "buffer_load_dwordx4 v[116:119], v4, s[96:99], s62 offen offset:1024\n\t"
    "v_mfma_f32_16x16x32_bf16 a[76:79], v[24:27], v[52:55], a[76:79]\n\t"
    "v_perm_b32 v18, v164, v160, s60\n\t"
    "v_mfma_f32_16x16x32_bf16 a[108:111], v[28:31], v[52:55], a[108:111]\n\t"
    "v_mfma_f32_16x16x32_bf16 a[104:107], v[28:31], v[48:51], a[104:107]\n\t"
    "v_perm_b32 v22, v164, v160, s61\n\t"
    "v_mfma_f32_16x16x32_bf16 a[72:75], v[24:27], v[48:51], a[72:75]\n\t"
    "v_mfma_f32_16x16x32_bf16 a[68:71], v[24:27], v[12:15], a[68:71]\n\t"
    "v_perm_b32 v19, v172, v168, s60\n\t"
    "v_mfma_f32_16x16x32_bf16 a[100:103], v[28:31], v[12:15], a[100:103]\n\t"
    "buffer_load_dwordx4 v[120:123], v4, s[96:99], s62 offen offset:2048\n\t"
    "v_mfma_f32_16x16x32_bf16 a[96:99], v[28:31], v[76:79], a[96:99]\n\t"
    "v_perm_b32 v23, v172, v168, s61\n\t"
    "v_mfma_f32_16x16x32_bf16 a[64:67], v[24:27], v[76:79], a[64:67]\n\t"
    "v_mfma_f32_16x16x32_bf16 a[128:131], v[32:35], v[76:79], a[128:131]\n\t"
    "v_perm_b32 v24, v149, v145, s60\n\t"
    "v_mfma_f32_16x16x32_bf16 a[160:163], v[36:39], v[76:79], a[160:163]\n\t"
    "v_mfma_f32_16x16x32_bf16 a[164:167], v[36:39], v[12:15], a[164:167]\n\t"
    "v_perm_b32 v28, v149, v145, s61\n\t"
    "v_mfma_f32_16x16x32_bf16 a[132:135], v[32:35], v[12:15], a[132:135]\n\t"
    "buffer_load_dwordx4 v[124:127], v4, s[96:99], s62 offen offset:3072\n\t"
    "v_mfma_f32_16x16x32_bf16 a[136:139], v[32:35], v[48:51], a[136:139]\n\t"
    "v_perm_b32 v25, v157, v153, s60\n\t"
    "v_mfma_f32_16x16x32_bf16 a[168:171], v[36:39], v[48:51], a[168:171]\n\t"
    "v_mfma_f32_16x16x32_bf16 a[172:175], v[36:39], v[52:55], a[172:175]\n\t"
    "v_perm_b32 v29, v157, v153, s61\n\t"
    "v_mfma_f32_16x16x32_bf16 a[140:143], v[32:35], v[52:55], a[140:143]\n\t"
    "v_mfma_f32_16x16x32_bf16 a[144:147], v[32:35], v[56:59], a[144:147]\n\t"
    "v_perm_b32 v26, v165, v161, s60\n\t"
    "v_mfma_f32_16x16x32_bf16 a[176:179], v[36:39], v[56:59], a[176:179]\n\t"
    "buffer_load_dwordx4 v[128:131], v5, s[96:99], s62 offen\n\t"
    "v_mfma_f32_16x16x32_bf16 a[180:183], v[36:39], v[60:63], a[180:183]\n\t"
    "v_perm_b32 v30, v165, v161, s61\n\t"
    "v_mfma_f32_16x16x32_bf16 a[148:151], v[32:35], v[60:63], a[148:151]\n\t"
    "v_mfma_f32_16x16x32_bf16 a[152:155], v[32:35], v[64:67], a[152:155]\n\t"
    "v_perm_b32 v27, v173, v169, s60\n\t"
    "v_mfma_f32_16x16x32_bf16 a[184:187], v[36:39], v[64:67], a[184:187]\n\t"
    "v_mfma_f32_16x16x32_bf16 a[188:191], v[36:39], v[68:71], a[188:191]\n\t"
    "v_perm_b32 v31, v173, v169, s61\n\t"
    "v_mfma_f32_16x16x32_bf16 a[156:159], v[32:35], v[68:71], a[156:159]\n\t"
    "buffer_load_dwordx4 v[132:135], v5, s[96:99], s62 offen offset:1024\n\t"
    "v_mfma_f32_16x16x32_bf16 a[220:223], v[40:43], v[68:71], a[220:223]\n\t"
    "v_perm_b32 v32, v150, v146, s60\n\t"
    "v_mfma_f32_16x16x32_bf16 a[252:255], v[44:47], v[68:71], a[252:255]\n\t"
    "v_mfma_f32_16x16x32_bf16 a[248:251], v[44:47], v[64:67], a[248:251]\n\t"
    "v_perm_b32 v36, v150, v146, s61\n\t"
    "v_mfma_f32_16x16x32_bf16 a[216:219], v[40:43], v[64:67], a[216:219]\n\t"
    "v_mfma_f32_16x16x32_bf16 a[212:215], v[40:43], v[60:63], a[212:215]\n\t"
    "v_perm_b32 v33, v158, v154, s60\n\t"
    "v_mfma_f32_16x16x32_bf16 a[244:247], v[44:47], v[60:63], a[244:247]\n\t"
    "buffer_load_dwordx4 v[136:139], v5, s[96:99], s62 offen offset:2048\n\t"
    "v_mfma_f32_16x16x32_bf16 a[240:243], v[44:47], v[56:59], a[240:243]\n\t"
    "v_perm_b32 v37, v158, v154, s61\n\t"
    "v_mfma_f32_16x16x32_bf16 a[208:211], v[40:43], v[56:59], a[208:211]\n\t"
    "v_mfma_f32_16x16x32_bf16 a[204:207], v[40:43], v[52:55], a[204:207]\n\t"
    "v_perm_b32 v34, v166, v162, s60\n\t"
    "v_mfma_f32_16x16x32_bf16 a[236:239], v[44:47], v[52:55], a[236:239]\n\t"
    "v_mfma_f32_16x16x32_bf16 a[232:235], v[44:47], v[48:51], a[232:235]\n\t"
    "v_perm_b32 v38, v166, v162, s61\n\t"
    "v_mfma_f32_16x16x32_bf16 a[200:203], v[40:43], v[48:51], a[200:203]\n\t"
    "buffer_load_dwordx4 v[140:143], v5, s[96:99], s62 offen offset:3072\n\t"
    "v_mfma_f32_16x16x32_bf16 a[196:199], v[40:43], v[12:15], a[196:199]\n\t"
    "v_perm_b32 v35, v174, v170, s60\n\t"
    "v_mfma_f32_16x16x32_bf16 a[228:231], v[44:47], v[12:15], a[228:231]\n\t"
    "v_mfma_f32_16x16x32_bf16 a[224:227], v[44:47], v[76:79], a[224:227]\n\t"
    "v_perm_b32 v39, v174, v170, s61\n\t"
    "v_mfma_f32_16x16x32_bf16 a[192:195], v[40:43], v[76:79], a[192:195]\n\t"
    "s_waitcnt lgkmcnt(0)\n\t"
    "v_mfma_f32_16x16x32_bf16 a[0:3], v[16:19], v[72:75], a[0:3]\n\t"
    "v_perm_b32 v40, v151, v147, s60\n\t"
    "v_mfma_f32_16x16x32_bf16 a[32:35], v[20:23], v[72:75], a[32:35]\n\t"
    "v_mfma_f32_16x16x32_bf16 a[36:39], v[20:23], v[76:79], a[36:39]\n\t"
    "v_perm_b32 v44, v151, v147, s61\n\t"
    "v_mfma_f32_16x16x32_bf16 a[4:7], v[16:19], v[76:79], a[4:7]\n\t"
    "ds_read_b128 v[68:71], v9 offset:17408\n\t"
    "v_mfma_f32_16x16x32_bf16 a[8:11], v[16:19], v[12:15], a[8:11]\n\t"
    "v_perm_b32 v41, v159, v155, s60\n\t"
    "v_mfma_f32_16x16x32_bf16 a[40:43], v[20:23], v[12:15], a[40:43]\n\t"
    "v_mfma_f32_16x16x32_bf16 a[44:47], v[20:23], v[48:51], a[44:47]\n\t"
    "v_perm_b32 v45, v159, v155, s61\n\t"
    "v_mfma_f32_16x16x32_bf16 a[12:15], v[16:19], v[48:51], a[12:15]\n\t"
    "v_mfma_f32_16x16x32_bf16 a[16:19], v[16:19], v[52:55], a[16:19]\n\t"
    "v_perm_b32 v42, v167, v163, s60\n\t"
    "v_mfma_f32_16x16x32_bf16 a[48:51], v[20:23], v[52:55], a[48:51]\n\t"
    "v_mfma_f32_16x16x32_bf16 a[52:55], v[20:23], v[56:59], a[52:55]\n\t"
    "v_perm_b32 v46, v167, v163, s61\n\t"
    "v_mfma_f32_16x16x32_bf16 a[20:23], v[16:19], v[56:59], a[20:23]\n\t"
    "ds_read_b128 v[80:83], v10 offset:18432\n\t"
    "v_mfma_f32_16x16x32_bf16 a[24:27], v[16:19], v[60:63], a[24:27]\n\t"
    "v_perm_b32 v43, v175, v171, s60\n\t"
    "v_mfma_f32_16x16x32_bf16 a[56:59], v[20:23], v[60:63], a[56:59]\n\t"
    "v_mfma_f32_16x16x32_bf16 a[60:63], v[20:23], v[64:67], a[60:63]\n\t"
    "v_perm_b32 v47, v175, v171, s61\n\t"
    "v_mfma_f32_16x16x32_bf16 a[28:31], v[16:19], v[64:67], a[28:31]\n\t"
    "s_waitcnt vmcnt(17)\n\t"
    "s_mov_b32 s62, 0x98000\n\t"
    "v_mfma_f32_16x16x32_bf16 a[92:95], v[24:27], v[64:67], a[92:95]\n\t"
    "v_perm_b32 v16, v180, v176, s60\n\t"
    "v_mfma_f32_16x16x32_bf16 a[124:127], v[28:31], v[64:67], a[124:127]\n\t"
    "buffer_load_dwordx4 v[144:147], v4, s[96:99], s62 offen\n\t"
    "v_mfma_f32_16x16x32_bf16 a[120:123], v[28:31], v[60:63], a[120:123]\n\t"
    "v_perm_b32 v20, v180, v176, s61\n\t"
    "v_mfma_f32_16x16x32_bf16 a[88:91], v[24:27], v[60:63], a[88:91]\n\t"
    "ds_read_b128 v[84:87], v10 offset:18688\n\t"
    "v_mfma_f32_16x16x32_bf16 a[84:87], v[24:27], v[56:59], a[84:87]\n\t"
    "v_perm_b32 v17, v188, v184, s60\n\t"
    "v_mfma_f32_16x16x32_bf16 a[116:119], v[28:31], v[56:59], a[116:119]\n\t"
    "v_mfma_f32_16x16x32_bf16 a[112:115], v[28:31], v[52:55], a[112:115]\n\t"
    "v_perm_b32 v21, v188, v184, s61\n\t"
    "v_mfma_f32_16x16x32_bf16 a[80:83], v[24:27], v[52:55], a[80:83]\n\t"
    "buffer_load_dwordx4 v[148:151], v4, s[96:99], s62 offen offset:1024\n\t"
    "v_mfma_f32_16x16x32_bf16 a[76:79], v[24:27], v[48:51], a[76:79]\n\t"
    "v_perm_b32 v18, v196, v192, s60\n\t"
    "v_mfma_f32_16x16x32_bf16 a[108:111], v[28:31], v[48:51], a[108:111]\n\t"
    "v_mfma_f32_16x16x32_bf16 a[104:107], v[28:31], v[12:15], a[104:107]\n\t"
    "v_perm_b32 v22, v196, v192, s61\n\t"
    "v_mfma_f32_16x16x32_bf16 a[72:75], v[24:27], v[12:15], a[72:75]\n\t"
    "ds_read_b128 v[88:91], v10 offset:18944\n\t"
    "v_mfma_f32_16x16x32_bf16 a[68:71], v[24:27], v[76:79], a[68:71]\n\t"
    "v_perm_b32 v19, v204, v200, s60\n\t"
    "v_mfma_f32_16x16x32_bf16 a[100:103], v[28:31], v[76:79], a[100:103]\n\t"
    "buffer_load_dwordx4 v[152:155], v4, s[96:99], s62 offen offset:2048\n\t"
    "v_mfma_f32_16x16x32_bf16 a[96:99], v[28:31], v[72:75], a[96:99]\n\t"
    "v_perm_b32 v23, v204, v200, s61\n\t"
    "v_mfma_f32_16x16x32_bf16 a[64:67], v[24:27], v[72:75], a[64:67]\n\t"
    "v_mfma_f32_16x16x32_bf16 a[128:131], v[32:35], v[72:75], a[128:131]\n\t"
    "v_perm_b32 v24, v181, v177, s60\n\t"
    "v_mfma_f32_16x16x32_bf16 a[160:163], v[36:39], v[72:75], a[160:163]\n\t"
    "v_mfma_f32_16x16x32_bf16 a[164:167], v[36:39], v[76:79], a[164:167]\n\t"
    "v_perm_b32 v28, v181, v177, s61\n\t"
    "v_mfma_f32_16x16x32_bf16 a[132:135], v[32:35], v[76:79], a[132:135]\n\t"
    "buffer_load_dwordx4 v[156:159], v4, s[96:99], s62 offen offset:3072\n\t"
    "ds_read_b128 v[92:95], v10 offset:19200\n\t"
    "v_mfma_f32_16x16x32_bf16 a[136:139], v[32:35], v[12:15], a[136:139]\n\t"
    "v_perm_b32 v25, v189, v185, s60\n\t"
    "v_mfma_f32_16x16x32_bf16 a[168:171], v[36:39], v[12:15], a[168:171]\n\t"
    "v_mfma_f32_16x16x32_bf16 a[172:175], v[36:39], v[48:51], a[172:175]\n\t"
    "v_perm_b32 v29, v189, v185, s61\n\t"
    "v_mfma_f32_16x16x32_bf16 a[140:143], v[32:35], v[48:51], a[140:143]\n\t"
    "v_mfma_f32_16x16x32_bf16 a[144:147], v[32:35], v[52:55], a[144:147]\n\t"
    "v_perm_b32 v26, v197, v193, s60\n\t"
    "v_mfma_f32_16x16x32_bf16 a[176:179], v[36:39], v[52:55], a[176:179]\n\t"
    "buffer_load_dwordx4 v[160:163], v5, s[96:99], s62 offen\n\t"
    "v_mfma_f32_16x16x32_bf16 a[180:183], v[36:39], v[56:59], a[180:183]\n\t"
    "v_perm_b32 v30, v197, v193, s61\n\t"
    "v_mfma_f32_16x16x32_bf16 a[148:151], v[32:35], v[56:59], a[148:151]\n\t"
    "v_mfma_f32_16x16x32_bf16 a[152:155], v[32:35], v[60:63], a[152:155]\n\t"
    "v_perm_b32 v27, v205, v201, s60\n\t"
    "v_mfma_f32_16x16x32_bf16 a[184:187], v[36:39], v[60:63], a[184:187]\n\t"
    "v_mfma_f32_16x16x32_bf16 a[188:191], v[36:39], v[64:67], a[188:191]\n\t"
    "v_perm_b32 v31, v205, v201, s61\n\t"
    "v_mfma_f32_16x16x32_bf16 a[156:159], v[32:35], v[64:67], a[156:159]\n\t"
    "buffer_load_dwordx4 v[164:167], v5, s[96:99], s62 offen offset:1024\n\t"
    "v_mfma_f32_16x16x32_bf16 a[220:223], v[40:43], v[64:67], a[220:223]\n\t"
    "v_perm_b32 v32, v182, v178, s60\n\t"
    "v_mfma_f32_16x16x32_bf16 a[252:255], v[44:47], v[64:67], a[252:255]\n\t"
    "v_mfma_f32_16x16x32_bf16 a[248:251], v[44:47], v[60:63], a[248:251]\n\t"
    "v_perm_b32 v36, v182, v178, s61\n\t"
    "v_mfma_f32_16x16x32_bf16 a[216:219], v[40:43], v[60:63], a[216:219]\n\t"
    "v_mfma_f32_16x16x32_bf16 a[212:215], v[40:43], v[56:59], a[212:215]\n\t"
    "v_perm_b32 v33, v190, v186, s60\n\t"
    "v_mfma_f32_16x16x32_bf16 a[244:247], v[44:47], v[56:59], a[244:247]\n\t"
    "buffer_load_dwordx4 v[168:171], v5, s[96:99], s62 offen offset:2048\n\t"
    "v_mfma_f32_16x16x32_bf16 a[240:243], v[44:47], v[52:55], a[240:243]\n\t"
    "v_perm_b32 v37, v190, v186, s61\n\t"
    "v_mfma_f32_16x16x32_bf16 a[208:211], v[40:43], v[52:55], a[208:211]\n\t"
    "v_mfma_f32_16x16x32_bf16 a[204:207], v[40:43], v[48:51], a[204:207]\n\t"
    "v_perm_b32 v34, v198, v194, s60\n\t"
    "v_mfma_f32_16x16x32_bf16 a[236:239], v[44:47], v[48:51], a[236:239]\n\t"
    "v_mfma_f32_16x16x32_bf16 a[232:235], v[44:47], v[12:15], a[232:235]\n\t"
    "v_perm_b32 v38, v198, v194, s61\n\t"
    "v_mfma_f32_16x16x32_bf16 a[200:203], v[40:43], v[12:15], a[200:203]\n\t"
    "buffer_load_dwordx4 v[172:175], v5, s[96:99], s62 offen offset:3072\n\t"
    "v_mfma_f32_16x16x32_bf16 a[196:199], v[40:43], v[76:79], a[196:199]\n\t"
    "v_perm_b32 v35, v206, v202, s60\n\t"
    "v_mfma_f32_16x16x32_bf16 a[228:231], v[44:47], v[76:79], a[228:231]\n\t"
    "v_mfma_f32_16x16x32_bf16 a[224:227], v[44:47], v[72:75], a[224:227]\n\t"
    "v_perm_b32 v39, v206, v202, s61\n\t"
    "v_mfma_f32_16x16x32_bf16 a[192:195], v[40:43], v[72:75], a[192:195]\n\t"
    "s_waitcnt lgkmcnt(0)\n\t"
    "v_mfma_f32_16x16x32_bf16 a[0:3], v[16:19], v[68:71], a[0:3]\n\t"
    "v_perm_b32 v40, v183, v179, s60\n\t"
    "v_mfma_f32_16x16x32_bf16 a[32:35], v[20:23], v[68:71], a[32:35]\n\t"
    "v_mfma_f32_16x16x32_bf16 a[36:39], v[20:23], v[72:75], a[36:39]\n\t"
    "v_perm_b32 v44, v183, v179, s61\n\t"
    "v_mfma_f32_16x16x32_bf16 a[4:7], v[16:19], v[72:75], a[4:7]\n\t"
    "v_mfma_f32_16x16x32_bf16 a[8:11], v[16:19], v[76:79], a[8:11]\n\t"
    "v_perm_b32 v41, v191, v187, s60\n\t"
    "v_mfma_f32_16x16x32_bf16 a[40:43], v[20:23], v[76:79], a[40:43]\n\t"
    "v_mfma_f32_16x16x32_bf16 a[44:47], v[20:23], v[12:15], a[44:47]\n\t"
    "v_perm_b32 v45, v191, v187, s61\n\t"
    "v_mfma_f32_16x16x32_bf16 a[12:15], v[16:19], v[12:15], a[12:15]\n\t"
    "v_mfma_f32_16x16x32_bf16 a[16:19], v[16:19], v[48:51], a[16:19]\n\t"
    "v_perm_b32 v42, v199, v195, s60\n\t"
    "v_mfma_f32_16x16x32_bf16 a[48:51], v[20:23], v[48:51], a[48:51]\n\t"
    "v_mfma_f32_16x16x32_bf16 a[52:55], v[20:23], v[52:55], a[52:55]\n\t"
    "v_perm_b32 v46, v199, v195, s61\n\t"
    "v_mfma_f32_16x16x32_bf16 a[20:23], v[16:19], v[52:55], a[20:23]\n\t"
    "ds_read_b128 v[96:99], v10 offset:19456\n\t"
    "v_mfma_f32_16x16x32_bf16 a[24:27], v[16:19], v[56:59], a[24:27]\n\t"
    "v_perm_b32 v43, v207, v203, s60\n\t"
    "v_mfma_f32_16x16x32_bf16 a[56:59], v[20:23], v[56:59], a[56:59]\n\t"
    "v_mfma_f32_16x16x32_bf16 a[60:63], v[20:23], v[60:63], a[60:63]\n\t"
    "v_perm_b32 v47, v207, v203, s61\n\t"
    "v_mfma_f32_16x16x32_bf16 a[28:31], v[16:19], v[60:63], a[28:31]\n\t"
    "s_waitcnt vmcnt(16)\n\t"
    "s_add_u32 s62, s70, 0x18000\n\t"
    "v_mfma_f32_16x16x32_bf16 a[92:95], v[24:27], v[60:63], a[92:95]\n\t"
    "v_perm_b32 v16, v212, v208, s60\n\t"
    "v_mfma_f32_16x16x32_bf16 a[124:127], v[28:31], v[60:63], a[124:127]\n\t"
    "buffer_load_dwordx4 v[176:179], v4, s[96:99], s62 offen\n\t"
    "v_mfma_f32_16x16x32_bf16 a[120:123], v[28:31], v[56:59], a[120:123]\n\t"
    "v_perm_b32 v20, v212, v208, s61\n\t"
    "v_mfma_f32_16x16x32_bf16 a[88:91], v[24:27], v[56:59], a[88:91]\n\t"
    "ds_read_b128 v[100:103], v10 offset:19712\n\t"
    "v_mfma_f32_16x16x32_bf16 a[84:87], v[24:27], v[52:55], a[84:87]\n\t"
    "v_perm_b32 v17, v220, v216, s60\n\t"
    "v_mfma_f32_16x16x32_bf16 a[116:119], v[28:31], v[52:55], a[116:119]\n\t"
    "v_mfma_f32_16x16x32_bf16 a[112:115], v[28:31], v[48:51], a[112:115]\n\t"
    "v_perm_b32 v21, v220, v216, s61\n\t"
    "v_mfma_f32_16x16x32_bf16 a[80:83], v[24:27], v[48:51], a[80:83]\n\t"
    "buffer_load_dwordx4 v[180:183], v4, s[96:99], s62 offen offset:1024\n\t"
    "v_mfma_f32_16x16x32_bf16 a[76:79], v[24:27], v[12:15], a[76:79]\n\t"
    "v_perm_b32 v18, v228, v224, s60\n\t"
    "v_mfma_f32_16x16x32_bf16 a[108:111], v[28:31], v[12:15], a[108:111]\n\t"
    "v_mfma_f32_16x16x32_bf16 a[104:107], v[28:31], v[76:79], a[104:107]\n\t"
    "v_perm_b32 v22, v228, v224, s61\n\t"
    "v_mfma_f32_16x16x32_bf16 a[72:75], v[24:27], v[76:79], a[72:75]\n\t"
    "ds_read_b128 v[104:107], v10 offset:19968\n\t"
    "v_mfma_f32_16x16x32_bf16 a[68:71], v[24:27], v[72:75], a[68:71]\n\t"
    "v_perm_b32 v19, v236, v232, s60\n\t"
    "v_mfma_f32_16x16x32_bf16 a[100:103], v[28:31], v[72:75], a[100:103]\n\t"
    "buffer_load_dwordx4 v[184:187], v4, s[96:99], s62 offen offset:2048\n\t"
    "v_mfma_f32_16x16x32_bf16 a[96:99], v[28:31], v[68:71], a[96:99]\n\t"
    "v_perm_b32 v23, v236, v232, s61\n\t"
    "v_mfma_f32_16x16x32_bf16 a[64:67], v[24:27], v[68:71], a[64:67]\n\t"
    "v_mfma_f32_16x16x32_bf16 a[128:131], v[32:35], v[68:71], a[128:131]\n\t"
    "v_perm_b32 v24, v213, v209, s60\n\t"
    "v_mfma_f32_16x16x32_bf16 a[160:163], v[36:39], v[68:71], a[160:163]\n\t"
    "v_mfma_f32_16x16x32_bf16 a[164:167], v[36:39], v[72:75], a[164:167]\n\t"
    "v_perm_b32 v28, v213, v209, s61\n\t"
    "v_mfma_f32_16x16x32_bf16 a[132:135], v[32:35], v[72:75], a[132:135]\n\t"
    "buffer_load_dwordx4 v[188:191], v4, s[96:99], s62 offen offset:3072\n\t"
    "ds_read_b128 v[108:111], v10 offset:20224\n\t"
    "v_mfma_f32_16x16x32_bf16 a[136:139], v[32:35], v[76:79], a[136:139]\n\t"
    "v_perm_b32 v25, v221, v217, s60\n\t"
    "v_mfma_f32_16x16x32_bf16 a[168:171], v[36:39], v[76:79], a[168:171]\n\t"
    "v_mfma_f32_16x16x32_bf16 a[172:175], v[36:39], v[12:15], a[172:175]\n\t"
    "v_perm_b32 v29, v221, v217, s61\n\t"
    "v_mfma_f32_16x16x32_bf16 a[140:143], v[32:35], v[12:15], a[140:143]\n\t"
    "v_mfma_f32_16x16x32_bf16 a[144:147], v[32:35], v[48:51], a[144:147]\n\t"
    "v_perm_b32 v26, v229, v225, s60\n\t"
    "v_mfma_f32_16x16x32_bf16 a[176:179], v[36:39], v[48:51], a[176:179]\n\t"
    "buffer_load_dwordx4 v[192:195], v5, s[96:99], s62 offen\n\t"
    "v_mfma_f32_16x16x32_bf16 a[180:183], v[36:39], v[52:55], a[180:183]\n\t"
    "v_perm_b32 v30, v229, v225, s61\n\t"
    "v_mfma_f32_16x16x32_bf16 a[148:151], v[32:35], v[52:55], a[148:151]\n\t"
    "v_mfma_f32_16x16x32_bf16 a[152:155], v[32:35], v[56:59], a[152:155]\n\t"
    "v_perm_b32 v27, v237, v233, s60\n\t"
    "v_mfma_f32_16x16x32_bf16 a[184:187], v[36:39], v[56:59], a[184:187]\n\t"
    "v_mfma_f32_16x16x32_bf16 a[188:191], v[36:39], v[60:63], a[188:191]\n\t"
    "v_perm_b32 v31, v237, v233, s61\n\t"
    "v_mfma_f32_16x16x32_bf16 a[156:159], v[32:35], v[60:63], a[156:159]\n\t"
    "buffer_load_dwordx4 v[196:199], v5, s[96:99], s62 offen offset:1024\n\t"
    "v_mfma_f32_16x16x32_bf16 a[220:223], v[40:43], v[60:63], a[220:223]\n\t"
    "v_perm_b32 v32, v214, v210, s60\n\t"
    "v_mfma_f32_16x16x32_bf16 a[252:255], v[44:47], v[60:63], a[252:255]\n\t"
    "v_mfma_f32_16x16x32_bf16 a[248:251], v[44:47], v[56:59], a[248:251]\n\t"
    "v_perm_b32 v36, v214, v210, s61\n\t"
    "v_mfma_f32_16x16x32_bf16 a[216:219], v[40:43], v[56:59], a[216:219]\n\t"
    "v_mfma_f32_16x16x32_bf16 a[212:215], v[40:43], v[52:55], a[212:215]\n\t"
    "v_perm_b32 v33, v222, v218, s60\n\t"
    "v_mfma_f32_16x16x32_bf16 a[244:247], v[44:47], v[52:55], a[244:247]\n\t"
    "buffer_load_dwordx4 v[200:203], v5, s[96:99], s62 offen offset:2048\n\t"
    "v_mfma_f32_16x16x32_bf16 a[240:243], v[44:47], v[48:51], a[240:243]\n\t"
    "v_perm_b32 v37, v222, v218, s61\n\t"
    "v_mfma_f32_16x16x32_bf16 a[208:211], v[40:43], v[48:51], a[208:211]\n\t"
    "v_mfma_f32_16x16x32_bf16 a[204:207], v[40:43], v[12:15], a[204:207]\n\t"
    "v_perm_b32 v34, v230, v226, s60\n\t"
    "v_mfma_f32_16x16x32_bf16 a[236:239], v[44:47], v[12:15], a[236:239]\n\t"
    "v_mfma_f32_16x16x32_bf16 a[232:235], v[44:47], v[76:79], a[232:235]\n\t"
    "v_perm_b32 v38, v230, v226, s61\n\t"
    "v_mfma_f32_16x16x32_bf16 a[200:203], v[40:43], v[76:79], a[200:203]\n\t"
    "buffer_load_dwordx4 v[204:207], v5, s[96:99], s62 offen offset:3072\n\t"
    "v_mfma_f32_16x16x32_bf16 a[196:199], v[40:43], v[72:75], a[196:199]\n\t"
    "v_perm_b32 v35, v238, v234, s60\n\t"
    "v_mfma_f32_16x16x32_bf16 a[228:231], v[44:47], v[72:75], a[228:231]\n\t"
    "v_mfma_f32_16x16x32_bf16 a[224:227], v[44:47], v[68:71], a[224:227]\n\t"
    "v_perm_b32 v39, v238, v234, s61\n\t"
    "v_mfma_f32_16x16x32_bf16 a[192:195], v[40:43], v[68:71], a[192:195]\n\t"
    "s_waitcnt lgkmcnt(0)\n\t"
    "v_mfma_f32_16x16x32_bf16 a[0:3], v[16:19], v[80:83], a[0:3]\n\t"
    "v_perm_b32 v40, v215, v211, s60\n\t"
    "v_mfma_f32_16x16x32_bf16 a[32:35], v[20:23], v[80:83], a[32:35]\n\t"
    "v_mfma_f32_16x16x32_bf16 a[36:39], v[20:23], v[84:87], a[36:39]\n\t"
    "v_perm_b32 v44, v215, v211, s61\n\t"
    "v_mfma_f32_16x16x32_bf16 a[4:7], v[16:19], v[84:87], a[4:7]\n\t"
    "ds_read_b128 v[12:15], v11 offset:18176\n\t"
    "v_mfma_f32_16x16x32_bf16 a[8:11], v[16:19], v[88:91], a[8:11]\n\t"
    "v_perm_b32 v41, v223, v219, s60\n\t"
    "v_mfma_f32_16x16x32_bf16 a[40:43], v[20:23], v[88:91], a[40:43]\n\t"
    "v_mfma_f32_16x16x32_bf16 a[44:47], v[20:23], v[92:95], a[44:47]\n\t"
    "v_perm_b32 v45, v223, v219, s61\n\t"
    "v_mfma_f32_16x16x32_bf16 a[12:15], v[16:19], v[92:95], a[12:15]\n\t"
    "v_mfma_f32_16x16x32_bf16 a[16:19], v[16:19], v[96:99], a[16:19]\n\t"
    "v_perm_b32 v42, v231, v227, s60\n\t"
    "v_mfma_f32_16x16x32_bf16 a[48:51], v[20:23], v[96:99], a[48:51]\n\t"
    "v_mfma_f32_16x16x32_bf16 a[52:55], v[20:23], v[100:103], a[52:55]\n\t"
    "v_perm_b32 v46, v231, v227, s61\n\t"
    "v_mfma_f32_16x16x32_bf16 a[20:23], v[16:19], v[100:103], a[20:23]\n\t"
    "v_mfma_f32_16x16x32_bf16 a[24:27], v[16:19], v[104:107], a[24:27]\n\t"
    "v_perm_b32 v43, v239, v235, s60\n\t"
    "v_mfma_f32_16x16x32_bf16 a[56:59], v[20:23], v[104:107], a[56:59]\n\t"
    "v_mfma_f32_16x16x32_bf16 a[60:63], v[20:23], v[108:111], a[60:63]\n\t"
    "v_perm_b32 v47, v239, v235, s61\n\t"
    "v_mfma_f32_16x16x32_bf16 a[28:31], v[16:19], v[108:111], a[28:31]\n\t"
    "s_waitcnt vmcnt(16)\n\t"
    "s_add_u32 s62, s72, 0x18000\n\t"
    "v_mfma_f32_16x16x32_bf16 a[92:95], v[24:27], v[108:111], a[92:95]\n\t"
    "v_perm_b32 v16, v116, v112, s60\n\t"
    "v_mfma_f32_16x16x32_bf16 a[124:127], v[28:31], v[108:111], a[124:127]\n\t"
    "buffer_load_dwordx4 v[208:211], v4, s[96:99], s62 offen\n\t"
    "v_mfma_f32_16x16x32_bf16 a[120:123], v[28:31], v[104:107], a[120:123]\n\t"
    "v_perm_b32 v20, v116, v112, s61\n\t"
    "v_mfma_f32_16x16x32_bf16 a[88:91], v[24:27], v[104:107], a[88:91]\n\t"
    "v_mfma_f32_16x16x32_bf16 a[84:87], v[24:27], v[100:103], a[84:87]\n\t"
    "v_perm_b32 v17, v124, v120, s60\n\t"
    "v_mfma_f32_16x16x32_bf16 a[116:119], v[28:31], v[100:103], a[116:119]\n\t"
    "v_mfma_f32_16x16x32_bf16 a[112:115], v[28:31], v[96:99], a[112:115]\n\t"
    "v_perm_b32 v21, v124, v120, s61\n\t"
    "v_mfma_f32_16x16x32_bf16 a[80:83], v[24:27], v[96:99], a[80:83]\n\t"
    "buffer_load_dwordx4 v[212:215], v4, s[96:99], s62 offen offset:1024\n\t"
    "v_mfma_f32_16x16x32_bf16 a[76:79], v[24:27], v[92:95], a[76:79]\n\t"
    "v_perm_b32 v18, v132, v128, s60\n\t"
    "v_mfma_f32_16x16x32_bf16 a[108:111], v[28:31], v[92:95], a[108:111]\n\t"
    "v_mfma_f32_16x16x32_bf16 a[104:107], v[28:31], v[88:91], a[104:107]\n\t"
    "v_perm_b32 v22, v132, v128, s61\n\t"
    "v_mfma_f32_16x16x32_bf16 a[72:75], v[24:27], v[88:91], a[72:75]\n\t"
    "v_mfma_f32_16x16x32_bf16 a[68:71], v[24:27], v[84:87], a[68:71]\n\t"
    "v_perm_b32 v19, v140, v136, s60\n\t"
    "v_mfma_f32_16x16x32_bf16 a[100:103], v[28:31], v[84:87], a[100:103]\n\t"
    "buffer_load_dwordx4 v[216:219], v4, s[96:99], s62 offen offset:2048\n\t"
    "v_mfma_f32_16x16x32_bf16 a[96:99], v[28:31], v[80:83], a[96:99]\n\t"
    "v_perm_b32 v23, v140, v136, s61\n\t"
    "v_mfma_f32_16x16x32_bf16 a[64:67], v[24:27], v[80:83], a[64:67]\n\t"
    "v_mfma_f32_16x16x32_bf16 a[128:131], v[32:35], v[80:83], a[128:131]\n\t"
    "v_perm_b32 v24, v117, v113, s60\n\t"
    "v_mfma_f32_16x16x32_bf16 a[160:163], v[36:39], v[80:83], a[160:163]\n\t"
    "v_mfma_f32_16x16x32_bf16 a[164:167], v[36:39], v[84:87], a[164:167]\n\t"
    "v_perm_b32 v28, v117, v113, s61\n\t"
    "v_mfma_f32_16x16x32_bf16 a[132:135], v[32:35], v[84:87], a[132:135]\n\t"
    "buffer_load_dwordx4 v[220:223], v4, s[96:99], s62 offen offset:3072\n\t"
    "v_mfma_f32_16x16x32_bf16 a[136:139], v[32:35], v[88:91], a[136:139]\n\t"
    "v_perm_b32 v25, v125, v121, s60\n\t"
    "v_mfma_f32_16x16x32_bf16 a[168:171], v[36:39], v[88:91], a[168:171]\n\t"
    "v_mfma_f32_16x16x32_bf16 a[172:175], v[36:39], v[92:95], a[172:175]\n\t"
    "v_perm_b32 v29, v125, v121, s61\n\t"
    "v_mfma_f32_16x16x32_bf16 a[140:143], v[32:35], v[92:95], a[140:143]\n\t"
    "v_mfma_f32_16x16x32_bf16 a[144:147], v[32:35], v[96:99], a[144:147]\n\t"
    "v_perm_b32 v26, v133, v129, s60\n\t"
    "v_mfma_f32_16x16x32_bf16 a[176:179], v[36:39], v[96:99], a[176:179]\n\t"
    "buffer_load_dwordx4 v[224:227], v5, s[96:99], s62 offen\n\t"
    "v_mfma_f32_16x16x32_bf16 a[180:183], v[36:39], v[100:103], a[180:183]\n\t"
    "v_perm_b32 v30, v133, v129, s61\n\t"
    "v_mfma_f32_16x16x32_bf16 a[148:151], v[32:35], v[100:103], a[148:151]\n\t"
    "v_mfma_f32_16x16x32_bf16 a[152:155], v[32:35], v[104:107], a[152:155]\n\t"
    "v_perm_b32 v27, v141, v137, s60\n\t"
    "v_mfma_f32_16x16x32_bf16 a[184:187], v[36:39], v[104:107], a[184:187]\n\t"
    "v_mfma_f32_16x16x32_bf16 a[188:191], v[36:39], v[108:111], a[188:191]\n\t"
    "v_perm_b32 v31, v141, v137, s61\n\t"
    "v_mfma_f32_16x16x32_bf16 a[156:159], v[32:35], v[108:111], a[156:159]\n\t"
    "buffer_load_dwordx4 v[228:231], v5, s[96:99], s62 offen offset:1024\n\t"
    "v_mfma_f32_16x16x32_bf16 a[220:223], v[40:43], v[108:111], a[220:223]\n\t"
    "v_perm_b32 v32, v118, v114, s60\n\t"
    "v_mfma_f32_16x16x32_bf16 a[252:255], v[44:47], v[108:111], a[252:255]\n\t"
    "v_mfma_f32_16x16x32_bf16 a[248:251], v[44:47], v[104:107], a[248:251]\n\t"
    "v_perm_b32 v36, v118, v114, s61\n\t"
    "v_mfma_f32_16x16x32_bf16 a[216:219], v[40:43], v[104:107], a[216:219]\n\t"
    "v_mfma_f32_16x16x32_bf16 a[212:215], v[40:43], v[100:103], a[212:215]\n\t"
    "v_perm_b32 v33, v126, v122, s60\n\t"
    "v_mfma_f32_16x16x32_bf16 a[244:247], v[44:47], v[100:103], a[244:247]\n\t"
    "buffer_load_dwordx4 v[232:235], v5, s[96:99], s62 offen offset:2048\n\t"
    "v_mfma_f32_16x16x32_bf16 a[240:243], v[44:47], v[96:99], a[240:243]\n\t"
    "v_perm_b32 v37, v126, v122, s61\n\t"
    "v_mfma_f32_16x16x32_bf16 a[208:211], v[40:43], v[96:99], a[208:211]\n\t"
    "v_mfma_f32_16x16x32_bf16 a[204:207], v[40:43], v[92:95], a[204:207]\n\t"
    "v_perm_b32 v34, v134, v130, s60\n\t"
    "v_mfma_f32_16x16x32_bf16 a[236:239], v[44:47], v[92:95], a[236:239]\n\t"
    "v_mfma_f32_16x16x32_bf16 a[232:235], v[44:47], v[88:91], a[232:235]\n\t"
    "v_perm_b32 v38, v134, v130, s61\n\t"
    "v_mfma_f32_16x16x32_bf16 a[200:203], v[40:43], v[88:91], a[200:203]\n\t"
    "buffer_load_dwordx4 v[236:239], v5, s[96:99], s62 offen offset:3072\n\t"
    "v_mfma_f32_16x16x32_bf16 a[196:199], v[40:43], v[84:87], a[196:199]\n\t"
    "v_perm_b32 v35, v142, v138, s60\n\t"
    "v_mfma_f32_16x16x32_bf16 a[228:231], v[44:47], v[84:87], a[228:231]\n\t"
    "v_mfma_f32_16x16x32_bf16 a[224:227], v[44:47], v[80:83], a[224:227]\n\t"
    "v_perm_b32 v39, v142, v138, s61\n\t"
    "v_mfma_f32_16x16x32_bf16 a[192:195], v[40:43], v[80:83], a[192:195]\n\t"
    "s_waitcnt lgkmcnt(0)\n\t"
    "s_waitcnt vmcnt(40)\n\t"
    "s_barrier\n\t"
    "v_mfma_f32_16x16x32_bf16 a[0:3], v[16:19], v[12:15], a[0:3]\n\t"
    "v_perm_b32 v40, v119, v115, s60\n\t"
    "v_mfma_f32_16x16x32_bf16 a[32:35], v[20:23], v[12:15], a[32:35]\n\t"
    "v_mfma_f32_16x16x32_bf16 a[36:39], v[20:23], v[80:83], a[36:39]\n\t"
    "v_perm_b32 v44, v119, v115, s61\n\t"
    "v_mfma_f32_16x16x32_bf16 a[4:7], v[16:19], v[80:83], a[4:7]\n\t"
    "ds_read_b128 v[108:111], v11 offset:17920\n\t"
    "v_mfma_f32_16x16x32_bf16 a[8:11], v[16:19], v[84:87], a[8:11]\n\t"
    "v_perm_b32 v41, v127, v123, s60\n\t"
    "v_mfma_f32_16x16x32_bf16 a[40:43], v[20:23], v[84:87], a[40:43]\n\t"
    "v_mfma_f32_16x16x32_bf16 a[44:47], v[20:23], v[88:91], a[44:47]\n\t"
    "v_perm_b32 v45, v127, v123, s61\n\t"
    "v_mfma_f32_16x16x32_bf16 a[12:15], v[16:19], v[88:91], a[12:15]\n\t"
    "v_mfma_f32_16x16x32_bf16 a[16:19], v[16:19], v[92:95], a[16:19]\n\t"
    "v_perm_b32 v42, v135, v131, s60\n\t"
    "v_mfma_f32_16x16x32_bf16 a[48:51], v[20:23], v[92:95], a[48:51]\n\t"
    "v_mfma_f32_16x16x32_bf16 a[52:55], v[20:23], v[96:99], a[52:55]\n\t"
    "v_perm_b32 v46, v135, v131, s61\n\t"
    "v_mfma_f32_16x16x32_bf16 a[20:23], v[16:19], v[96:99], a[20:23]\n\t"
    "v_mfma_f32_16x16x32_bf16 a[24:27], v[16:19], v[100:103], a[24:27]\n\t"
    "v_perm_b32 v43, v143, v139, s60\n\t"
    "v_mfma_f32_16x16x32_bf16 a[56:59], v[20:23], v[100:103], a[56:59]\n\t"
    "v_mfma_f32_16x16x32_bf16 a[60:63], v[20:23], v[104:107], a[60:63]\n\t"
    "v_perm_b32 v47, v143, v139, s61\n\t"
    "v_mfma_f32_16x16x32_bf16 a[28:31], v[16:19], v[104:107], a[28:31]\n\t"
    "s_waitcnt vmcnt(16)\n\t"
    "s_add_u32 s62, %[tb], 0x20000\n\t"
    "v_mfma_f32_16x16x32_bf16 a[92:95], v[24:27], v[104:107], a[92:95]\n\t"
    "v_perm_b32 v16, v148, v144, s60\n\t"
    "v_mfma_f32_16x16x32_bf16 a[124:127], v[28:31], v[104:107], a[124:127]\n\t"
    "buffer_load_dwordx4 v[112:115], v4, s[96:99], s62 offen\n\t"
    "v_mfma_f32_16x16x32_bf16 a[120:123], v[28:31], v[100:103], a[120:123]\n\t"
    "v_perm_b32 v20, v148, v144, s61\n\t"
    "v_mfma_f32_16x16x32_bf16 a[88:91], v[24:27], v[100:103], a[88:91]\n\t"
    "v_mfma_f32_16x16x32_bf16 a[84:87], v[24:27], v[96:99], a[84:87]\n\t"
    "v_perm_b32 v17, v156, v152, s60\n\t"
    "v_mfma_f32_16x16x32_bf16 a[116:119], v[28:31], v[96:99], a[116:119]\n\t"
    "v_mfma_f32_16x16x32_bf16 a[112:115], v[28:31], v[92:95], a[112:115]\n\t"
    "v_perm_b32 v21, v156, v152, s61\n\t"
    "v_mfma_f32_16x16x32_bf16 a[80:83], v[24:27], v[92:95], a[80:83]\n\t"
    "buffer_load_dwordx4 v[116:119], v4, s[96:99], s62 offen offset:1024\n\t"
    "v_mfma_f32_16x16x32_bf16 a[76:79], v[24:27], v[88:91], a[76:79]\n\t"
    "v_perm_b32 v18, v164, v160, s60\n\t"
    "v_mfma_f32_16x16x32_bf16 a[108:111], v[28:31], v[88:91], a[108:111]\n\t"
    "v_mfma_f32_16x16x32_bf16 a[104:107], v[28:31], v[84:87], a[104:107]\n\t"
    "v_perm_b32 v22, v164, v160, s61\n\t"
    "v_mfma_f32_16x16x32_bf16 a[72:75], v[24:27], v[84:87], a[72:75]\n\t"
    "v_mfma_f32_16x16x32_bf16 a[68:71], v[24:27], v[80:83], a[68:71]\n\t"
    "v_perm_b32 v19, v172, v168, s60\n\t"
    "v_mfma_f32_16x16x32_bf16 a[100:103], v[28:31], v[80:83], a[100:103]\n\t"
    "buffer_load_dwordx4 v[120:123], v4, s[96:99], s62 offen offset:2048\n\t"
    "v_mfma_f32_16x16x32_bf16 a[96:99], v[28:31], v[12:15], a[96:99]\n\t"
    "v_perm_b32 v23, v172, v168, s61\n\t"
    "v_mfma_f32_16x16x32_bf16 a[64:67], v[24:27], v[12:15], a[64:67]\n\t"
    "v_mfma_f32_16x16x32_bf16 a[128:131], v[32:35], v[12:15], a[128:131]\n\t"
    "v_perm_b32 v24, v149, v145, s60\n\t"
    "v_mfma_f32_16x16x32_bf16 a[160:163], v[36:39], v[12:15], a[160:163]\n\t"
    "v_mfma_f32_16x16x32_bf16 a[164:167], v[36:39], v[80:83], a[164:167]\n\t"
    "v_perm_b32 v28, v149, v145, s61\n\t"
    "v_mfma_f32_16x16x32_bf16 a[132:135], v[32:35], v[80:83], a[132:135]\n\t"
    "buffer_load_dwordx4 v[124:127], v4, s[96:99], s62 offen offset:3072\n\t"
    "v_mfma_f32_16x16x32_bf16 a[136:139], v[32:35], v[84:87], a[136:139]\n\t"
    "v_perm_b32 v25, v157, v153, s60\n\t"
    "v_mfma_f32_16x16x32_bf16 a[168:171], v[36:39], v[84:87], a[168:171]\n\t"
    "v_mfma_f32_16x16x32_bf16 a[172:175], v[36:39], v[88:91], a[172:175]\n\t"
    "v_perm_b32 v29, v157, v153, s61\n\t"
    "v_mfma_f32_16x16x32_bf16 a[140:143], v[32:35], v[88:91], a[140:143]\n\t"
    "v_mfma_f32_16x16x32_bf16 a[144:147], v[32:35], v[92:95], a[144:147]\n\t"
    "v_perm_b32 v26, v165, v161, s60\n\t"
    "v_mfma_f32_16x16x32_bf16 a[176:179], v[36:39], v[92:95], a[176:179]\n\t"
    "buffer_load_dwordx4 v[128:131], v5, s[96:99], s62 offen\n\t"
    "v_mfma_f32_16x16x32_bf16 a[180:183], v[36:39], v[96:99], a[180:183]\n\t"
    "v_perm_b32 v30, v165, v161, s61\n\t"
    "v_mfma_f32_16x16x32_bf16 a[148:151], v[32:35], v[96:99], a[148:151]\n\t"
    "v_mfma_f32_16x16x32_bf16 a[152:155], v[32:35], v[100:103], a[152:155]\n\t"
    "v_perm_b32 v27, v173, v169, s60\n\t"
    "v_mfma_f32_16x16x32_bf16 a[184:187], v[36:39], v[100:103], a[184:187]\n\t"
    "v_mfma_f32_16x16x32_bf16 a[188:191], v[36:39], v[104:107], a[188:191]\n\t"
    "v_perm_b32 v31, v173, v169, s61\n\t"
    "v_mfma_f32_16x16x32_bf16 a[156:159], v[32:35], v[104:107], a[156:159]\n\t"
    "buffer_load_dwordx4 v[132:135], v5, s[96:99], s62 offen offset:1024\n\t"
    "v_mfma_f32_16x16x32_bf16 a[220:223], v[40:43], v[104:107], a[220:223]\n\t"
    "v_perm_b32 v32, v150, v146, s60\n\t"
    "v_mfma_f32_16x16x32_bf16 a[252:255], v[44:47], v[104:107], a[252:255]\n\t"
    "v_mfma_f32_16x16x32_bf16 a[248:251], v[44:47], v[100:103], a[248:251]\n\t"
    "v_perm_b32 v36, v150, v146, s61\n\t"
    "v_mfma_f32_16x16x32_bf16 a[216:219], v[40:43], v[100:103], a[216:219]\n\t"
    "v_mfma_f32_16x16x32_bf16 a[212:215], v[40:43], v[96:99], a[212:215]\n\t"
    "v_perm_b32 v33, v158, v154, s60\n\t"
    "v_mfma_f32_16x16x32_bf16 a[244:247], v[44:47], v[96:99], a[244:247]\n\t"
    "buffer_load_dwordx4 v[136:139], v5, s[96:99], s62 offen offset:2048\n\t"
    "v_mfma_f32_16x16x32_bf16 a[240:243], v[44:47], v[92:95], a[240:243]\n\t"
    "v_perm_b32 v37, v158, v154, s61\n\t"
    "v_mfma_f32_16x16x32_bf16 a[208:211], v[40:43], v[92:95], a[208:211]\n\t"
    "v_mfma_f32_16x16x32_bf16 a[204:207], v[40:43], v[88:91], a[204:207]\n\t"
    "v_perm_b32 v34, v166, v162, s60\n\t"
    "v_mfma_f32_16x16x32_bf16 a[236:239], v[44:47], v[88:91], a[236:239]\n\t"
    "v_mfma_f32_16x16x32_bf16 a[232:235], v[44:47], v[84:87], a[232:235]\n\t"
    "v_perm_b32 v38, v166, v162, s61\n\t"
    "v_mfma_f32_16x16x32_bf16 a[200:203], v[40:43], v[84:87], a[200:203]\n\t"
    "buffer_load_dwordx4 v[140:143], v5, s[96:99], s62 offen offset:3072\n\t"
    "v_mfma_f32_16x16x32_bf16 a[196:199], v[40:43], v[80:83], a[196:199]\n\t"
    "v_perm_b32 v35, v174, v170, s60\n\t"
    "v_mfma_f32_16x16x32_bf16 a[228:231], v[44:47], v[80:83], a[228:231]\n\t"
    "v_mfma_f32_16x16x32_bf16 a[224:227], v[44:47], v[12:15], a[224:227]\n\t"
    "v_perm_b32 v39, v174, v170, s61\n\t"
    "v_mfma_f32_16x16x32_bf16 a[192:195], v[40:43], v[12:15], a[192:195]\n\t"
    "s_waitcnt lgkmcnt(0)\n\t"
    "v_mfma_f32_16x16x32_bf16 a[0:3], v[16:19], v[108:111], a[0:3]\n\t"
    "v_perm_b32 v40, v151, v147, s60\n\t"
    "v_mfma_f32_16x16x32_bf16 a[32:35], v[20:23], v[108:111], a[32:35]\n\t"
    "v_mfma_f32_16x16x32_bf16 a[36:39], v[20:23], v[12:15], a[36:39]\n\t"
    "v_perm_b32 v44, v151, v147, s61\n\t"
    "v_mfma_f32_16x16x32_bf16 a[4:7], v[16:19], v[12:15], a[4:7]\n\t"
    "ds_read_b128 v[104:107], v11 offset:17664\n\t"
    "v_mfma_f32_16x16x32_bf16 a[8:11], v[16:19], v[80:83], a[8:11]\n\t"
    "v_perm_b32 v41, v159, v155, s60\n\t"
    "v_mfma_f32_16x16x32_bf16 a[40:43], v[20:23], v[80:83], a[40:43]\n\t"
    "v_mfma_f32_16x16x32_bf16 a[44:47], v[20:23], v[84:87], a[44:47]\n\t"
    "v_perm_b32 v45, v159, v155, s61\n\t"
    "v_mfma_f32_16x16x32_bf16 a[12:15], v[16:19], v[84:87], a[12:15]\n\t"
    "v_mfma_f32_16x16x32_bf16 a[16:19], v[16:19], v[88:91], a[16:19]\n\t"
    "v_perm_b32 v42, v167, v163, s60\n\t"
    "v_mfma_f32_16x16x32_bf16 a[48:51], v[20:23], v[88:91], a[48:51]\n\t"
    "v_mfma_f32_16x16x32_bf16 a[52:55], v[20:23], v[92:95], a[52:55]\n\t"
    "v_perm_b32 v46, v167, v163, s61\n\t"
    "v_mfma_f32_16x16x32_bf16 a[20:23], v[16:19], v[92:95], a[20:23]\n\t"
    "v_mfma_f32_16x16x32_bf16 a[24:27], v[16:19], v[96:99], a[24:27]\n\t"
    "v_perm_b32 v43, v175, v171, s60\n\t"
    "v_mfma_f32_16x16x32_bf16 a[56:59], v[20:23], v[96:99], a[56:59]\n\t"
    "v_mfma_f32_16x16x32_bf16 a[60:63], v[20:23], v[100:103], a[60:63]\n\t"
    "v_perm_b32 v47, v175, v171, s61\n\t"
    "v_mfma_f32_16x16x32_bf16 a[28:31], v[16:19], v[100:103], a[28:31]\n\t"
    "s_waitcnt vmcnt(16)\n\t"
    "s_add_u32 s62, s63, 0x20000\n\t"
    "v_mfma_f32_16x16x32_bf16 a[92:95], v[24:27], v[100:103], a[92:95]\n\t"
    "v_perm_b32 v16, v180, v176, s60\n\t"
    "v_mfma_f32_16x16x32_bf16 a[124:127], v[28:31], v[100:103], a[124:127]\n\t"
    "buffer_load_dwordx4 v[144:147], v4, s[96:99], s62 offen\n\t"
    "v_mfma_f32_16x16x32_bf16 a[120:123], v[28:31], v[96:99], a[120:123]\n\t"
    "v_perm_b32 v20, v180, v176, s61\n\t"
    "v_mfma_f32_16x16x32_bf16 a[88:91], v[24:27], v[96:99], a[88:91]\n\t"
    "v_mfma_f32_16x16x32_bf16 a[84:87], v[24:27], v[92:95], a[84:87]\n\t"
    "v_perm_b32 v17, v188, v184, s60\n\t"
    "v_mfma_f32_16x16x32_bf16 a[116:119], v[28:31], v[92:95], a[116:119]\n\t"
    "v_mfma_f32_16x16x32_bf16 a[112:115], v[28:31], v[88:91], a[112:115]\n\t"
    "v_perm_b32 v21, v188, v184, s61\n\t"
    "v_mfma_f32_16x16x32_bf16 a[80:83], v[24:27], v[88:91], a[80:83]\n\t"
    "buffer_load_dwordx4 v[148:151], v4, s[96:99], s62 offen offset:1024\n\t"
    "v_mfma_f32_16x16x32_bf16 a[76:79], v[24:27], v[84:87], a[76:79]\n\t"
    "v_perm_b32 v18, v196, v192, s60\n\t"
    "v_mfma_f32_16x16x32_bf16 a[108:111], v[28:31], v[84:87], a[108:111]\n\t"
    "v_mfma_f32_16x16x32_bf16 a[104:107], v[28:31], v[80:83], a[104:107]\n\t"
    "v_perm_b32 v22, v196, v192, s61\n\t"
    "v_mfma_f32_16x16x32_bf16 a[72:75], v[24:27], v[80:83], a[72:75]\n\t"
    "v_mfma_f32_16x16x32_bf16 a[68:71], v[24:27], v[12:15], a[68:71]\n\t"
    "v_perm_b32 v19, v204, v200, s60\n\t"
    "v_mfma_f32_16x16x32_bf16 a[100:103], v[28:31], v[12:15], a[100:103]\n\t"
    "buffer_load_dwordx4 v[152:155], v4, s[96:99], s62 offen offset:2048\n\t"
    "v_mfma_f32_16x16x32_bf16 a[96:99], v[28:31], v[108:111], a[96:99]\n\t"
    "v_perm_b32 v23, v204, v200, s61\n\t"
    "v_mfma_f32_16x16x32_bf16 a[64:67], v[24:27], v[108:111], a[64:67]\n\t"
    "v_mfma_f32_16x16x32_bf16 a[128:131], v[32:35], v[108:111], a[128:131]\n\t"
    "v_perm_b32 v24, v181, v177, s60\n\t"
    "v_mfma_f32_16x16x32_bf16 a[160:163], v[36:39], v[108:111], a[160:163]\n\t"
    "v_mfma_f32_16x16x32_bf16 a[164:167], v[36:39], v[12:15], a[164:167]\n\t"
    "v_perm_b32 v28, v181, v177, s61\n\t"
    "v_mfma_f32_16x16x32_bf16 a[132:135], v[32:35], v[12:15], a[132:135]\n\t"
    "buffer_load_dwordx4 v[156:159], v4, s[96:99], s62 offen offset:3072\n\t"
    "v_mfma_f32_16x16x32_bf16 a[136:139], v[32:35], v[80:83], a[136:139]\n\t"
    "v_perm_b32 v25, v189, v185, s60\n\t"
    "v_mfma_f32_16x16x32_bf16 a[168:171], v[36:39], v[80:83], a[168:171]\n\t"
    "v_mfma_f32_16x16x32_bf16 a[172:175], v[36:39], v[84:87], a[172:175]\n\t"
    "v_perm_b32 v29, v189, v185, s61\n\t"
    "v_mfma_f32_16x16x32_bf16 a[140:143], v[32:35], v[84:87], a[140:143]\n\t"
    "v_mfma_f32_16x16x32_bf16 a[144:147], v[32:35], v[88:91], a[144:147]\n\t"
    "v_perm_b32 v26, v197, v193, s60\n\t"
    "v_mfma_f32_16x16x32_bf16 a[176:179], v[36:39], v[88:91], a[176:179]\n\t"
    "buffer_load_dwordx4 v[160:163], v5, s[96:99], s62 offen\n\t"
    "v_mfma_f32_16x16x32_bf16 a[180:183], v[36:39], v[92:95], a[180:183]\n\t"
    "v_perm_b32 v30, v197, v193, s61\n\t"
    "v_mfma_f32_16x16x32_bf16 a[148:151], v[32:35], v[92:95], a[148:151]\n\t"
    "v_mfma_f32_16x16x32_bf16 a[152:155], v[32:35], v[96:99], a[152:155]\n\t"
    "v_perm_b32 v27, v205, v201, s60\n\t"
    "v_mfma_f32_16x16x32_bf16 a[184:187], v[36:39], v[96:99], a[184:187]\n\t"
    "v_mfma_f32_16x16x32_bf16 a[188:191], v[36:39], v[100:103], a[188:191]\n\t"
    "v_perm_b32 v31, v205, v201, s61\n\t"
    "v_mfma_f32_16x16x32_bf16 a[156:159], v[32:35], v[100:103], a[156:159]\n\t"
    "buffer_load_dwordx4 v[164:167], v5, s[96:99], s62 offen offset:1024\n\t"
    "v_mfma_f32_16x16x32_bf16 a[220:223], v[40:43], v[100:103], a[220:223]\n\t"
    "v_perm_b32 v32, v182, v178, s60\n\t"
    "v_mfma_f32_16x16x32_bf16 a[252:255], v[44:47], v[100:103], a[252:255]\n\t"
    "v_mfma_f32_16x16x32_bf16 a[248:251], v[44:47], v[96:99], a[248:251]\n\t"
    "v_perm_b32 v36, v182, v178, s61\n\t"
    "v_mfma_f32_16x16x32_bf16 a[216:219], v[40:43], v[96:99], a[216:219]\n\t"
    "v_mfma_f32_16x16x32_bf16 a[212:215], v[40:43], v[92:95], a[212:215]\n\t"
    "v_perm_b32 v33, v190, v186, s60\n\t"
    "v_mfma_f32_16x16x32_bf16 a[244:247], v[44:47], v[92:95], a[244:247]\n\t"
    "buffer_load_dwordx4 v[168:171], v5, s[96:99], s62 offen offset:2048\n\t"
    "v_mfma_f32_16x16x32_bf16 a[240:243], v[44:47], v[88:91], a[240:243]\n\t"
    "v_perm_b32 v37, v190, v186, s61\n\t"
    "v_mfma_f32_16x16x32_bf16 a[208:211], v[40:43], v[88:91], a[208:211]\n\t"
    "v_mfma_f32_16x16x32_bf16 a[204:207], v[40:43], v[84:87], a[204:207]\n\t"
    "v_perm_b32 v34, v198, v194, s60\n\t"
    "v_mfma_f32_16x16x32_bf16 a[236:239], v[44:47], v[84:87], a[236:239]\n\t"
    "v_mfma_f32_16x16x32_bf16 a[232:235], v[44:47], v[80:83], a[232:235]\n\t"
    "v_perm_b32 v38, v198, v194, s61\n\t"
    "v_mfma_f32_16x16x32_bf16 a[200:203], v[40:43], v[80:83], a[200:203]\n\t"
    "buffer_load_dwordx4 v[172:175], v5, s[96:99], s62 offen offset:3072\n\t"
    "v_mfma_f32_16x16x32_bf16 a[196:199], v[40:43], v[12:15], a[196:199]\n\t"
    "v_perm_b32 v35, v206, v202, s60\n\t"
    "v_mfma_f32_16x16x32_bf16 a[228:231], v[44:47], v[12:15], a[228:231]\n\t"
    "v_mfma_f32_16x16x32_bf16 a[224:227], v[44:47], v[108:111], a[224:227]\n\t"
    "v_perm_b32 v39, v206, v202, s61\n\t"
    "v_mfma_f32_16x16x32_bf16 a[192:195], v[40:43], v[108:111], a[192:195]\n\t"
    "s_waitcnt lgkmcnt(0)\n\t"
    "v_mfma_f32_16x16x32_bf16 a[0:3], v[16:19], v[104:107], a[0:3]\n\t"
    "v_perm_b32 v40, v183, v179, s60\n\t"
    "v_mfma_f32_16x16x32_bf16 a[32:35], v[20:23], v[104:107], a[32:35]\n\t"
    "v_mfma_f32_16x16x32_bf16 a[36:39], v[20:23], v[108:111], a[36:39]\n\t"
    "v_perm_b32 v44, v183, v179, s61\n\t"
    "v_mfma_f32_16x16x32_bf16 a[4:7], v[16:19], v[108:111], a[4:7]\n\t"
    "ds_read_b128 v[100:103], v11 offset:17408\n\t"
    "v_mfma_f32_16x16x32_bf16 a[8:11], v[16:19], v[12:15], a[8:11]\n\t"
    "v_perm_b32 v41, v191, v187, s60\n\t"
    "v_mfma_f32_16x16x32_bf16 a[40:43], v[20:23], v[12:15], a[40:43]\n\t"
    "v_mfma_f32_16x16x32_bf16 a[44:47], v[20:23], v[80:83], a[44:47]\n\t"
    "v_perm_b32 v45, v191, v187, s61\n\t"
    "v_mfma_f32_16x16x32_bf16 a[12:15], v[16:19], v[80:83], a[12:15]\n\t"
    "v_mfma_f32_16x16x32_bf16 a[16:19], v[16:19], v[84:87], a[16:19]\n\t"
    "v_perm_b32 v42, v199, v195, s60\n\t"
    "v_mfma_f32_16x16x32_bf16 a[48:51], v[20:23], v[84:87], a[48:51]\n\t"
    "v_mfma_f32_16x16x32_bf16 a[52:55], v[20:23], v[88:91], a[52:55]\n\t"
    "v_perm_b32 v46, v199, v195, s61\n\t"
    "v_mfma_f32_16x16x32_bf16 a[20:23], v[16:19], v[88:91], a[20:23]\n\t"
    "ds_read_b128 v[48:51], v8 offset:35840\n\t"
    "v_mfma_f32_16x16x32_bf16 a[24:27], v[16:19], v[92:95], a[24:27]\n\t"
    "v_perm_b32 v43, v207, v203, s60\n\t"
    "v_mfma_f32_16x16x32_bf16 a[56:59], v[20:23], v[92:95], a[56:59]\n\t"
    "v_mfma_f32_16x16x32_bf16 a[60:63], v[20:23], v[96:99], a[60:63]\n\t"
    "v_perm_b32 v47, v207, v203, s61\n\t"
    "v_mfma_f32_16x16x32_bf16 a[28:31], v[16:19], v[96:99], a[28:31]\n\t"
    "s_waitcnt vmcnt(16)\n\t"
    "s_mov_b32 s62, 0xa0000\n\t"
    "v_mfma_f32_16x16x32_bf16 a[92:95], v[24:27], v[96:99], a[92:95]\n\t"
    "v_perm_b32 v16, v212, v208, s60\n\t"
    "v_mfma_f32_16x16x32_bf16 a[124:127], v[28:31], v[96:99], a[124:127]\n\t"
    "buffer_load_dwordx4 v[176:179], v4, s[96:99], s62 offen\n\t"
    "v_mfma_f32_16x16x32_bf16 a[120:123], v[28:31], v[92:95], a[120:123]\n\t"
    "v_perm_b32 v20, v212, v208, s61\n\t"
    "v_mfma_f32_16x16x32_bf16 a[88:91], v[24:27], v[92:95], a[88:91]\n\t"
    "ds_read_b128 v[52:55], v8 offset:36096\n\t"
    "v_mfma_f32_16x16x32_bf16 a[84:87], v[24:27], v[88:91], a[84:87]\n\t"
    "v_perm_b32 v17, v220, v216, s60\n\t"
    "v_mfma_f32_16x16x32_bf16 a[116:119], v[28:31], v[88:91], a[116:119]\n\t"
    "v_mfma_f32_16x16x32_bf16 a[112:115], v[28:31], v[84:87], a[112:115]\n\t"
    "v_perm_b32 v21, v220, v216, s61\n\t"
    "v_mfma_f32_16x16x32_bf16 a[80:83], v[24:27], v[84:87], a[80:83]\n\t"
    "buffer_load_dwordx4 v[180:183], v4, s[96:99], s62 offen offset:1024\n\t"
    "v_mfma_f32_16x16x32_bf16 a[76:79], v[24:27], v[80:83], a[76:79]\n\t"
    "v_perm_b32 v18, v228, v224, s60\n\t"
    "v_mfma_f32_16x16x32_bf16 a[108:111], v[28:31], v[80:83], a[108:111]\n\t"
    "v_mfma_f32_16x16x32_bf16 a[104:107], v[28:31], v[12:15], a[104:107]\n\t"
    "v_perm_b32 v22, v228, v224, s61\n\t"
    "v_mfma_f32_16x16x32_bf16 a[72:75], v[24:27], v[12:15], a[72:75]\n\t"
    "ds_read_b128 v[56:59], v8 offset:36352\n\t"
    "v_mfma_f32_16x16x32_bf16 a[68:71], v[24:27], v[108:111], a[68:71]\n\t"
    "v_perm_b32 v19, v236, v232, s60\n\t"
    "v_mfma_f32_16x16x32_bf16 a[100:103], v[28:31], v[108:111], a[100:103]\n\t"
    "buffer_load_dwordx4 v[184:187], v4, s[96:99], s62 offen offset:2048\n\t"
    "v_mfma_f32_16x16x32_bf16 a[96:99], v[28:31], v[104:107], a[96:99]\n\t"
    "v_perm_b32 v23, v236, v232, s61\n\t"
    "v_mfma_f32_16x16x32_bf16 a[64:67], v[24:27], v[104:107], a[64:67]\n\t"
    "v_mfma_f32_16x16x32_bf16 a[128:131], v[32:35], v[104:107], a[128:131]\n\t"
    "v_perm_b32 v24, v213, v209, s60\n\t"
    "v_mfma_f32_16x16x32_bf16 a[160:163], v[36:39], v[104:107], a[160:163]\n\t"
    "v_mfma_f32_16x16x32_bf16 a[164:167], v[36:39], v[108:111], a[164:167]\n\t"
    "v_perm_b32 v28, v213, v209, s61\n\t"
    "v_mfma_f32_16x16x32_bf16 a[132:135], v[32:35], v[108:111], a[132:135]\n\t"
    "buffer_load_dwordx4 v[188:191], v4, s[96:99], s62 offen offset:3072\n\t"
    "ds_read_b128 v[60:63], v8 offset:36608\n\t"
    "v_mfma_f32_16x16x32_bf16 a[136:139], v[32:35], v[12:15], a[136:139]\n\t"
    "v_perm_b32 v25, v221, v217, s60\n\t"
    "v_mfma_f32_16x16x32_bf16 a[168:171], v[36:39], v[12:15], a[168:171]\n\t"
    "v_mfma_f32_16x16x32_bf16 a[172:175], v[36:39], v[80:83], a[172:175]\n\t"
    "v_perm_b32 v29, v221, v217, s61\n\t"
    "v_mfma_f32_16x16x32_bf16 a[140:143], v[32:35], v[80:83], a[140:143]\n\t"
    "v_mfma_f32_16x16x32_bf16 a[144:147], v[32:35], v[84:87], a[144:147]\n\t"
    "v_perm_b32 v26, v229, v225, s60\n\t"
    "v_mfma_f32_16x16x32_bf16 a[176:179], v[36:39], v[84:87], a[176:179]\n\t"
    "buffer_load_dwordx4 v[192:195], v5, s[96:99], s62 offen\n\t"
    "v_mfma_f32_16x16x32_bf16 a[180:183], v[36:39], v[88:91], a[180:183]\n\t"
    "v_perm_b32 v30, v229, v225, s61\n\t"
    "v_mfma_f32_16x16x32_bf16 a[148:151], v[32:35], v[88:91], a[148:151]\n\t"
    "v_mfma_f32_16x16x32_bf16 a[152:155], v[32:35], v[92:95], a[152:155]\n\t"
    "v_perm_b32 v27, v237, v233, s60\n\t"
    "v_mfma_f32_16x16x32_bf16 a[184:187], v[36:39], v[92:95], a[184:187]\n\t"
    "v_mfma_f32_16x16x32_bf16 a[188:191], v[36:39], v[96:99], a[188:191]\n\t"
    "v_perm_b32 v31, v237, v233, s61\n\t"
    "v_mfma_f32_16x16x32_bf16 a[156:159], v[32:35], v[96:99], a[156:159]\n\t"
    "buffer_load_dwordx4 v[196:199], v5, s[96:99], s62 offen offset:1024\n\t"
    "v_mfma_f32_16x16x32_bf16 a[220:223], v[40:43], v[96:99], a[220:223]\n\t"
    "v_perm_b32 v32, v214, v210, s60\n\t"
    "v_mfma_f32_16x16x32_bf16 a[252:255], v[44:47], v[96:99], a[252:255]\n\t"
    "v_mfma_f32_16x16x32_bf16 a[248:251], v[44:47], v[92:95], a[248:251]\n\t"
    "v_perm_b32 v36, v214, v210, s61\n\t"
    "v_mfma_f32_16x16x32_bf16 a[216:219], v[40:43], v[92:95], a[216:219]\n\t"
    "v_mfma_f32_16x16x32_bf16 a[212:215], v[40:43], v[88:91], a[212:215]\n\t"
    "v_perm_b32 v33, v222, v218, s60\n\t"
    "v_mfma_f32_16x16x32_bf16 a[244:247], v[44:47], v[88:91], a[244:247]\n\t"
    "buffer_load_dwordx4 v[200:203], v5, s[96:99], s62 offen offset:2048\n\t"
    "v_mfma_f32_16x16x32_bf16 a[240:243], v[44:47], v[84:87], a[240:243]\n\t"
    "v_perm_b32 v37, v222, v218, s61\n\t"
    "v_mfma_f32_16x16x32_bf16 a[208:211], v[40:43], v[84:87], a[208:211]\n\t"
    "v_mfma_f32_16x16x32_bf16 a[204:207], v[40:43], v[80:83], a[204:207]\n\t"
    "v_perm_b32 v34, v230, v226, s60\n\t"
    "v_mfma_f32_16x16x32_bf16 a[236:239], v[44:47], v[80:83], a[236:239]\n\t"
    "v_mfma_f32_16x16x32_bf16 a[232:235], v[44:47], v[12:15], a[232:235]\n\t"
    "v_perm_b32 v38, v230, v226, s61\n\t"
    "v_mfma_f32_16x16x32_bf16 a[200:203], v[40:43], v[12:15], a[200:203]\n\t"
    "buffer_load_dwordx4 v[204:207], v5, s[96:99], s62 offen offset:3072\n\t"
    "v_mfma_f32_16x16x32_bf16 a[196:199], v[40:43], v[108:111], a[196:199]\n\t"
    "v_perm_b32 v35, v238, v234, s60\n\t"
    "v_mfma_f32_16x16x32_bf16 a[228:231], v[44:47], v[108:111], a[228:231]\n\t"
    "v_mfma_f32_16x16x32_bf16 a[224:227], v[44:47], v[104:107], a[224:227]\n\t"
    "v_perm_b32 v39, v238, v234, s61\n\t"
    "v_mfma_f32_16x16x32_bf16 a[192:195], v[40:43], v[104:107], a[192:195]\n\t"
    "s_waitcnt lgkmcnt(0)\n\t"
    "v_mfma_f32_16x16x32_bf16 a[0:3], v[16:19], v[100:103], a[0:3]\n\t"
    "v_perm_b32 v40, v215, v211, s60\n\t"
    "v_mfma_f32_16x16x32_bf16 a[32:35], v[20:23], v[100:103], a[32:35]\n\t"
    "v_mfma_f32_16x16x32_bf16 a[36:39], v[20:23], v[104:107], a[36:39]\n\t"
    "v_perm_b32 v44, v215, v211, s61\n\t"
    "v_mfma_f32_16x16x32_bf16 a[4:7], v[16:19], v[104:107], a[4:7]\n\t"
    "v_mfma_f32_16x16x32_bf16 a[8:11], v[16:19], v[108:111], a[8:11]\n\t"
    "v_perm_b32 v41, v223, v219, s60\n\t"
    "v_mfma_f32_16x16x32_bf16 a[40:43], v[20:23], v[108:111], a[40:43]\n\t"
    "v_mfma_f32_16x16x32_bf16 a[44:47], v[20:23], v[12:15], a[44:47]\n\t"
    "v_perm_b32 v45, v223, v219, s61\n\t"
    "v_mfma_f32_16x16x32_bf16 a[12:15], v[16:19], v[12:15], a[12:15]\n\t"
    "v_mfma_f32_16x16x32_bf16 a[16:19], v[16:19], v[80:83], a[16:19]\n\t"
    "v_perm_b32 v42, v231, v227, s60\n\t"
    "v_mfma_f32_16x16x32_bf16 a[48:51], v[20:23], v[80:83], a[48:51]\n\t"
    "v_mfma_f32_16x16x32_bf16 a[52:55], v[20:23], v[84:87], a[52:55]\n\t"
    "v_perm_b32 v46, v231, v227, s61\n\t"
    "v_mfma_f32_16x16x32_bf16 a[20:23], v[16:19], v[84:87], a[20:23]\n\t"
    "ds_read_b128 v[64:67], v8 offset:36864\n\t"
    "v_mfma_f32_16x16x32_bf16 a[24:27], v[16:19], v[88:91], a[24:27]\n\t"
    "v_perm_b32 v43, v239, v235, s60\n\t"
    "v_mfma_f32_16x16x32_bf16 a[56:59], v[20:23], v[88:91], a[56:59]\n\t"
    "v_mfma_f32_16x16x32_bf16 a[60:63], v[20:23], v[92:95], a[60:63]\n\t"
    "v_perm_b32 v47, v239, v235, s61\n\t"
    "v_mfma_f32_16x16x32_bf16 a[28:31], v[16:19], v[92:95], a[28:31]\n\t"
    "s_waitcnt vmcnt(16)\n\t"
    "s_add_u32 s62, s70, 0x20000\n\t"
    "v_mfma_f32_16x16x32_bf16 a[92:95], v[24:27], v[92:95], a[92:95]\n\t"
    "v_perm_b32 v16, v116, v112, s60\n\t"
    "v_mfma_f32_16x16x32_bf16 a[124:127], v[28:31], v[92:95], a[124:127]\n\t"
    "buffer_load_dwordx4 v[208:211], v4, s[96:99], s62 offen\n\t"
    "v_mfma_f32_16x16x32_bf16 a[120:123], v[28:31], v[88:91], a[120:123]\n\t"
    "v_perm_b32 v20, v116, v112, s61\n\t"
    "v_mfma_f32_16x16x32_bf16 a[88:91], v[24:27], v[88:91], a[88:91]\n\t"
    "ds_read_b128 v[68:71], v8 offset:37120\n\t"
    "v_mfma_f32_16x16x32_bf16 a[84:87], v[24:27], v[84:87], a[84:87]\n\t"
    "v_perm_b32 v17, v124, v120, s60\n\t"
    "v_mfma_f32_16x16x32_bf16 a[116:119], v[28:31], v[84:87], a[116:119]\n\t"
    "v_mfma_f32_16x16x32_bf16 a[112:115], v[28:31], v[80:83], a[112:115]\n\t"
    "v_perm_b32 v21, v124, v120, s61\n\t"
    "v_mfma_f32_16x16x32_bf16 a[80:83], v[24:27], v[80:83], a[80:83]\n\t"
    "buffer_load_dwordx4 v[212:215], v4, s[96:99], s62 offen offset:1024\n\t"
    "v_mfma_f32_16x16x32_bf16 a[76:79], v[24:27], v[12:15], a[76:79]\n\t"
    "v_perm_b32 v18, v132, v128, s60\n\t"
    "v_mfma_f32_16x16x32_bf16 a[108:111], v[28:31], v[12:15], a[108:111]\n\t"
    "v_mfma_f32_16x16x32_bf16 a[104:107], v[28:31], v[108:111], a[104:107]\n\t"
    "v_perm_b32 v22, v132, v128, s61\n\t"
    "v_mfma_f32_16x16x32_bf16 a[72:75], v[24:27], v[108:111], a[72:75]\n\t"
    "ds_read_b128 v[72:75], v8 offset:37376\n\t"
    "v_mfma_f32_16x16x32_bf16 a[68:71], v[24:27], v[104:107], a[68:71]\n\t"
    "v_perm_b32 v19, v140, v136, s60\n\t"
    "v_mfma_f32_16x16x32_bf16 a[100:103], v[28:31], v[104:107], a[100:103]\n\t"
    "buffer_load_dwordx4 v[216:219], v4, s[96:99], s62 offen offset:2048\n\t"
    "v_mfma_f32_16x16x32_bf16 a[96:99], v[28:31], v[100:103], a[96:99]\n\t"
    "v_perm_b32 v23, v140, v136, s61\n\t"
    "v_mfma_f32_16x16x32_bf16 a[64:67], v[24:27], v[100:103], a[64:67]\n\t"
    "v_mfma_f32_16x16x32_bf16 a[128:131], v[32:35], v[100:103], a[128:131]\n\t"
    "v_perm_b32 v24, v117, v113, s60\n\t"
    "v_mfma_f32_16x16x32_bf16 a[160:163], v[36:39], v[100:103], a[160:163]\n\t"
    "v_mfma_f32_16x16x32_bf16 a[164:167], v[36:39], v[104:107], a[164:167]\n\t"
    "v_perm_b32 v28, v117, v113, s61\n\t"
    "v_mfma_f32_16x16x32_bf16 a[132:135], v[32:35], v[104:107], a[132:135]\n\t"
    "buffer_load_dwordx4 v[220:223], v4, s[96:99], s62 offen offset:3072\n\t"
    "ds_read_b128 v[76:79], v8 offset:37632\n\t"
    "v_mfma_f32_16x16x32_bf16 a[136:139], v[32:35], v[108:111], a[136:139]\n\t"
    "v_perm_b32 v25, v125, v121, s60\n\t"
    "v_mfma_f32_16x16x32_bf16 a[168:171], v[36:39], v[108:111], a[168:171]\n\t"
    "v_mfma_f32_16x16x32_bf16 a[172:175], v[36:39], v[12:15], a[172:175]\n\t"
    "v_perm_b32 v29, v125, v121, s61\n\t"
    "v_mfma_f32_16x16x32_bf16 a[140:143], v[32:35], v[12:15], a[140:143]\n\t"
    "v_mfma_f32_16x16x32_bf16 a[144:147], v[32:35], v[80:83], a[144:147]\n\t"
    "v_perm_b32 v26, v133, v129, s60\n\t"
    "v_mfma_f32_16x16x32_bf16 a[176:179], v[36:39], v[80:83], a[176:179]\n\t"
    "buffer_load_dwordx4 v[224:227], v5, s[96:99], s62 offen\n\t"
    "v_mfma_f32_16x16x32_bf16 a[180:183], v[36:39], v[84:87], a[180:183]\n\t"
    "v_perm_b32 v30, v133, v129, s61\n\t"
    "v_mfma_f32_16x16x32_bf16 a[148:151], v[32:35], v[84:87], a[148:151]\n\t"
    "v_mfma_f32_16x16x32_bf16 a[152:155], v[32:35], v[88:91], a[152:155]\n\t"
    "v_perm_b32 v27, v141, v137, s60\n\t"
    "v_mfma_f32_16x16x32_bf16 a[184:187], v[36:39], v[88:91], a[184:187]\n\t"
    "v_mfma_f32_16x16x32_bf16 a[188:191], v[36:39], v[92:95], a[188:191]\n\t"
    "v_perm_b32 v31, v141, v137, s61\n\t"
    "v_mfma_f32_16x16x32_bf16 a[156:159], v[32:35], v[92:95], a[156:159]\n\t"
    "buffer_load_dwordx4 v[228:231], v5, s[96:99], s62 offen offset:1024\n\t"
    "v_mfma_f32_16x16x32_bf16 a[220:223], v[40:43], v[92:95], a[220:223]\n\t"
    "v_perm_b32 v32, v118, v114, s60\n\t"
    "v_mfma_f32_16x16x32_bf16 a[252:255], v[44:47], v[92:95], a[252:255]\n\t"
    "v_mfma_f32_16x16x32_bf16 a[248:251], v[44:47], v[88:91], a[248:251]\n\t"
    "v_perm_b32 v36, v118, v114, s61\n\t"
    "v_mfma_f32_16x16x32_bf16 a[216:219], v[40:43], v[88:91], a[216:219]\n\t"
    "v_mfma_f32_16x16x32_bf16 a[212:215], v[40:43], v[84:87], a[212:215]\n\t"
    "v_perm_b32 v33, v126, v122, s60\n\t"
    "v_mfma_f32_16x16x32_bf16 a[244:247], v[44:47], v[84:87], a[244:247]\n\t"
    "buffer_load_dwordx4 v[232:235], v5, s[96:99], s62 offen offset:2048\n\t"
    "v_mfma_f32_16x16x32_bf16 a[240:243], v[44:47], v[80:83], a[240:243]\n\t"
    "v_perm_b32 v37, v126, v122, s61\n\t"
    "v_mfma_f32_16x16x32_bf16 a[208:211], v[40:43], v[80:83], a[208:211]\n\t"
    "v_mfma_f32_16x16x32_bf16 a[204:207], v[40:43], v[12:15], a[204:207]\n\t"
    "v_perm_b32 v34, v134, v130, s60\n\t"
    "v_mfma_f32_16x16x32_bf16 a[236:239], v[44:47], v[12:15], a[236:239]\n\t"
    "v_mfma_f32_16x16x32_bf16 a[232:235], v[44:47], v[108:111], a[232:235]\n\t"
    "v_perm_b32 v38, v134, v130, s61\n\t"
    "v_mfma_f32_16x16x32_bf16 a[200:203], v[40:43], v[108:111], a[200:203]\n\t"
    "buffer_load_dwordx4 v[236:239], v5, s[96:99], s62 offen offset:3072\n\t"
    "v_mfma_f32_16x16x32_bf16 a[196:199], v[40:43], v[104:107], a[196:199]\n\t"
    "v_perm_b32 v35, v142, v138, s60\n\t"
    "v_mfma_f32_16x16x32_bf16 a[228:231], v[44:47], v[104:107], a[228:231]\n\t"
    "v_mfma_f32_16x16x32_bf16 a[224:227], v[44:47], v[100:103], a[224:227]\n\t"
    "v_perm_b32 v39, v142, v138, s61\n\t"
    "v_mfma_f32_16x16x32_bf16 a[192:195], v[40:43], v[100:103], a[192:195]\n\t"
    "s_waitcnt lgkmcnt(0)\n\t"
    "v_mfma_f32_16x16x32_bf16 a[0:3], v[16:19], v[48:51], a[0:3]\n\t"
    "v_perm_b32 v40, v119, v115, s60\n\t"
    "v_mfma_f32_16x16x32_bf16 a[32:35], v[20:23], v[48:51], a[32:35]\n\t"
    "v_mfma_f32_16x16x32_bf16 a[36:39], v[20:23], v[52:55], a[36:39]\n\t"
    "v_perm_b32 v44, v119, v115, s61\n\t"
    "v_mfma_f32_16x16x32_bf16 a[4:7], v[16:19], v[52:55], a[4:7]\n\t"
    "ds_read_b128 v[12:15], v9 offset:35584\n\t"
    "v_mfma_f32_16x16x32_bf16 a[8:11], v[16:19], v[56:59], a[8:11]\n\t"
    "v_perm_b32 v41, v127, v123, s60\n\t"
    "v_mfma_f32_16x16x32_bf16 a[40:43], v[20:23], v[56:59], a[40:43]\n\t"
    "v_mfma_f32_16x16x32_bf16 a[44:47], v[20:23], v[60:63], a[44:47]\n\t"
    "v_perm_b32 v45, v127, v123, s61\n\t"
    "v_mfma_f32_16x16x32_bf16 a[12:15], v[16:19], v[60:63], a[12:15]\n\t"
    "v_mfma_f32_16x16x32_bf16 a[16:19], v[16:19], v[64:67], a[16:19]\n\t"
    "v_perm_b32 v42, v135, v131, s60\n\t"
    "v_mfma_f32_16x16x32_bf16 a[48:51], v[20:23], v[64:67], a[48:51]\n\t"
    "v_mfma_f32_16x16x32_bf16 a[52:55], v[20:23], v[68:71], a[52:55]\n\t"
    "v_perm_b32 v46, v135, v131, s61\n\t"
    "v_mfma_f32_16x16x32_bf16 a[20:23], v[16:19], v[68:71], a[20:23]\n\t"
    "v_mfma_f32_16x16x32_bf16 a[24:27], v[16:19], v[72:75], a[24:27]\n\t"
    "v_perm_b32 v43, v143, v139, s60\n\t"
    "v_mfma_f32_16x16x32_bf16 a[56:59], v[20:23], v[72:75], a[56:59]\n\t"
    "v_mfma_f32_16x16x32_bf16 a[60:63], v[20:23], v[76:79], a[60:63]\n\t"
    "v_perm_b32 v47, v143, v139, s61\n\t"
    "v_mfma_f32_16x16x32_bf16 a[28:31], v[16:19], v[76:79], a[28:31]\n\t"
    "s_waitcnt vmcnt(16)\n\t"
    "s_add_u32 s62, s72, 0x20000\n\t"
    "v_mfma_f32_16x16x32_bf16 a[92:95], v[24:27], v[76:79], a[92:95]\n\t"
    "v_perm_b32 v16, v148, v144, s60\n\t"
    "v_mfma_f32_16x16x32_bf16 a[124:127], v[28:31], v[76:79], a[124:127]\n\t"
    "buffer_load_dwordx4 v[112:115], v4, s[96:99], s62 offen\n\t"
    "v_mfma_f32_16x16x32_bf16 a[120:123], v[28:31], v[72:75], a[120:123]\n\t"
    "v_perm_b32 v20, v148, v144, s61\n\t"
    "v_mfma_f32_16x16x32_bf16 a[88:91], v[24:27], v[72:75], a[88:91]\n\t"
    "v_mfma_f32_16x16x32_bf16 a[84:87], v[24:27], v[68:71], a[84:87]\n\t"
    "v_perm_b32 v17, v156, v152, s60\n\t"
    "v_mfma_f32_16x16x32_bf16 a[116:119], v[28:31], v[68:71], a[116:119]\n\t"
    "v_mfma_f32_16x16x32_bf16 a[112:115], v[28:31], v[64:67], a[112:115]\n\t"
    "v_perm_b32 v21, v156, v152, s61\n\t"
    "v_mfma_f32_16x16x32_bf16 a[80:83], v[24:27], v[64:67], a[80:83]\n\t"
    "buffer_load_dwordx4 v[116:119], v4, s[96:99], s62 offen offset:1024\n\t"
    "v_mfma_f32_16x16x32_bf16 a[76:79], v[24:27], v[60:63], a[76:79]\n\t"
    "v_perm_b32 v18, v164, v160, s60\n\t"
    "v_mfma_f32_16x16x32_bf16 a[108:111], v[28:31], v[60:63], a[108:111]\n\t"
    "v_mfma_f32_16x16x32_bf16 a[104:107], v[28:31], v[56:59], a[104:107]\n\t"
    "v_perm_b32 v22, v164, v160, s61\n\t"
    "v_mfma_f32_16x16x32_bf16 a[72:75], v[24:27], v[56:59], a[72:75]\n\t"
    "v_mfma_f32_16x16x32_bf16 a[68:71], v[24:27], v[52:55], a[68:71]\n\t"
    "v_perm_b32 v19, v172, v168, s60\n\t"
    "v_mfma_f32_16x16x32_bf16 a[100:103], v[28:31], v[52:55], a[100:103]\n\t"
    "buffer_load_dwordx4 v[120:123], v4, s[96:99], s62 offen offset:2048\n\t"
    "v_mfma_f32_16x16x32_bf16 a[96:99], v[28:31], v[48:51], a[96:99]\n\t"
    "v_perm_b32 v23, v172, v168, s61\n\t"
    "v_mfma_f32_16x16x32_bf16 a[64:67], v[24:27], v[48:51], a[64:67]\n\t"
    "v_mfma_f32_16x16x32_bf16 a[128:131], v[32:35], v[48:51], a[128:131]\n\t"
    "v_perm_b32 v24, v149, v145, s60\n\t"
    "v_mfma_f32_16x16x32_bf16 a[160:163], v[36:39], v[48:51], a[160:163]\n\t"
    "v_mfma_f32_16x16x32_bf16 a[164:167], v[36:39], v[52:55], a[164:167]\n\t"
    "v_perm_b32 v28, v149, v145, s61\n\t"
    "v_mfma_f32_16x16x32_bf16 a[132:135], v[32:35], v[52:55], a[132:135]\n\t"
    "buffer_load_dwordx4 v[124:127], v4, s[96:99], s62 offen offset:3072\n\t"
    "v_mfma_f32_16x16x32_bf16 a[136:139], v[32:35], v[56:59], a[136:139]\n\t"
    "v_perm_b32 v25, v157, v153, s60\n\t"
    "v_mfma_f32_16x16x32_bf16 a[168:171], v[36:39], v[56:59], a[168:171]\n\t"
    "v_mfma_f32_16x16x32_bf16 a[172:175], v[36:39], v[60:63], a[172:175]\n\t"
    "v_perm_b32 v29, v157, v153, s61\n\t"
    "v_mfma_f32_16x16x32_bf16 a[140:143], v[32:35], v[60:63], a[140:143]\n\t"
    "v_mfma_f32_16x16x32_bf16 a[144:147], v[32:35], v[64:67], a[144:147]\n\t"
    "v_perm_b32 v26, v165, v161, s60\n\t"
    "v_mfma_f32_16x16x32_bf16 a[176:179], v[36:39], v[64:67], a[176:179]\n\t"
    "buffer_load_dwordx4 v[128:131], v5, s[96:99], s62 offen\n\t"
    "v_mfma_f32_16x16x32_bf16 a[180:183], v[36:39], v[68:71], a[180:183]\n\t"
    "v_perm_b32 v30, v165, v161, s61\n\t"
    "v_mfma_f32_16x16x32_bf16 a[148:151], v[32:35], v[68:71], a[148:151]\n\t"
    "v_mfma_f32_16x16x32_bf16 a[152:155], v[32:35], v[72:75], a[152:155]\n\t"
    "v_perm_b32 v27, v173, v169, s60\n\t"
    "v_mfma_f32_16x16x32_bf16 a[184:187], v[36:39], v[72:75], a[184:187]\n\t"
    "v_mfma_f32_16x16x32_bf16 a[188:191], v[36:39], v[76:79], a[188:191]\n\t"
    "v_perm_b32 v31, v173, v169, s61\n\t"
    "v_mfma_f32_16x16x32_bf16 a[156:159], v[32:35], v[76:79], a[156:159]\n\t"
    "buffer_load_dwordx4 v[132:135], v5, s[96:99], s62 offen offset:1024\n\t"
    "v_mfma_f32_16x16x32_bf16 a[220:223], v[40:43], v[76:79], a[220:223]\n\t"
    "v_perm_b32 v32, v150, v146, s60\n\t"
    "v_mfma_f32_16x16x32_bf16 a[252:255], v[44:47], v[76:79], a[252:255]\n\t"
    "v_mfma_f32_16x16x32_bf16 a[248:251], v[44:47], v[72:75], a[248:251]\n\t"
    "v_perm_b32 v36, v150, v146, s61\n\t"
    "v_mfma_f32_16x16x32_bf16 a[216:219], v[40:43], v[72:75], a[216:219]\n\t"
    "v_mfma_f32_16x16x32_bf16 a[212:215], v[40:43], v[68:71], a[212:215]\n\t"
    "v_perm_b32 v33, v158, v154, s60\n\t"
    "v_mfma_f32_16x16x32_bf16 a[244:247], v[44:47], v[68:71], a[244:247]\n\t"
    "buffer_load_dwordx4 v[136:139], v5, s[96:99], s62 offen offset:2048\n\t"
    "v_mfma_f32_16x16x32_bf16 a[240:243], v[44:47], v[64:67], a[240:243]\n\t"
    "v_perm_b32 v37, v158, v154, s61\n\t"
    "v_mfma_f32_16x16x32_bf16 a[208:211], v[40:43], v[64:67], a[208:211]\n\t"
    "v_mfma_f32_16x16x32_bf16 a[204:207], v[40:43], v[60:63], a[204:207]\n\t"
    "v_perm_b32 v34, v166, v162, s60\n\t"
    "v_mfma_f32_16x16x32_bf16 a[236:239], v[44:47], v[60:63], a[236:239]\n\t"
    "v_mfma_f32_16x16x32_bf16 a[232:235], v[44:47], v[56:59], a[232:235]\n\t"
    "v_perm_b32 v38, v166, v162, s61\n\t"
    "v_mfma_f32_16x16x32_bf16 a[200:203], v[40:43], v[56:59], a[200:203]\n\t"
    "buffer_load_dwordx4 v[140:143], v5, s[96:99], s62 offen offset:3072\n\t"
    "v_mfma_f32_16x16x32_bf16 a[196:199], v[40:43], v[52:55], a[196:199]\n\t"
    "v_perm_b32 v35, v174, v170, s60\n\t"
    "v_mfma_f32_16x16x32_bf16 a[228:231], v[44:47], v[52:55], a[228:231]\n\t"
    "v_mfma_f32_16x16x32_bf16 a[224:227], v[44:47], v[48:51], a[224:227]\n\t"
    "v_perm_b32 v39, v174, v170, s61\n\t"
    "v_mfma_f32_16x16x32_bf16 a[192:195], v[40:43], v[48:51], a[192:195]\n\t"
    "s_waitcnt lgkmcnt(0)\n\t"
    "v_mfma_f32_16x16x32_bf16 a[0:3], v[16:19], v[12:15], a[0:3]\n\t"
    "v_perm_b32 v40, v151, v147, s60\n\t"
    "v_mfma_f32_16x16x32_bf16 a[32:35], v[20:23], v[12:15], a[32:35]\n\t"
    "v_mfma_f32_16x16x32_bf16 a[36:39], v[20:23], v[48:51], a[36:39]\n\t"
    "v_perm_b32 v44, v151, v147, s61\n\t"
    "v_mfma_f32_16x16x32_bf16 a[4:7], v[16:19], v[48:51], a[4:7]\n\t"
    "ds_read_b128 v[76:79], v9 offset:35328\n\t"
    "v_mfma_f32_16x16x32_bf16 a[8:11], v[16:19], v[52:55], a[8:11]\n\t"
    "v_perm_b32 v41, v159, v155, s60\n\t"
    "v_mfma_f32_16x16x32_bf16 a[40:43], v[20:23], v[52:55], a[40:43]\n\t"
    "v_mfma_f32_16x16x32_bf16 a[44:47], v[20:23], v[56:59], a[44:47]\n\t"
    "v_perm_b32 v45, v159, v155, s61\n\t"
    "v_mfma_f32_16x16x32_bf16 a[12:15], v[16:19], v[56:59], a[12:15]\n\t"
    "v_mfma_f32_16x16x32_bf16 a[16:19], v[16:19], v[60:63], a[16:19]\n\t"
    "v_perm_b32 v42, v167, v163, s60\n\t"
    "v_mfma_f32_16x16x32_bf16 a[48:51], v[20:23], v[60:63], a[48:51]\n\t"
    "v_mfma_f32_16x16x32_bf16 a[52:55], v[20:23], v[64:67], a[52:55]\n\t"
    "v_perm_b32 v46, v167, v163, s61\n\t"
    "v_mfma_f32_16x16x32_bf16 a[20:23], v[16:19], v[64:67], a[20:23]\n\t"
    "v_mfma_f32_16x16x32_bf16 a[24:27], v[16:19], v[68:71], a[24:27]\n\t"
    "v_perm_b32 v43, v175, v171, s60\n\t"
    "v_mfma_f32_16x16x32_bf16 a[56:59], v[20:23], v[68:71], a[56:59]\n\t"
    "v_mfma_f32_16x16x32_bf16 a[60:63], v[20:23], v[72:75], a[60:63]\n\t"
    "v_perm_b32 v47, v175, v171, s61\n\t"
    "v_mfma_f32_16x16x32_bf16 a[28:31], v[16:19], v[72:75], a[28:31]\n\t"
    "s_waitcnt vmcnt(16)\n\t"
    "s_add_u32 s62, %[tb], 0x28000\n\t"
    "v_mfma_f32_16x16x32_bf16 a[92:95], v[24:27], v[72:75], a[92:95]\n\t"
    "v_perm_b32 v16, v180, v176, s60\n\t"
    "v_mfma_f32_16x16x32_bf16 a[124:127], v[28:31], v[72:75], a[124:127]\n\t"
    "buffer_load_dwordx4 v[144:147], v4, s[96:99], s62 offen\n\t"
    "v_mfma_f32_16x16x32_bf16 a[120:123], v[28:31], v[68:71], a[120:123]\n\t"
    "v_perm_b32 v20, v180, v176, s61\n\t"
    "v_mfma_f32_16x16x32_bf16 a[88:91], v[24:27], v[68:71], a[88:91]\n\t"
    "v_mfma_f32_16x16x32_bf16 a[84:87], v[24:27], v[64:67], a[84:87]\n\t"
    "v_perm_b32 v17, v188, v184, s60\n\t"
    "v_mfma_f32_16x16x32_bf16 a[116:119], v[28:31], v[64:67], a[116:119]\n\t"
    "v_mfma_f32_16x16x32_bf16 a[112:115], v[28:31], v[60:63], a[112:115]\n\t"
    "v_perm_b32 v21, v188, v184, s61\n\t"
    "v_mfma_f32_16x16x32_bf16 a[80:83], v[24:27], v[60:63], a[80:83]\n\t"
    "buffer_load_dwordx4 v[148:151], v4, s[96:99], s62 offen offset:1024\n\t"
    "v_mfma_f32_16x16x32_bf16 a[76:79], v[24:27], v[56:59], a[76:79]\n\t"
    "v_perm_b32 v18, v196, v192, s60\n\t"
    "v_mfma_f32_16x16x32_bf16 a[108:111], v[28:31], v[56:59], a[108:111]\n\t"
    "v_mfma_f32_16x16x32_bf16 a[104:107], v[28:31], v[52:55], a[104:107]\n\t"
    "v_perm_b32 v22, v196, v192, s61\n\t"
    "v_mfma_f32_16x16x32_bf16 a[72:75], v[24:27], v[52:55], a[72:75]\n\t"
    "v_mfma_f32_16x16x32_bf16 a[68:71], v[24:27], v[48:51], a[68:71]\n\t"
    "v_perm_b32 v19, v204, v200, s60\n\t"
    "v_mfma_f32_16x16x32_bf16 a[100:103], v[28:31], v[48:51], a[100:103]\n\t"
    "buffer_load_dwordx4 v[152:155], v4, s[96:99], s62 offen offset:2048\n\t"
    "v_mfma_f32_16x16x32_bf16 a[96:99], v[28:31], v[12:15], a[96:99]\n\t"
    "v_perm_b32 v23, v204, v200, s61\n\t"
    "v_mfma_f32_16x16x32_bf16 a[64:67], v[24:27], v[12:15], a[64:67]\n\t"
    "v_mfma_f32_16x16x32_bf16 a[128:131], v[32:35], v[12:15], a[128:131]\n\t"
    "v_perm_b32 v24, v181, v177, s60\n\t"
    "v_mfma_f32_16x16x32_bf16 a[160:163], v[36:39], v[12:15], a[160:163]\n\t"
    "v_mfma_f32_16x16x32_bf16 a[164:167], v[36:39], v[48:51], a[164:167]\n\t"
    "v_perm_b32 v28, v181, v177, s61\n\t"
    "v_mfma_f32_16x16x32_bf16 a[132:135], v[32:35], v[48:51], a[132:135]\n\t"
    "buffer_load_dwordx4 v[156:159], v4, s[96:99], s62 offen offset:3072\n\t"
    "v_mfma_f32_16x16x32_bf16 a[136:139], v[32:35], v[52:55], a[136:139]\n\t"
    "v_perm_b32 v25, v189, v185, s60\n\t"
    "v_mfma_f32_16x16x32_bf16 a[168:171], v[36:39], v[52:55], a[168:171]\n\t"
    "v_mfma_f32_16x16x32_bf16 a[172:175], v[36:39], v[56:59], a[172:175]\n\t"
    "v_perm_b32 v29, v189, v185, s61\n\t"
    "v_mfma_f32_16x16x32_bf16 a[140:143], v[32:35], v[56:59], a[140:143]\n\t"
    "v_mfma_f32_16x16x32_bf16 a[144:147], v[32:35], v[60:63], a[144:147]\n\t"
    "v_perm_b32 v26, v197, v193, s60\n\t"
    "v_mfma_f32_16x16x32_bf16 a[176:179], v[36:39], v[60:63], a[176:179]\n\t"
    "buffer_load_dwordx4 v[160:163], v5, s[96:99], s62 offen\n\t"
    "v_mfma_f32_16x16x32_bf16 a[180:183], v[36:39], v[64:67], a[180:183]\n\t"
    "v_perm_b32 v30, v197, v193, s61\n\t"
    "v_mfma_f32_16x16x32_bf16 a[148:151], v[32:35], v[64:67], a[148:151]\n\t"
    "v_mfma_f32_16x16x32_bf16 a[152:155], v[32:35], v[68:71], a[152:155]\n\t"
    "v_perm_b32 v27, v205, v201, s60\n\t"
    "v_mfma_f32_16x16x32_bf16 a[184:187], v[36:39], v[68:71], a[184:187]\n\t"
    "v_mfma_f32_16x16x32_bf16 a[188:191], v[36:39], v[72:75], a[188:191]\n\t"
    "v_perm_b32 v31, v205, v201, s61\n\t"
    "v_mfma_f32_16x16x32_bf16 a[156:159], v[32:35], v[72:75], a[156:159]\n\t"
    "buffer_load_dwordx4 v[164:167], v5, s[96:99], s62 offen offset:1024\n\t"
    "v_mfma_f32_16x16x32_bf16 a[220:223], v[40:43], v[72:75], a[220:223]\n\t"
    "v_perm_b32 v32, v182, v178, s60\n\t"
    "v_mfma_f32_16x16x32_bf16 a[252:255], v[44:47], v[72:75], a[252:255]\n\t"
    "v_mfma_f32_16x16x32_bf16 a[248:251], v[44:47], v[68:71], a[248:251]\n\t"
    "v_perm_b32 v36, v182, v178, s61\n\t"
    "v_mfma_f32_16x16x32_bf16 a[216:219], v[40:43], v[68:71], a[216:219]\n\t"
    "v_mfma_f32_16x16x32_bf16 a[212:215], v[40:43], v[64:67], a[212:215]\n\t"
    "v_perm_b32 v33, v190, v186, s60\n\t"
    "v_mfma_f32_16x16x32_bf16 a[244:247], v[44:47], v[64:67], a[244:247]\n\t"
    "buffer_load_dwordx4 v[168:171], v5, s[96:99], s62 offen offset:2048\n\t"
    "v_mfma_f32_16x16x32_bf16 a[240:243], v[44:47], v[60:63], a[240:243]\n\t"
    "v_perm_b32 v37, v190, v186, s61\n\t"
    "v_mfma_f32_16x16x32_bf16 a[208:211], v[40:43], v[60:63], a[208:211]\n\t"
    "v_mfma_f32_16x16x32_bf16 a[204:207], v[40:43], v[56:59], a[204:207]\n\t"
    "v_perm_b32 v34, v198, v194, s60\n\t"
    "v_mfma_f32_16x16x32_bf16 a[236:239], v[44:47], v[56:59], a[236:239]\n\t"
    "v_mfma_f32_16x16x32_bf16 a[232:235], v[44:47], v[52:55], a[232:235]\n\t"
    "v_perm_b32 v38, v198, v194, s61\n\t"
    "v_mfma_f32_16x16x32_bf16 a[200:203], v[40:43], v[52:55], a[200:203]\n\t"
    "buffer_load_dwordx4 v[172:175], v5, s[96:99], s62 offen offset:3072\n\t"
    "v_mfma_f32_16x16x32_bf16 a[196:199], v[40:43], v[48:51], a[196:199]\n\t"
    "v_perm_b32 v35, v206, v202, s60\n\t"
    "v_mfma_f32_16x16x32_bf16 a[228:231], v[44:47], v[48:51], a[228:231]\n\t"
    "v_mfma_f32_16x16x32_bf16 a[224:227], v[44:47], v[12:15], a[224:227]\n\t"
    "v_perm_b32 v39, v206, v202, s61\n\t"
    "v_mfma_f32_16x16x32_bf16 a[192:195], v[40:43], v[12:15], a[192:195]\n\t"
    "s_waitcnt lgkmcnt(0)\n\t"
    "v_mfma_f32_16x16x32_bf16 a[0:3], v[16:19], v[76:79], a[0:3]\n\t"
    "v_perm_b32 v40, v183, v179, s60\n\t"
    "v_mfma_f32_16x16x32_bf16 a[32:35], v[20:23], v[76:79], a[32:35]\n\t"
    "v_mfma_f32_16x16x32_bf16 a[36:39], v[20:23], v[12:15], a[36:39]\n\t"
    "v_perm_b32 v44, v183, v179, s61\n\t"
    "v_mfma_f32_16x16x32_bf16 a[4:7], v[16:19], v[12:15], a[4:7]\n\t"
    "ds_read_b128 v[72:75], v9 offset:35072\n\t"
    "v_mfma_f32_16x16x32_bf16 a[8:11], v[16:19], v[48:51], a[8:11]\n\t"
    "v_perm_b32 v41, v191, v187, s60\n\t"
    "v_mfma_f32_16x16x32_bf16 a[40:43], v[20:23], v[48:51], a[40:43]\n\t"
    "v_mfma_f32_16x16x32_bf16 a[44:47], v[20:23], v[52:55], a[44:47]\n\t"
    "v_perm_b32 v45, v191, v187, s61\n\t"
    "v_mfma_f32_16x16x32_bf16 a[12:15], v[16:19], v[52:55], a[12:15]\n\t"
    "v_mfma_f32_16x16x32_bf16 a[16:19], v[16:19], v[56:59], a[16:19]\n\t"
    "v_perm_b32 v42, v199, v195, s60\n\t"
    "v_mfma_f32_16x16x32_bf16 a[48:51], v[20:23], v[56:59], a[48:51]\n\t"
    "v_mfma_f32_16x16x32_bf16 a[52:55], v[20:23], v[60:63], a[52:55]\n\t"
    "v_perm_b32 v46, v199, v195, s61\n\t"
    "v_mfma_f32_16x16x32_bf16 a[20:23], v[16:19], v[60:63], a[20:23]\n\t"
    "v_mfma_f32_16x16x32_bf16 a[24:27], v[16:19], v[64:67], a[24:27]\n\t"
    "v_perm_b32 v43, v207, v203, s60\n\t"
    "v_mfma_f32_16x16x32_bf16 a[56:59], v[20:23], v[64:67], a[56:59]\n\t"
    "v_mfma_f32_16x16x32_bf16 a[60:63], v[20:23], v[68:71], a[60:63]\n\t"
    "v_perm_b32 v47, v207, v203, s61\n\t"
    "v_mfma_f32_16x16x32_bf16 a[28:31], v[16:19], v[68:71], a[28:31]\n\t"
    "s_waitcnt vmcnt(16)\n\t"
    "s_add_u32 s62, s63, 0x28000\n\t"
    "v_mfma_f32_16x16x32_bf16 a[92:95], v[24:27], v[68:71], a[92:95]\n\t"
    "v_perm_b32 v16, v212, v208, s60\n\t"
    "v_mfma_f32_16x16x32_bf16 a[124:127], v[28:31], v[68:71], a[124:127]\n\t"
    "buffer_load_dwordx4 v[176:179], v4, s[96:99], s62 offen\n\t"
    "v_mfma_f32_16x16x32_bf16 a[120:123], v[28:31], v[64:67], a[120:123]\n\t"
    "v_perm_b32 v20, v212, v208, s61\n\t"
    "v_mfma_f32_16x16x32_bf16 a[88:91], v[24:27], v[64:67], a[88:91]\n\t"
    "v_mfma_f32_16x16x32_bf16 a[84:87], v[24:27], v[60:63], a[84:87]\n\t"
    "v_perm_b32 v17, v220, v216, s60\n\t"
    "v_mfma_f32_16x16x32_bf16 a[116:119], v[28:31], v[60:63], a[116:119]\n\t"
    "v_mfma_f32_16x16x32_bf16 a[112:115], v[28:31], v[56:59], a[112:115]\n\t"
    "v_perm_b32 v21, v220, v216, s61\n\t"
    "v_mfma_f32_16x16x32_bf16 a[80:83], v[24:27], v[56:59], a[80:83]\n\t"
    "buffer_load_dwordx4 v[180:183], v4, s[96:99], s62 offen offset:1024\n\t"
    "v_mfma_f32_16x16x32_bf16 a[76:79], v[24:27], v[52:55], a[76:79]\n\t"
    "v_perm_b32 v18, v228, v224, s60\n\t"
    "v_mfma_f32_16x16x32_bf16 a[108:111], v[28:31], v[52:55], a[108:111]\n\t"
    "v_mfma_f32_16x16x32_bf16 a[104:107], v[28:31], v[48:51], a[104:107]\n\t"
    "v_perm_b32 v22, v228, v224, s61\n\t"
    "v_mfma_f32_16x16x32_bf16 a[72:75], v[24:27], v[48:51], a[72:75]\n\t"
    "v_mfma_f32_16x16x32_bf16 a[68:71], v[24:27], v[12:15], a[68:71]\n\t"
    "v_perm_b32 v19, v236, v232, s60\n\t"
    "v_mfma_f32_16x16x32_bf16 a[100:103], v[28:31], v[12:15], a[100:103]\n\t"
    "buffer_load_dwordx4 v[184:187], v4, s[96:99], s62 offen offset:2048\n\t"
    "v_mfma_f32_16x16x32_bf16 a[96:99], v[28:31], v[76:79], a[96:99]\n\t"
    "v_perm_b32 v23, v236, v232, s61\n\t"
    "v_mfma_f32_16x16x32_bf16 a[64:67], v[24:27], v[76:79], a[64:67]\n\t"
    "v_mfma_f32_16x16x32_bf16 a[128:131], v[32:35], v[76:79], a[128:131]\n\t"
    "v_perm_b32 v24, v213, v209, s60\n\t"
    "v_mfma_f32_16x16x32_bf16 a[160:163], v[36:39], v[76:79], a[160:163]\n\t"
    "v_mfma_f32_16x16x32_bf16 a[164:167], v[36:39], v[12:15], a[164:167]\n\t"
    "v_perm_b32 v28, v213, v209, s61\n\t"
    "v_mfma_f32_16x16x32_bf16 a[132:135], v[32:35], v[12:15], a[132:135]\n\t"
    "buffer_load_dwordx4 v[188:191], v4, s[96:99], s62 offen offset:3072\n\t"
    "v_mfma_f32_16x16x32_bf16 a[136:139], v[32:35], v[48:51], a[136:139]\n\t"
    "v_perm_b32 v25, v221, v217, s60\n\t"
    "v_mfma_f32_16x16x32_bf16 a[168:171], v[36:39], v[48:51], a[168:171]\n\t"
    "v_mfma_f32_16x16x32_bf16 a[172:175], v[36:39], v[52:55], a[172:175]\n\t"
    "v_perm_b32 v29, v221, v217, s61\n\t"
    "v_mfma_f32_16x16x32_bf16 a[140:143], v[32:35], v[52:55], a[140:143]\n\t"
    "v_mfma_f32_16x16x32_bf16 a[144:147], v[32:35], v[56:59], a[144:147]\n\t"
    "v_perm_b32 v26, v229, v225, s60\n\t"
    "v_mfma_f32_16x16x32_bf16 a[176:179], v[36:39], v[56:59], a[176:179]\n\t"
    "buffer_load_dwordx4 v[192:195], v5, s[96:99], s62 offen\n\t"
    "v_mfma_f32_16x16x32_bf16 a[180:183], v[36:39], v[60:63], a[180:183]\n\t"
    "v_perm_b32 v30, v229, v225, s61\n\t"
    "v_mfma_f32_16x16x32_bf16 a[148:151], v[32:35], v[60:63], a[148:151]\n\t"
    "v_mfma_f32_16x16x32_bf16 a[152:155], v[32:35], v[64:67], a[152:155]\n\t"
    "v_perm_b32 v27, v237, v233, s60\n\t"
    "v_mfma_f32_16x16x32_bf16 a[184:187], v[36:39], v[64:67], a[184:187]\n\t"
    "v_mfma_f32_16x16x32_bf16 a[188:191], v[36:39], v[68:71], a[188:191]\n\t"
    "v_perm_b32 v31, v237, v233, s61\n\t"
    "v_mfma_f32_16x16x32_bf16 a[156:159], v[32:35], v[68:71], a[156:159]\n\t"
    "buffer_load_dwordx4 v[196:199], v5, s[96:99], s62 offen offset:1024\n\t"
    "v_mfma_f32_16x16x32_bf16 a[220:223], v[40:43], v[68:71], a[220:223]\n\t"
    "v_perm_b32 v32, v214, v210, s60\n\t"
    "v_mfma_f32_16x16x32_bf16 a[252:255], v[44:47], v[68:71], a[252:255]\n\t"
    "v_mfma_f32_16x16x32_bf16 a[248:251], v[44:47], v[64:67], a[248:251]\n\t"
    "v_perm_b32 v36, v214, v210, s61\n\t"
    "v_mfma_f32_16x16x32_bf16 a[216:219], v[40:43], v[64:67], a[216:219]\n\t"
    "v_mfma_f32_16x16x32_bf16 a[212:215], v[40:43], v[60:63], a[212:215]\n\t"
    "v_perm_b32 v33, v222, v218, s60\n\t"
    "v_mfma_f32_16x16x32_bf16 a[244:247], v[44:47], v[60:63], a[244:247]\n\t"
    "buffer_load_dwordx4 v[200:203], v5, s[96:99], s62 offen offset:2048\n\t"
    "v_mfma_f32_16x16x32_bf16 a[240:243], v[44:47], v[56:59], a[240:243]\n\t"
    "v_perm_b32 v37, v222, v218, s61\n\t"
    "v_mfma_f32_16x16x32_bf16 a[208:211], v[40:43], v[56:59], a[208:211]\n\t"
    "v_mfma_f32_16x16x32_bf16 a[204:207], v[40:43], v[52:55], a[204:207]\n\t"
    "v_perm_b32 v34, v230, v226, s60\n\t"
    "v_mfma_f32_16x16x32_bf16 a[236:239], v[44:47], v[52:55], a[236:239]\n\t"
    "v_mfma_f32_16x16x32_bf16 a[232:235], v[44:47], v[48:51], a[232:235]\n\t"
    "v_perm_b32 v38, v230, v226, s61\n\t"
    "v_mfma_f32_16x16x32_bf16 a[200:203], v[40:43], v[48:51], a[200:203]\n\t"
    "buffer_load_dwordx4 v[204:207], v5, s[96:99], s62 offen offset:3072\n\t"
    "v_mfma_f32_16x16x32_bf16 a[196:199], v[40:43], v[12:15], a[196:199]\n\t"
    "v_perm_b32 v35, v238, v234, s60\n\t"
    "v_mfma_f32_16x16x32_bf16 a[228:231], v[44:47], v[12:15], a[228:231]\n\t"
    "v_mfma_f32_16x16x32_bf16 a[224:227], v[44:47], v[76:79], a[224:227]\n\t"
    "v_perm_b32 v39, v238, v234, s61\n\t"
    "v_mfma_f32_16x16x32_bf16 a[192:195], v[40:43], v[76:79], a[192:195]\n\t"
    "s_waitcnt lgkmcnt(0)\n\t"
    "v_mfma_f32_16x16x32_bf16 a[0:3], v[16:19], v[72:75], a[0:3]\n\t"
    "v_perm_b32 v40, v215, v211, s60\n\t"
    "v_mfma_f32_16x16x32_bf16 a[32:35], v[20:23], v[72:75], a[32:35]\n\t"
    "v_mfma_f32_16x16x32_bf16 a[36:39], v[20:23], v[76:79], a[36:39]\n\t"
    "v_perm_b32 v44, v215, v211, s61\n\t"
    "v_mfma_f32_16x16x32_bf16 a[4:7], v[16:19], v[76:79], a[4:7]\n\t"
    "ds_read_b128 v[68:71], v9 offset:34816\n\t"
    "v_mfma_f32_16x16x32_bf16 a[8:11], v[16:19], v[12:15], a[8:11]\n\t"
    "v_perm_b32 v41, v223, v219, s60\n\t"
    "v_mfma_f32_16x16x32_bf16 a[40:43], v[20:23], v[12:15], a[40:43]\n\t"
    "v_mfma_f32_16x16x32_bf16 a[44:47], v[20:23], v[48:51], a[44:47]\n\t"
    "v_perm_b32 v45, v223, v219, s61\n\t"
    "v_mfma_f32_16x16x32_bf16 a[12:15], v[16:19], v[48:51], a[12:15]\n\t"
    "v_mfma_f32_16x16x32_bf16 a[16:19], v[16:19], v[52:55], a[16:19]\n\t"
    "v_perm_b32 v42, v231, v227, s60\n\t"
    "v_mfma_f32_16x16x32_bf16 a[48:51], v[20:23], v[52:55], a[48:51]\n\t"
    "v_mfma_f32_16x16x32_bf16 a[52:55], v[20:23], v[56:59], a[52:55]\n\t"
    "v_perm_b32 v46, v231, v227, s61\n\t"
    "v_mfma_f32_16x16x32_bf16 a[20:23], v[16:19], v[56:59], a[20:23]\n\t"
    "ds_read_b128 v[80:83], v10 offset:35840\n\t"
    "v_mfma_f32_16x16x32_bf16 a[24:27], v[16:19], v[60:63], a[24:27]\n\t"
    "v_perm_b32 v43, v239, v235, s60\n\t"
    "v_mfma_f32_16x16x32_bf16 a[56:59], v[20:23], v[60:63], a[56:59]\n\t"
    "v_mfma_f32_16x16x32_bf16 a[60:63], v[20:23], v[64:67], a[60:63]\n\t"
    "v_perm_b32 v47, v239, v235, s61\n\t"
    "v_mfma_f32_16x16x32_bf16 a[28:31], v[16:19], v[64:67], a[28:31]\n\t"
    "s_waitcnt vmcnt(16)\n\t"
    "s_mov_b32 s62, 0xa8000\n\t"
    "v_mfma_f32_16x16x32_bf16 a[92:95], v[24:27], v[64:67], a[92:95]\n\t"
    "v_perm_b32 v16, v116, v112, s60\n\t"
    "v_mfma_f32_16x16x32_bf16 a[124:127], v[28:31], v[64:67], a[124:127]\n\t"
    "buffer_load_dwordx4 v[208:211], v4, s[96:99], s62 offen\n\t"
    "v_mfma_f32_16x16x32_bf16 a[120:123], v[28:31], v[60:63], a[120:123]\n\t"
    "v_perm_b32 v20, v116, v112, s61\n\t"
    "v_mfma_f32_16x16x32_bf16 a[88:91], v[24:27], v[60:63], a[88:91]\n\t"
    "ds_read_b128 v[84:87], v10 offset:36096\n\t"
    "v_mfma_f32_16x16x32_bf16 a[84:87], v[24:27], v[56:59], a[84:87]\n\t"
    "v_perm_b32 v17, v124, v120, s60\n\t"
    "v_mfma_f32_16x16x32_bf16 a[116:119], v[28:31], v[56:59], a[116:119]\n\t"
    "v_mfma_f32_16x16x32_bf16 a[112:115], v[28:31], v[52:55], a[112:115]\n\t"
    "v_perm_b32 v21, v124, v120, s61\n\t"
    "v_mfma_f32_16x16x32_bf16 a[80:83], v[24:27], v[52:55], a[80:83]\n\t"
    "buffer_load_dwordx4 v[212:215], v4, s[96:99], s62 offen offset:1024\n\t"
    "v_mfma_f32_16x16x32_bf16 a[76:79], v[24:27], v[48:51], a[76:79]\n\t"
    "v_perm_b32 v18, v132, v128, s60\n\t"
    "v_mfma_f32_16x16x32_bf16 a[108:111], v[28:31], v[48:51], a[108:111]\n\t"
    "v_mfma_f32_16x16x32_bf16 a[104:107], v[28:31], v[12:15], a[104:107]\n\t"
    "v_perm_b32 v22, v132, v128, s61\n\t"
    "v_mfma_f32_16x16x32_bf16 a[72:75], v[24:27], v[12:15], a[72:75]\n\t"
    "ds_read_b128 v[88:91], v10 offset:36352\n\t"
    "v_mfma_f32_16x16x32_bf16 a[68:71], v[24:27], v[76:79], a[68:71]\n\t"
    "v_perm_b32 v19, v140, v136, s60\n\t"
    "v_mfma_f32_16x16x32_bf16 a[100:103], v[28:31], v[76:79], a[100:103]\n\t"
    "buffer_load_dwordx4 v[216:219], v4, s[96:99], s62 offen offset:2048\n\t"
    "v_mfma_f32_16x16x32_bf16 a[96:99], v[28:31], v[72:75], a[96:99]\n\t"
    "v_perm_b32 v23, v140, v136, s61\n\t"
    "v_mfma_f32_16x16x32_bf16 a[64:67], v[24:27], v[72:75], a[64:67]\n\t"
    "v_mfma_f32_16x16x32_bf16 a[128:131], v[32:35], v[72:75], a[128:131]\n\t"
    "v_perm_b32 v24, v117, v113, s60\n\t"
    "v_mfma_f32_16x16x32_bf16 a[160:163], v[36:39], v[72:75], a[160:163]\n\t"
    "v_mfma_f32_16x16x32_bf16 a[164:167], v[36:39], v[76:79], a[164:167]\n\t"
    "v_perm_b32 v28, v117, v113, s61\n\t"
    "v_mfma_f32_16x16x32_bf16 a[132:135], v[32:35], v[76:79], a[132:135]\n\t"
    "buffer_load_dwordx4 v[220:223], v4, s[96:99], s62 offen offset:3072\n\t"
    "ds_read_b128 v[92:95], v10 offset:36608\n\t"
    "v_mfma_f32_16x16x32_bf16 a[136:139], v[32:35], v[12:15], a[136:139]\n\t"
    "v_perm_b32 v25, v125, v121, s60\n\t"
    "v_mfma_f32_16x16x32_bf16 a[168:171], v[36:39], v[12:15], a[168:171]\n\t"
    "v_mfma_f32_16x16x32_bf16 a[172:175], v[36:39], v[48:51], a[172:175]\n\t"
    "v_perm_b32 v29, v125, v121, s61\n\t"
    "v_mfma_f32_16x16x32_bf16 a[140:143], v[32:35], v[48:51], a[140:143]\n\t"
    "v_mfma_f32_16x16x32_bf16 a[144:147], v[32:35], v[52:55], a[144:147]\n\t"
    "v_perm_b32 v26, v133, v129, s60\n\t"
    "v_mfma_f32_16x16x32_bf16 a[176:179], v[36:39], v[52:55], a[176:179]\n\t"
    "buffer_load_dwordx4 v[224:227], v5, s[96:99], s62 offen\n\t"
    "v_mfma_f32_16x16x32_bf16 a[180:183], v[36:39], v[56:59], a[180:183]\n\t"
    "v_perm_b32 v30, v133, v129, s61\n\t"
    "v_mfma_f32_16x16x32_bf16 a[148:151], v[32:35], v[56:59], a[148:151]\n\t"
    "v_mfma_f32_16x16x32_bf16 a[152:155], v[32:35], v[60:63], a[152:155]\n\t"
    "v_perm_b32 v27, v141, v137, s60\n\t"
    "v_mfma_f32_16x16x32_bf16 a[184:187], v[36:39], v[60:63], a[184:187]\n\t"
    "v_mfma_f32_16x16x32_bf16 a[188:191], v[36:39], v[64:67], a[188:191]\n\t"
    "v_perm_b32 v31, v141, v137, s61\n\t"
    "v_mfma_f32_16x16x32_bf16 a[156:159], v[32:35], v[64:67], a[156:159]\n\t"
    "buffer_load_dwordx4 v[228:231], v5, s[96:99], s62 offen offset:1024\n\t"
    "v_mfma_f32_16x16x32_bf16 a[220:223], v[40:43], v[64:67], a[220:223]\n\t"
    "v_perm_b32 v32, v118, v114, s60\n\t"
    "v_mfma_f32_16x16x32_bf16 a[252:255], v[44:47], v[64:67], a[252:255]\n\t"
    "v_mfma_f32_16x16x32_bf16 a[248:251], v[44:47], v[60:63], a[248:251]\n\t"
    "v_perm_b32 v36, v118, v114, s61\n\t"
    "v_mfma_f32_16x16x32_bf16 a[216:219], v[40:43], v[60:63], a[216:219]\n\t"
    "v_mfma_f32_16x16x32_bf16 a[212:215], v[40:43], v[56:59], a[212:215]\n\t"
    "v_perm_b32 v33, v126, v122, s60\n\t"
    "v_mfma_f32_16x16x32_bf16 a[244:247], v[44:47], v[56:59], a[244:247]\n\t"
    "buffer_load_dwordx4 v[232:235], v5, s[96:99], s62 offen offset:2048\n\t"
    "v_mfma_f32_16x16x32_bf16 a[240:243], v[44:47], v[52:55], a[240:243]\n\t"
    "v_perm_b32 v37, v126, v122, s61\n\t"
    "v_mfma_f32_16x16x32_bf16 a[208:211], v[40:43], v[52:55], a[208:211]\n\t"
    "v_mfma_f32_16x16x32_bf16 a[204:207], v[40:43], v[48:51], a[204:207]\n\t"
    "v_perm_b32 v34, v134, v130, s60\n\t"
    "v_mfma_f32_16x16x32_bf16 a[236:239], v[44:47], v[48:51], a[236:239]\n\t"
    "v_mfma_f32_16x16x32_bf16 a[232:235], v[44:47], v[12:15], a[232:235]\n\t"
    "v_perm_b32 v38, v134, v130, s61\n\t"
    "v_mfma_f32_16x16x32_bf16 a[200:203], v[40:43], v[12:15], a[200:203]\n\t"
    "buffer_load_dwordx4 v[236:239], v5, s[96:99], s62 offen offset:3072\n\t"
    "v_mfma_f32_16x16x32_bf16 a[196:199], v[40:43], v[76:79], a[196:199]\n\t"
    "v_perm_b32 v35, v142, v138, s60\n\t"
    "v_mfma_f32_16x16x32_bf16 a[228:231], v[44:47], v[76:79], a[228:231]\n\t"
    "v_mfma_f32_16x16x32_bf16 a[224:227], v[44:47], v[72:75], a[224:227]\n\t"
    "v_perm_b32 v39, v142, v138, s61\n\t"
    "v_mfma_f32_16x16x32_bf16 a[192:195], v[40:43], v[72:75], a[192:195]\n\t"
    "s_waitcnt lgkmcnt(0)\n\t"
    "v_mfma_f32_16x16x32_bf16 a[0:3], v[16:19], v[68:71], a[0:3]\n\t"
    "v_perm_b32 v40, v119, v115, s60\n\t"
    "v_mfma_f32_16x16x32_bf16 a[32:35], v[20:23], v[68:71], a[32:35]\n\t"
    "v_mfma_f32_16x16x32_bf16 a[36:39], v[20:23], v[72:75], a[36:39]\n\t"
    "v_perm_b32 v44, v119, v115, s61\n\t"
    "v_mfma_f32_16x16x32_bf16 a[4:7], v[16:19], v[72:75], a[4:7]\n\t"
    "v_mfma_f32_16x16x32_bf16 a[8:11], v[16:19], v[76:79], a[8:11]\n\t"
    "v_perm_b32 v41, v127, v123, s60\n\t"
    "v_mfma_f32_16x16x32_bf16 a[40:43], v[20:23], v[76:79], a[40:43]\n\t"
    "v_mfma_f32_16x16x32_bf16 a[44:47], v[20:23], v[12:15], a[44:47]\n\t"
    "v_perm_b32 v45, v127, v123, s61\n\t"
    "v_mfma_f32_16x16x32_bf16 a[12:15], v[16:19], v[12:15], a[12:15]\n\t"
    "v_mfma_f32_16x16x32_bf16 a[16:19], v[16:19], v[48:51], a[16:19]\n\t"
    "v_perm_b32 v42, v135, v131, s60\n\t"
    "v_mfma_f32_16x16x32_bf16 a[48:51], v[20:23], v[48:51], a[48:51]\n\t"
    "v_mfma_f32_16x16x32_bf16 a[52:55], v[20:23], v[52:55], a[52:55]\n\t"
    "v_perm_b32 v46, v135, v131, s61\n\t"
    "v_mfma_f32_16x16x32_bf16 a[20:23], v[16:19], v[52:55], a[20:23]\n\t"
    "ds_read_b128 v[96:99], v10 offset:36864\n\t"
    "v_mfma_f32_16x16x32_bf16 a[24:27], v[16:19], v[56:59], a[24:27]\n\t"
    "v_perm_b32 v43, v143, v139, s60\n\t"
    "v_mfma_f32_16x16x32_bf16 a[56:59], v[20:23], v[56:59], a[56:59]\n\t"
    "v_mfma_f32_16x16x32_bf16 a[60:63], v[20:23], v[60:63], a[60:63]\n\t"
    "v_perm_b32 v47, v143, v139, s61\n\t"
    "v_mfma_f32_16x16x32_bf16 a[28:31], v[16:19], v[60:63], a[28:31]\n\t"
    "s_waitcnt vmcnt(16)\n\t"
    "s_add_u32 s62, s70, 0x28000\n\t"
    "v_mfma_f32_16x16x32_bf16 a[92:95], v[24:27], v[60:63], a[92:95]\n\t"
    "v_perm_b32 v16, v148, v144, s60\n\t"
    "v_mfma_f32_16x16x32_bf16 a[124:127], v[28:31], v[60:63], a[124:127]\n\t"
    "buffer_load_dwordx4 v[112:115], v4, s[96:99], s62 offen\n\t"
    "v_mfma_f32_16x16x32_bf16 a[120:123], v[28:31], v[56:59], a[120:123]\n\t"
    "v_perm_b32 v20, v148, v144, s61\n\t"
    "v_mfma_f32_16x16x32_bf16 a[88:91], v[24:27], v[56:59], a[88:91]\n\t"
    "ds_read_b128 v[100:103], v10 offset:37120\n\t"
    "v_mfma_f32_16x16x32_bf16 a[84:87], v[24:27], v[52:55], a[84:87]\n\t"
    "v_perm_b32 v17, v156, v152, s60\n\t"
    "v_mfma_f32_16x16x32_bf16 a[116:119], v[28:31], v[52:55], a[116:119]\n\t"
    "v_mfma_f32_16x16x32_bf16 a[112:115], v[28:31], v[48:51], a[112:115]\n\t"
    "v_perm_b32 v21, v156, v152, s61\n\t"
    "v_mfma_f32_16x16x32_bf16 a[80:83], v[24:27], v[48:51], a[80:83]\n\t"
    "buffer_load_dwordx4 v[116:119], v4, s[96:99], s62 offen offset:1024\n\t"
    "v_mfma_f32_16x16x32_bf16 a[76:79], v[24:27], v[12:15], a[76:79]\n\t"
    "v_perm_b32 v18, v164, v160, s60\n\t"
    "v_mfma_f32_16x16x32_bf16 a[108:111], v[28:31], v[12:15], a[108:111]\n\t"
    "v_mfma_f32_16x16x32_bf16 a[104:107], v[28:31], v[76:79], a[104:107]\n\t"
    "v_perm_b32 v22, v164, v160, s61\n\t"
    "v_mfma_f32_16x16x32_bf16 a[72:75], v[24:27], v[76:79], a[72:75]\n\t"
    "ds_read_b128 v[104:107], v10 offset:37376\n\t"
    "v_mfma_f32_16x16x32_bf16 a[68:71], v[24:27], v[72:75], a[68:71]\n\t"
    "v_perm_b32 v19, v172, v168, s60\n\t"
    "v_mfma_f32_16x16x32_bf16 a[100:103], v[28:31], v[72:75], a[100:103]\n\t"
    "buffer_load_dwordx4 v[120:123], v4, s[96:99], s62 offen offset:2048\n\t"
    "v_mfma_f32_16x16x32_bf16 a[96:99], v[28:31], v[68:71], a[96:99]\n\t"
    "v_perm_b32 v23, v172, v168, s61\n\t"
    "v_mfma_f32_16x16x32_bf16 a[64:67], v[24:27], v[68:71], a[64:67]\n\t"
    "v_mfma_f32_16x16x32_bf16 a[128:131], v[32:35], v[68:71], a[128:131]\n\t"
    "v_perm_b32 v24, v149, v145, s60\n\t"
    "v_mfma_f32_16x16x32_bf16 a[160:163], v[36:39], v[68:71], a[160:163]\n\t"
    "v_mfma_f32_16x16x32_bf16 a[164:167], v[36:39], v[72:75], a[164:167]\n\t"
    "v_perm_b32 v28, v149, v145, s61\n\t"
    "v_mfma_f32_16x16x32_bf16 a[132:135], v[32:35], v[72:75], a[132:135]\n\t"
    "buffer_load_dwordx4 v[124:127], v4, s[96:99], s62 offen offset:3072\n\t"
    "ds_read_b128 v[108:111], v10 offset:37632\n\t"
    "v_mfma_f32_16x16x32_bf16 a[136:139], v[32:35], v[76:79], a[136:139]\n\t"
    "v_perm_b32 v25, v157, v153, s60\n\t"
    "v_mfma_f32_16x16x32_bf16 a[168:171], v[36:39], v[76:79], a[168:171]\n\t"
    "v_mfma_f32_16x16x32_bf16 a[172:175], v[36:39], v[12:15], a[172:175]\n\t"
    "v_perm_b32 v29, v157, v153, s61\n\t"
    "v_mfma_f32_16x16x32_bf16 a[140:143], v[32:35], v[12:15], a[140:143]\n\t"
    "v_mfma_f32_16x16x32_bf16 a[144:147], v[32:35], v[48:51], a[144:147]\n\t"
    "v_perm_b32 v26, v165, v161, s60\n\t"
    "v_mfma_f32_16x16x32_bf16 a[176:179], v[36:39], v[48:51], a[176:179]\n\t"
    "buffer_load_dwordx4 v[128:131], v5, s[96:99], s62 offen\n\t"
    "v_mfma_f32_16x16x32_bf16 a[180:183], v[36:39], v[52:55], a[180:183]\n\t"
    "v_perm_b32 v30, v165, v161, s61\n\t"
    "v_mfma_f32_16x16x32_bf16 a[148:151], v[32:35], v[52:55], a[148:151]\n\t"
    "v_mfma_f32_16x16x32_bf16 a[152:155], v[32:35], v[56:59], a[152:155]\n\t"
    "v_perm_b32 v27, v173, v169, s60\n\t"
    "v_mfma_f32_16x16x32_bf16 a[184:187], v[36:39], v[56:59], a[184:187]\n\t"
    "v_mfma_f32_16x16x32_bf16 a[188:191], v[36:39], v[60:63], a[188:191]\n\t"
    "v_perm_b32 v31, v173, v169, s61\n\t"
    "v_mfma_f32_16x16x32_bf16 a[156:159], v[32:35], v[60:63], a[156:159]\n\t"
    "buffer_load_dwordx4 v[132:135], v5, s[96:99], s62 offen offset:1024\n\t"
    "v_mfma_f32_16x16x32_bf16 a[220:223], v[40:43], v[60:63], a[220:223]\n\t"
    "v_perm_b32 v32, v150, v146, s60\n\t"
    "v_mfma_f32_16x16x32_bf16 a[252:255], v[44:47], v[60:63], a[252:255]\n\t"
    "v_mfma_f32_16x16x32_bf16 a[248:251], v[44:47], v[56:59], a[248:251]\n\t"
    "v_perm_b32 v36, v150, v146, s61\n\t"
    "v_mfma_f32_16x16x32_bf16 a[216:219], v[40:43], v[56:59], a[216:219]\n\t"
    "v_mfma_f32_16x16x32_bf16 a[212:215], v[40:43], v[52:55], a[212:215]\n\t"
    "v_perm_b32 v33, v158, v154, s60\n\t"
    "v_mfma_f32_16x16x32_bf16 a[244:247], v[44:47], v[52:55], a[244:247]\n\t"
    "buffer_load_dwordx4 v[136:139], v5, s[96:99], s62 offen offset:2048\n\t"
    "v_mfma_f32_16x16x32_bf16 a[240:243], v[44:47], v[48:51], a[240:243]\n\t"
    "v_perm_b32 v37, v158, v154, s61\n\t"
    "v_mfma_f32_16x16x32_bf16 a[208:211], v[40:43], v[48:51], a[208:211]\n\t"
    "v_mfma_f32_16x16x32_bf16 a[204:207], v[40:43], v[12:15], a[204:207]\n\t"
    "v_perm_b32 v34, v166, v162, s60\n\t"
    "v_mfma_f32_16x16x32_bf16 a[236:239], v[44:47], v[12:15], a[236:239]\n\t"
    "v_mfma_f32_16x16x32_bf16 a[232:235], v[44:47], v[76:79], a[232:235]\n\t"
    "v_perm_b32 v38, v166, v162, s61\n\t"
    "v_mfma_f32_16x16x32_bf16 a[200:203], v[40:43], v[76:79], a[200:203]\n\t"
    "buffer_load_dwordx4 v[140:143], v5, s[96:99], s62 offen offset:3072\n\t"
    "v_mfma_f32_16x16x32_bf16 a[196:199], v[40:43], v[72:75], a[196:199]\n\t"
    "v_perm_b32 v35, v174, v170, s60\n\t"
    "v_mfma_f32_16x16x32_bf16 a[228:231], v[44:47], v[72:75], a[228:231]\n\t"
    "v_mfma_f32_16x16x32_bf16 a[224:227], v[44:47], v[68:71], a[224:227]\n\t"
    "v_perm_b32 v39, v174, v170, s61\n\t"
    "v_mfma_f32_16x16x32_bf16 a[192:195], v[40:43], v[68:71], a[192:195]\n\t"
    "s_waitcnt lgkmcnt(0)\n\t"
    "v_mfma_f32_16x16x32_bf16 a[0:3], v[16:19], v[80:83], a[0:3]\n\t"
    "v_perm_b32 v40, v151, v147, s60\n\t"
    "v_mfma_f32_16x16x32_bf16 a[32:35], v[20:23], v[80:83], a[32:35]\n\t"
    "v_mfma_f32_16x16x32_bf16 a[36:39], v[20:23], v[84:87], a[36:39]\n\t"
    "v_perm_b32 v44, v151, v147, s61\n\t"
    "v_mfma_f32_16x16x32_bf16 a[4:7], v[16:19], v[84:87], a[4:7]\n\t"
    "ds_read_b128 v[12:15], v11 offset:35584\n\t"
    "v_mfma_f32_16x16x32_bf16 a[8:11], v[16:19], v[88:91], a[8:11]\n\t"
    "v_perm_b32 v41, v159, v155, s60\n\t"
    "v_mfma_f32_16x16x32_bf16 a[40:43], v[20:23], v[88:91], a[40:43]\n\t"
    "v_mfma_f32_16x16x32_bf16 a[44:47], v[20:23], v[92:95], a[44:47]\n\t"
    "v_perm_b32 v45, v159, v155, s61\n\t"
    "v_mfma_f32_16x16x32_bf16 a[12:15], v[16:19], v[92:95], a[12:15]\n\t"
    "v_mfma_f32_16x16x32_bf16 a[16:19], v[16:19], v[96:99], a[16:19]\n\t"
    "v_perm_b32 v42, v167, v163, s60\n\t"
    "v_mfma_f32_16x16x32_bf16 a[48:51], v[20:23], v[96:99], a[48:51]\n\t"
    "v_mfma_f32_16x16x32_bf16 a[52:55], v[20:23], v[100:103], a[52:55]\n\t"
    "v_perm_b32 v46, v167, v163, s61\n\t"
    "v_mfma_f32_16x16x32_bf16 a[20:23], v[16:19], v[100:103], a[20:23]\n\t"
    "v_mfma_f32_16x16x32_bf16 a[24:27], v[16:19], v[104:107], a[24:27]\n\t"
    "v_perm_b32 v43, v175, v171, s60\n\t"
    "v_mfma_f32_16x16x32_bf16 a[56:59], v[20:23], v[104:107], a[56:59]\n\t"
    "v_mfma_f32_16x16x32_bf16 a[60:63], v[20:23], v[108:111], a[60:63]\n\t"
    "v_perm_b32 v47, v175, v171, s61\n\t"
    "v_mfma_f32_16x16x32_bf16 a[28:31], v[16:19], v[108:111], a[28:31]\n\t"
    "s_waitcnt vmcnt(16)\n\t"
    "s_add_u32 s62, s72, 0x28000\n\t"
    "v_mfma_f32_16x16x32_bf16 a[92:95], v[24:27], v[108:111], a[92:95]\n\t"
    "v_perm_b32 v16, v180, v176, s60\n\t"
    "v_mfma_f32_16x16x32_bf16 a[124:127], v[28:31], v[108:111], a[124:127]\n\t"
    "buffer_load_dwordx4 v[144:147], v4, s[96:99], s62 offen\n\t"
    "v_mfma_f32_16x16x32_bf16 a[120:123], v[28:31], v[104:107], a[120:123]\n\t"
    "v_perm_b32 v20, v180, v176, s61\n\t"
    "v_mfma_f32_16x16x32_bf16 a[88:91], v[24:27], v[104:107], a[88:91]\n\t"
    "v_mfma_f32_16x16x32_bf16 a[84:87], v[24:27], v[100:103], a[84:87]\n\t"
    "v_perm_b32 v17, v188, v184, s60\n\t"
    "v_mfma_f32_16x16x32_bf16 a[116:119], v[28:31], v[100:103], a[116:119]\n\t"
    "v_mfma_f32_16x16x32_bf16 a[112:115], v[28:31], v[96:99], a[112:115]\n\t"
    "v_perm_b32 v21, v188, v184, s61\n\t"
    "v_mfma_f32_16x16x32_bf16 a[80:83], v[24:27], v[96:99], a[80:83]\n\t"
    "buffer_load_dwordx4 v[148:151], v4, s[96:99], s62 offen offset:1024\n\t"
    "v_mfma_f32_16x16x32_bf16 a[76:79], v[24:27], v[92:95], a[76:79]\n\t"
    "v_perm_b32 v18, v196, v192, s60\n\t"
    "v_mfma_f32_16x16x32_bf16 a[108:111], v[28:31], v[92:95], a[108:111]\n\t"
    "v_mfma_f32_16x16x32_bf16 a[104:107], v[28:31], v[88:91], a[104:107]\n\t"
    "v_perm_b32 v22, v196, v192, s61\n\t"
    "v_mfma_f32_16x16x32_bf16 a[72:75], v[24:27], v[88:91], a[72:75]\n\t"
    "v_mfma_f32_16x16x32_bf16 a[68:71], v[24:27], v[84:87], a[68:71]\n\t"
    "v_perm_b32 v19, v204, v200, s60\n\t"
    "v_mfma_f32_16x16x32_bf16 a[100:103], v[28:31], v[84:87], a[100:103]\n\t"
    "buffer_load_dwordx4 v[152:155], v4, s[96:99], s62 offen offset:2048\n\t"
    "v_mfma_f32_16x16x32_bf16 a[96:99], v[28:31], v[80:83], a[96:99]\n\t"
    "v_perm_b32 v23, v204, v200, s61\n\t"
    "v_mfma_f32_16x16x32_bf16 a[64:67], v[24:27], v[80:83], a[64:67]\n\t"
    "v_mfma_f32_16x16x32_bf16 a[128:131], v[32:35], v[80:83], a[128:131]\n\t"
    "v_perm_b32 v24, v181, v177, s60\n\t"
    "v_mfma_f32_16x16x32_bf16 a[160:163], v[36:39], v[80:83], a[160:163]\n\t"
    "v_mfma_f32_16x16x32_bf16 a[164:167], v[36:39], v[84:87], a[164:167]\n\t"
    "v_perm_b32 v28, v181, v177, s61\n\t"
    "v_mfma_f32_16x16x32_bf16 a[132:135], v[32:35], v[84:87], a[132:135]\n\t"
    "buffer_load_dwordx4 v[156:159], v4, s[96:99], s62 offen offset:3072\n\t"
    "v_mfma_f32_16x16x32_bf16 a[136:139], v[32:35], v[88:91], a[136:139]\n\t"
    "v_perm_b32 v25, v189, v185, s60\n\t"
    "v_mfma_f32_16x16x32_bf16 a[168:171], v[36:39], v[88:91], a[168:171]\n\t"
    "v_mfma_f32_16x16x32_bf16 a[172:175], v[36:39], v[92:95], a[172:175]\n\t"
    "v_perm_b32 v29, v189, v185, s61\n\t"
    "v_mfma_f32_16x16x32_bf16 a[140:143], v[32:35], v[92:95], a[140:143]\n\t"
    "v_mfma_f32_16x16x32_bf16 a[144:147], v[32:35], v[96:99], a[144:147]\n\t"
    "v_perm_b32 v26, v197, v193, s60\n\t"
    "v_mfma_f32_16x16x32_bf16 a[176:179], v[36:39], v[96:99], a[176:179]\n\t"
    "buffer_load_dwordx4 v[160:163], v5, s[96:99], s62 offen\n\t"
    "v_mfma_f32_16x16x32_bf16 a[180:183], v[36:39], v[100:103], a[180:183]\n\t"
    "v_perm_b32 v30, v197, v193, s61\n\t"
    "v_mfma_f32_16x16x32_bf16 a[148:151], v[32:35], v[100:103], a[148:151]\n\t"
    "v_mfma_f32_16x16x32_bf16 a[152:155], v[32:35], v[104:107], a[152:155]\n\t"
    "v_perm_b32 v27, v205, v201, s60\n\t"
    "v_mfma_f32_16x16x32_bf16 a[184:187], v[36:39], v[104:107], a[184:187]\n\t"
    "v_mfma_f32_16x16x32_bf16 a[188:191], v[36:39], v[108:111], a[188:191]\n\t"
    "v_perm_b32 v31, v205, v201, s61\n\t"
    "v_mfma_f32_16x16x32_bf16 a[156:159], v[32:35], v[108:111], a[156:159]\n\t"
    "buffer_load_dwordx4 v[164:167], v5, s[96:99], s62 offen offset:1024\n\t"
    "v_mfma_f32_16x16x32_bf16 a[220:223], v[40:43], v[108:111], a[220:223]\n\t"
    "v_perm_b32 v32, v182, v178, s60\n\t"
    "v_mfma_f32_16x16x32_bf16 a[252:255], v[44:47], v[108:111], a[252:255]\n\t"
    "v_mfma_f32_16x16x32_bf16 a[248:251], v[44:47], v[104:107], a[248:251]\n\t"
    "v_perm_b32 v36, v182, v178, s61\n\t"
    "v_mfma_f32_16x16x32_bf16 a[216:219], v[40:43], v[104:107], a[216:219]\n\t"
    "v_mfma_f32_16x16x32_bf16 a[212:215], v[40:43], v[100:103], a[212:215]\n\t"
    "v_perm_b32 v33, v190, v186, s60\n\t"
    "v_mfma_f32_16x16x32_bf16 a[244:247], v[44:47], v[100:103], a[244:247]\n\t"
    "buffer_load_dwordx4 v[168:171], v5, s[96:99], s62 offen offset:2048\n\t"
    "v_mfma_f32_16x16x32_bf16 a[240:243], v[44:47], v[96:99], a[240:243]\n\t"
    "v_perm_b32 v37, v190, v186, s61\n\t"
    "v_mfma_f32_16x16x32_bf16 a[208:211], v[40:43], v[96:99], a[208:211]\n\t"
    "v_mfma_f32_16x16x32_bf16 a[204:207], v[40:43], v[92:95], a[204:207]\n\t"
    "v_perm_b32 v34, v198, v194, s60\n\t"
    "v_mfma_f32_16x16x32_bf16 a[236:239], v[44:47], v[92:95], a[236:239]\n\t"
    "v_mfma_f32_16x16x32_bf16 a[232:235], v[44:47], v[88:91], a[232:235]\n\t"
    "v_perm_b32 v38, v198, v194, s61\n\t"
    "v_mfma_f32_16x16x32_bf16 a[200:203], v[40:43], v[88:91], a[200:203]\n\t"
    "buffer_load_dwordx4 v[172:175], v5, s[96:99], s62 offen offset:3072\n\t"
    "v_mfma_f32_16x16x32_bf16 a[196:199], v[40:43], v[84:87], a[196:199]\n\t"
    "v_perm_b32 v35, v206, v202, s60\n\t"
    "v_mfma_f32_16x16x32_bf16 a[228:231], v[44:47], v[84:87], a[228:231]\n\t"
    "v_mfma_f32_16x16x32_bf16 a[224:227], v[44:47], v[80:83], a[224:227]\n\t"
    "v_perm_b32 v39, v206, v202, s61\n\t"
    "v_mfma_f32_16x16x32_bf16 a[192:195], v[40:43], v[80:83], a[192:195]\n\t"
    "s_waitcnt lgkmcnt(0)\n\t"
    "v_mfma_f32_16x16x32_bf16 a[0:3], v[16:19], v[12:15], a[0:3]\n\t"
    "v_perm_b32 v40, v183, v179, s60\n\t"
    "v_mfma_f32_16x16x32_bf16 a[32:35], v[20:23], v[12:15], a[32:35]\n\t"
    "v_mfma_f32_16x16x32_bf16 a[36:39], v[20:23], v[80:83], a[36:39]\n\t"
    "v_perm_b32 v44, v183, v179, s61\n\t"
    "v_mfma_f32_16x16x32_bf16 a[4:7], v[16:19], v[80:83], a[4:7]\n\t"
    "ds_read_b128 v[108:111], v11 offset:35328\n\t"
    "v_mfma_f32_16x16x32_bf16 a[8:11], v[16:19], v[84:87], a[8:11]\n\t"
    "v_perm_b32 v41, v191, v187, s60\n\t"
    "v_mfma_f32_16x16x32_bf16 a[40:43], v[20:23], v[84:87], a[40:43]\n\t"
    "v_mfma_f32_16x16x32_bf16 a[44:47], v[20:23], v[88:91], a[44:47]\n\t"
    "v_perm_b32 v45, v191, v187, s61\n\t"
    "v_mfma_f32_16x16x32_bf16 a[12:15], v[16:19], v[88:91], a[12:15]\n\t"
    "v_mfma_f32_16x16x32_bf16 a[16:19], v[16:19], v[92:95], a[16:19]\n\t"
    "v_perm_b32 v42, v199, v195, s60\n\t"
    "v_mfma_f32_16x16x32_bf16 a[48:51], v[20:23], v[92:95], a[48:51]\n\t"
    "v_mfma_f32_16x16x32_bf16 a[52:55], v[20:23], v[96:99], a[52:55]\n\t"
    "v_perm_b32 v46, v199, v195, s61\n\t"
    "v_mfma_f32_16x16x32_bf16 a[20:23], v[16:19], v[96:99], a[20:23]\n\t"
    "v_mfma_f32_16x16x32_bf16 a[24:27], v[16:19], v[100:103], a[24:27]\n\t"
    "v_perm_b32 v43, v207, v203, s60\n\t"
    "v_mfma_f32_16x16x32_bf16 a[56:59], v[20:23], v[100:103], a[56:59]\n\t"
    "v_mfma_f32_16x16x32_bf16 a[60:63], v[20:23], v[104:107], a[60:63]\n\t"
    "v_perm_b32 v47, v207, v203, s61\n\t"
    "v_mfma_f32_16x16x32_bf16 a[28:31], v[16:19], v[104:107], a[28:31]\n\t"
    "s_waitcnt vmcnt(16)\n\t"
    "s_add_u32 s62, %[tb], 0x30000\n\t"
    "v_mfma_f32_16x16x32_bf16 a[92:95], v[24:27], v[104:107], a[92:95]\n\t"
    "v_perm_b32 v16, v212, v208, s60\n\t"
    "v_mfma_f32_16x16x32_bf16 a[124:127], v[28:31], v[104:107], a[124:127]\n\t"
    "buffer_load_dwordx4 v[176:179], v4, s[96:99], s62 offen\n\t"
    "v_mfma_f32_16x16x32_bf16 a[120:123], v[28:31], v[100:103], a[120:123]\n\t"
    "v_perm_b32 v20, v212, v208, s61\n\t"
    "v_mfma_f32_16x16x32_bf16 a[88:91], v[24:27], v[100:103], a[88:91]\n\t"
    "v_mfma_f32_16x16x32_bf16 a[84:87], v[24:27], v[96:99], a[84:87]\n\t"
    "v_perm_b32 v17, v220, v216, s60\n\t"
    "v_mfma_f32_16x16x32_bf16 a[116:119], v[28:31], v[96:99], a[116:119]\n\t"
    "v_mfma_f32_16x16x32_bf16 a[112:115], v[28:31], v[92:95], a[112:115]\n\t"
    "v_perm_b32 v21, v220, v216, s61\n\t"
    "v_mfma_f32_16x16x32_bf16 a[80:83], v[24:27], v[92:95], a[80:83]\n\t"
    "buffer_load_dwordx4 v[180:183], v4, s[96:99], s62 offen offset:1024\n\t"
    "v_mfma_f32_16x16x32_bf16 a[76:79], v[24:27], v[88:91], a[76:79]\n\t"
    "v_perm_b32 v18, v228, v224, s60\n\t"
    "v_mfma_f32_16x16x32_bf16 a[108:111], v[28:31], v[88:91], a[108:111]\n\t"
    "v_mfma_f32_16x16x32_bf16 a[104:107], v[28:31], v[84:87], a[104:107]\n\t"
    "v_perm_b32 v22, v228, v224, s61\n\t"
    "v_mfma_f32_16x16x32_bf16 a[72:75], v[24:27], v[84:87], a[72:75]\n\t"
    "v_mfma_f32_16x16x32_bf16 a[68:71], v[24:27], v[80:83], a[68:71]\n\t"
    "v_perm_b32 v19, v236, v232, s60\n\t"
    "v_mfma_f32_16x16x32_bf16 a[100:103], v[28:31], v[80:83], a[100:103]\n\t"
    "buffer_load_dwordx4 v[184:187], v4, s[96:99], s62 offen offset:2048\n\t"
    "v_mfma_f32_16x16x32_bf16 a[96:99], v[28:31], v[12:15], a[96:99]\n\t"
    "v_perm_b32 v23, v236, v232, s61\n\t"
    "v_mfma_f32_16x16x32_bf16 a[64:67], v[24:27], v[12:15], a[64:67]\n\t"
    "v_mfma_f32_16x16x32_bf16 a[128:131], v[32:35], v[12:15], a[128:131]\n\t"
    "v_perm_b32 v24, v213, v209, s60\n\t"
    "v_mfma_f32_16x16x32_bf16 a[160:163], v[36:39], v[12:15], a[160:163]\n\t"
    "v_mfma_f32_16x16x32_bf16 a[164:167], v[36:39], v[80:83], a[164:167]\n\t"
    "v_perm_b32 v28, v213, v209, s61\n\t"
    "v_mfma_f32_16x16x32_bf16 a[132:135], v[32:35], v[80:83], a[132:135]\n\t"
    "buffer_load_dwordx4 v[188:191], v4, s[96:99], s62 offen offset:3072\n\t"
    "v_mfma_f32_16x16x32_bf16 a[136:139], v[32:35], v[84:87], a[136:139]\n\t"
    "v_perm_b32 v25, v221, v217, s60\n\t"
    "v_mfma_f32_16x16x32_bf16 a[168:171], v[36:39], v[84:87], a[168:171]\n\t"
    "v_mfma_f32_16x16x32_bf16 a[172:175], v[36:39], v[88:91], a[172:175]\n\t"
    "v_perm_b32 v29, v221, v217, s61\n\t"
    "v_mfma_f32_16x16x32_bf16 a[140:143], v[32:35], v[88:91], a[140:143]\n\t"
    "v_mfma_f32_16x16x32_bf16 a[144:147], v[32:35], v[92:95], a[144:147]\n\t"
    "v_perm_b32 v26, v229, v225, s60\n\t"
    "v_mfma_f32_16x16x32_bf16 a[176:179], v[36:39], v[92:95], a[176:179]\n\t"
    "buffer_load_dwordx4 v[192:195], v5, s[96:99], s62 offen\n\t"
    "v_mfma_f32_16x16x32_bf16 a[180:183], v[36:39], v[96:99], a[180:183]\n\t"
    "v_perm_b32 v30, v229, v225, s61\n\t"
    "v_mfma_f32_16x16x32_bf16 a[148:151], v[32:35], v[96:99], a[148:151]\n\t"
    "v_mfma_f32_16x16x32_bf16 a[152:155], v[32:35], v[100:103], a[152:155]\n\t"
    "v_perm_b32 v27, v237, v233, s60\n\t"
    "v_mfma_f32_16x16x32_bf16 a[184:187], v[36:39], v[100:103], a[184:187]\n\t"
    "v_mfma_f32_16x16x32_bf16 a[188:191], v[36:39], v[104:107], a[188:191]\n\t"
    "v_perm_b32 v31, v237, v233, s61\n\t"
    "v_mfma_f32_16x16x32_bf16 a[156:159], v[32:35], v[104:107], a[156:159]\n\t"
    "buffer_load_dwordx4 v[196:199], v5, s[96:99], s62 offen offset:1024\n\t"
    "v_mfma_f32_16x16x32_bf16 a[220:223], v[40:43], v[104:107], a[220:223]\n\t"
    "v_perm_b32 v32, v214, v210, s60\n\t"
    "v_mfma_f32_16x16x32_bf16 a[252:255], v[44:47], v[104:107], a[252:255]\n\t"
    "v_mfma_f32_16x16x32_bf16 a[248:251], v[44:47], v[100:103], a[248:251]\n\t"
    "v_perm_b32 v36, v214, v210, s61\n\t"
    "v_mfma_f32_16x16x32_bf16 a[216:219], v[40:43], v[100:103], a[216:219]\n\t"
    "v_mfma_f32_16x16x32_bf16 a[212:215], v[40:43], v[96:99], a[212:215]\n\t"
    "v_perm_b32 v33, v222, v218, s60\n\t"
    "v_mfma_f32_16x16x32_bf16 a[244:247], v[44:47], v[96:99], a[244:247]\n\t"
    "buffer_load_dwordx4 v[200:203], v5, s[96:99], s62 offen offset:2048\n\t"
    "v_mfma_f32_16x16x32_bf16 a[240:243], v[44:47], v[92:95], a[240:243]\n\t"
    "v_perm_b32 v37, v222, v218, s61\n\t"
    "v_mfma_f32_16x16x32_bf16 a[208:211], v[40:43], v[92:95], a[208:211]\n\t"
    "v_mfma_f32_16x16x32_bf16 a[204:207], v[40:43], v[88:91], a[204:207]\n\t"
    "v_perm_b32 v34, v230, v226, s60\n\t"
    "v_mfma_f32_16x16x32_bf16 a[236:239], v[44:47], v[88:91], a[236:239]\n\t"
    "v_mfma_f32_16x16x32_bf16 a[232:235], v[44:47], v[84:87], a[232:235]\n\t"
    "v_perm_b32 v38, v230, v226, s61\n\t"
    "v_mfma_f32_16x16x32_bf16 a[200:203], v[40:43], v[84:87], a[200:203]\n\t"
    "buffer_load_dwordx4 v[204:207], v5, s[96:99], s62 offen offset:3072\n\t"
    "v_mfma_f32_16x16x32_bf16 a[196:199], v[40:43], v[80:83], a[196:199]\n\t"
    "v_perm_b32 v35, v238, v234, s60\n\t"
    "v_mfma_f32_16x16x32_bf16 a[228:231], v[44:47], v[80:83], a[228:231]\n\t"
    "v_mfma_f32_16x16x32_bf16 a[224:227], v[44:47], v[12:15], a[224:227]\n\t"
    "v_perm_b32 v39, v238, v234, s61\n\t"
    "v_mfma_f32_16x16x32_bf16 a[192:195], v[40:43], v[12:15], a[192:195]\n\t"
    "s_waitcnt lgkmcnt(0)\n\t"
    "v_mfma_f32_16x16x32_bf16 a[0:3], v[16:19], v[108:111], a[0:3]\n\t"
    "v_perm_b32 v40, v215, v211, s60\n\t"
    "v_mfma_f32_16x16x32_bf16 a[32:35], v[20:23], v[108:111], a[32:35]\n\t"
    "v_mfma_f32_16x16x32_bf16 a[36:39], v[20:23], v[12:15], a[36:39]\n\t"
    "v_perm_b32 v44, v215, v211, s61\n\t"
    "v_mfma_f32_16x16x32_bf16 a[4:7], v[16:19], v[12:15], a[4:7]\n\t"
    "ds_read_b128 v[104:107], v11 offset:35072\n\t"
    "v_mfma_f32_16x16x32_bf16 a[8:11], v[16:19], v[80:83], a[8:11]\n\t"
    "v_perm_b32 v41, v223, v219, s60\n\t"
    "v_mfma_f32_16x16x32_bf16 a[40:43], v[20:23], v[80:83], a[40:43]\n\t"
    "v_mfma_f32_16x16x32_bf16 a[44:47], v[20:23], v[84:87], a[44:47]\n\t"
    "v_perm_b32 v45, v223, v219, s61\n\t"
    "v_mfma_f32_16x16x32_bf16 a[12:15], v[16:19], v[84:87], a[12:15]\n\t"
    "v_mfma_f32_16x16x32_bf16 a[16:19], v[16:19], v[88:91], a[16:19]\n\t"
    "v_perm_b32 v42, v231, v227, s60\n\t"
    "v_mfma_f32_16x16x32_bf16 a[48:51], v[20:23], v[88:91], a[48:51]\n\t"
    "v_mfma_f32_16x16x32_bf16 a[52:55], v[20:23], v[92:95], a[52:55]\n\t"
    "v_perm_b32 v46, v231, v227, s61\n\t"
    "v_mfma_f32_16x16x32_bf16 a[20:23], v[16:19], v[92:95], a[20:23]\n\t"
    "v_mfma_f32_16x16x32_bf16 a[24:27], v[16:19], v[96:99], a[24:27]\n\t"
    "v_perm_b32 v43, v239, v235, s60\n\t"
    "v_mfma_f32_16x16x32_bf16 a[56:59], v[20:23], v[96:99], a[56:59]\n\t"
    "v_mfma_f32_16x16x32_bf16 a[60:63], v[20:23], v[100:103], a[60:63]\n\t"
    "v_perm_b32 v47, v239, v235, s61\n\t"
    "v_mfma_f32_16x16x32_bf16 a[28:31], v[16:19], v[100:103], a[28:31]\n\t"
    "s_waitcnt vmcnt(16)\n\t"
    "s_add_u32 s62, s63, 0x30000\n\t"
    "v_mfma_f32_16x16x32_bf16 a[92:95], v[24:27], v[100:103], a[92:95]\n\t"
    "v_perm_b32 v16, v116, v112, s60\n\t"
    "v_mfma_f32_16x16x32_bf16 a[124:127], v[28:31], v[100:103], a[124:127]\n\t"
    "buffer_load_dwordx4 v[208:211], v4, s[96:99], s62 offen\n\t"
    "v_mfma_f32_16x16x32_bf16 a[120:123], v[28:31], v[96:99], a[120:123]\n\t"
    "v_perm_b32 v20, v116, v112, s61\n\t"
    "v_mfma_f32_16x16x32_bf16 a[88:91], v[24:27], v[96:99], a[88:91]\n\t"
    "v_mfma_f32_16x16x32_bf16 a[84:87], v[24:27], v[92:95], a[84:87]\n\t"
    "v_perm_b32 v17, v124, v120, s60\n\t"
    "v_mfma_f32_16x16x32_bf16 a[116:119], v[28:31], v[92:95], a[116:119]\n\t"
    "v_mfma_f32_16x16x32_bf16 a[112:115], v[28:31], v[88:91], a[112:115]\n\t"
    "v_perm_b32 v21, v124, v120, s61\n\t"
    "v_mfma_f32_16x16x32_bf16 a[80:83], v[24:27], v[88:91], a[80:83]\n\t"
    "buffer_load_dwordx4 v[212:215], v4, s[96:99], s62 offen offset:1024\n\t"
    "v_mfma_f32_16x16x32_bf16 a[76:79], v[24:27], v[84:87], a[76:79]\n\t"
    "v_perm_b32 v18, v132, v128, s60\n\t"
    "v_mfma_f32_16x16x32_bf16 a[108:111], v[28:31], v[84:87], a[108:111]\n\t"
    "v_mfma_f32_16x16x32_bf16 a[104:107], v[28:31], v[80:83], a[104:107]\n\t"
    "v_perm_b32 v22, v132, v128, s61\n\t"
    "v_mfma_f32_16x16x32_bf16 a[72:75], v[24:27], v[80:83], a[72:75]\n\t"
    "v_mfma_f32_16x16x32_bf16 a[68:71], v[24:27], v[12:15], a[68:71]\n\t"
    "v_perm_b32 v19, v140, v136, s60\n\t"
    "v_mfma_f32_16x16x32_bf16 a[100:103], v[28:31], v[12:15], a[100:103]\n\t"
    "buffer_load_dwordx4 v[216:219], v4, s[96:99], s62 offen offset:2048\n\t"
    "v_mfma_f32_16x16x32_bf16 a[96:99], v[28:31], v[108:111], a[96:99]\n\t"
    "v_perm_b32 v23, v140, v136, s61\n\t"
    "v_mfma_f32_16x16x32_bf16 a[64:67], v[24:27], v[108:111], a[64:67]\n\t"
    "v_mfma_f32_16x16x32_bf16 a[128:131], v[32:35], v[108:111], a[128:131]\n\t"
    "v_perm_b32 v24, v117, v113, s60\n\t"
    "v_mfma_f32_16x16x32_bf16 a[160:163], v[36:39], v[108:111], a[160:163]\n\t"
    "v_mfma_f32_16x16x32_bf16 a[164:167], v[36:39], v[12:15], a[164:167]\n\t"
    "v_perm_b32 v28, v117, v113, s61\n\t"
    "v_mfma_f32_16x16x32_bf16 a[132:135], v[32:35], v[12:15], a[132:135]\n\t"
    "buffer_load_dwordx4 v[220:223], v4, s[96:99], s62 offen offset:3072\n\t"
    "v_mfma_f32_16x16x32_bf16 a[136:139], v[32:35], v[80:83], a[136:139]\n\t"
    "v_perm_b32 v25, v125, v121, s60\n\t"
    "v_mfma_f32_16x16x32_bf16 a[168:171], v[36:39], v[80:83], a[168:171]\n\t"
    "v_mfma_f32_16x16x32_bf16 a[172:175], v[36:39], v[84:87], a[172:175]\n\t"
    "v_perm_b32 v29, v125, v121, s61\n\t"
    "v_mfma_f32_16x16x32_bf16 a[140:143], v[32:35], v[84:87], a[140:143]\n\t"
    "v_mfma_f32_16x16x32_bf16 a[144:147], v[32:35], v[88:91], a[144:147]\n\t"
    "v_perm_b32 v26, v133, v129, s60\n\t"
    "v_mfma_f32_16x16x32_bf16 a[176:179], v[36:39], v[88:91], a[176:179]\n\t"
    "buffer_load_dwordx4 v[224:227], v5, s[96:99], s62 offen\n\t"
    "v_mfma_f32_16x16x32_bf16 a[180:183], v[36:39], v[92:95], a[180:183]\n\t"
    "v_perm_b32 v30, v133, v129, s61\n\t"
    "v_mfma_f32_16x16x32_bf16 a[148:151], v[32:35], v[92:95], a[148:151]\n\t"
    "v_mfma_f32_16x16x32_bf16 a[152:155], v[32:35], v[96:99], a[152:155]\n\t"
    "v_perm_b32 v27, v141, v137, s60\n\t"
    "v_mfma_f32_16x16x32_bf16 a[184:187], v[36:39], v[96:99], a[184:187]\n\t"
    "v_mfma_f32_16x16x32_bf16 a[188:191], v[36:39], v[100:103], a[188:191]\n\t"
    "v_perm_b32 v31, v141, v137, s61\n\t"
    "v_mfma_f32_16x16x32_bf16 a[156:159], v[32:35], v[100:103], a[156:159]\n\t"
    "buffer_load_dwordx4 v[228:231], v5, s[96:99], s62 offen offset:1024\n\t"
    "v_mfma_f32_16x16x32_bf16 a[220:223], v[40:43], v[100:103], a[220:223]\n\t"
    "v_perm_b32 v32, v118, v114, s60\n\t"
    "v_mfma_f32_16x16x32_bf16 a[252:255], v[44:47], v[100:103], a[252:255]\n\t"
    "v_mfma_f32_16x16x32_bf16 a[248:251], v[44:47], v[96:99], a[248:251]\n\t"
    "v_perm_b32 v36, v118, v114, s61\n\t"
    "v_mfma_f32_16x16x32_bf16 a[216:219], v[40:43], v[96:99], a[216:219]\n\t"
    "v_mfma_f32_16x16x32_bf16 a[212:215], v[40:43], v[92:95], a[212:215]\n\t"
    "v_perm_b32 v33, v126, v122, s60\n\t"
    "v_mfma_f32_16x16x32_bf16 a[244:247], v[44:47], v[92:95], a[244:247]\n\t"
    "buffer_load_dwordx4 v[232:235], v5, s[96:99], s62 offen offset:2048\n\t"
    "v_mfma_f32_16x16x32_bf16 a[240:243], v[44:47], v[88:91], a[240:243]\n\t"
    "v_perm_b32 v37, v126, v122, s61\n\t"
    "v_mfma_f32_16x16x32_bf16 a[208:211], v[40:43], v[88:91], a[208:211]\n\t"
    "v_mfma_f32_16x16x32_bf16 a[204:207], v[40:43], v[84:87], a[204:207]\n\t"
    "v_perm_b32 v34, v134, v130, s60\n\t"
    "v_mfma_f32_16x16x32_bf16 a[236:239], v[44:47], v[84:87], a[236:239]\n\t"
    "v_mfma_f32_16x16x32_bf16 a[232:235], v[44:47], v[80:83], a[232:235]\n\t"
    "v_perm_b32 v38, v134, v130, s61\n\t"
    "v_mfma_f32_16x16x32_bf16 a[200:203], v[40:43], v[80:83], a[200:203]\n\t"
    "buffer_load_dwordx4 v[236:239], v5, s[96:99], s62 offen offset:3072\n\t"
    "v_mfma_f32_16x16x32_bf16 a[196:199], v[40:43], v[12:15], a[196:199]\n\t"
    "v_perm_b32 v35, v142, v138, s60\n\t"
    "v_mfma_f32_16x16x32_bf16 a[228:231], v[44:47], v[12:15], a[228:231]\n\t"
    "v_mfma_f32_16x16x32_bf16 a[224:227], v[44:47], v[108:111], a[224:227]\n\t"
    "v_perm_b32 v39, v142, v138, s61\n\t"
    "v_mfma_f32_16x16x32_bf16 a[192:195], v[40:43], v[108:111], a[192:195]\n\t"
    "s_waitcnt lgkmcnt(0)\n\t"
    "v_mfma_f32_16x16x32_bf16 a[0:3], v[16:19], v[104:107], a[0:3]\n\t"
    "v_perm_b32 v40, v119, v115, s60\n\t"
    "v_mfma_f32_16x16x32_bf16 a[32:35], v[20:23], v[104:107], a[32:35]\n\t"
    "v_mfma_f32_16x16x32_bf16 a[36:39], v[20:23], v[108:111], a[36:39]\n\t"
    "v_perm_b32 v44, v119, v115, s61\n\t"
    "v_mfma_f32_16x16x32_bf16 a[4:7], v[16:19], v[108:111], a[4:7]\n\t"
    "ds_read_b128 v[100:103], v11 offset:34816\n\t"
    "v_mfma_f32_16x16x32_bf16 a[8:11], v[16:19], v[12:15], a[8:11]\n\t"
    "v_perm_b32 v41, v127, v123, s60\n\t"
    "v_mfma_f32_16x16x32_bf16 a[40:43], v[20:23], v[12:15], a[40:43]\n\t"
    "v_mfma_f32_16x16x32_bf16 a[44:47], v[20:23], v[80:83], a[44:47]\n\t"
    "v_perm_b32 v45, v127, v123, s61\n\t"
    "v_mfma_f32_16x16x32_bf16 a[12:15], v[16:19], v[80:83], a[12:15]\n\t"
    "v_mfma_f32_16x16x32_bf16 a[16:19], v[16:19], v[84:87], a[16:19]\n\t"
    "v_perm_b32 v42, v135, v131, s60\n\t"
    "v_mfma_f32_16x16x32_bf16 a[48:51], v[20:23], v[84:87], a[48:51]\n\t"
    "v_mfma_f32_16x16x32_bf16 a[52:55], v[20:23], v[88:91], a[52:55]\n\t"
    "v_perm_b32 v46, v135, v131, s61\n\t"
    "v_mfma_f32_16x16x32_bf16 a[20:23], v[16:19], v[88:91], a[20:23]\n\t"
    "ds_read_b128 v[48:51], v8 offset:53248\n\t"
    "v_mfma_f32_16x16x32_bf16 a[24:27], v[16:19], v[92:95], a[24:27]\n\t"
    "v_perm_b32 v43, v143, v139, s60\n\t"
    "v_mfma_f32_16x16x32_bf16 a[56:59], v[20:23], v[92:95], a[56:59]\n\t"
    "v_mfma_f32_16x16x32_bf16 a[60:63], v[20:23], v[96:99], a[60:63]\n\t"
    "v_perm_b32 v47, v143, v139, s61\n\t"
    "v_mfma_f32_16x16x32_bf16 a[28:31], v[16:19], v[96:99], a[28:31]\n\t"
    "s_waitcnt vmcnt(16)\n\t"
    "s_mov_b32 s62, 0xb0000\n\t"
    "v_mfma_f32_16x16x32_bf16 a[92:95], v[24:27], v[96:99], a[92:95]\n\t"
    "v_perm_b32 v16, v148, v144, s60\n\t"
    "v_mfma_f32_16x16x32_bf16 a[124:127], v[28:31], v[96:99], a[124:127]\n\t"
    "buffer_load_dwordx4 v[112:115], v4, s[96:99], s62 offen\n\t"
    "v_mfma_f32_16x16x32_bf16 a[120:123], v[28:31], v[92:95], a[120:123]\n\t"
    "v_perm_b32 v20, v148, v144, s61\n\t"
    "v_mfma_f32_16x16x32_bf16 a[88:91], v[24:27], v[92:95], a[88:91]\n\t"
    "ds_read_b128 v[52:55], v8 offset:53504\n\t"
    "v_mfma_f32_16x16x32_bf16 a[84:87], v[24:27], v[88:91], a[84:87]\n\t"
    "v_perm_b32 v17, v156, v152, s60\n\t"
    "v_mfma_f32_16x16x32_bf16 a[116:119], v[28:31], v[88:91], a[116:119]\n\t"
    "v_mfma_f32_16x16x32_bf16 a[112:115], v[28:31], v[84:87], a[112:115]\n\t"
    "v_perm_b32 v21, v156, v152, s61\n\t"
    "v_mfma_f32_16x16x32_bf16 a[80:83], v[24:27], v[84:87], a[80:83]\n\t"
    "buffer_load_dwordx4 v[116:119], v4, s[96:99], s62 offen offset:1024\n\t"
    "v_mfma_f32_16x16x32_bf16 a[76:79], v[24:27], v[80:83], a[76:79]\n\t"
    "v_perm_b32 v18, v164, v160, s60\n\t"
    "v_mfma_f32_16x16x32_bf16 a[108:111], v[28:31], v[80:83], a[108:111]\n\t"
    "v_mfma_f32_16x16x32_bf16 a[104:107], v[28:31], v[12:15], a[104:107]\n\t"
    "v_perm_b32 v22, v164, v160, s61\n\t"
    "v_mfma_f32_16x16x32_bf16 a[72:75], v[24:27], v[12:15], a[72:75]\n\t"
    "ds_read_b128 v[56:59], v8 offset:53760\n\t"
    "v_mfma_f32_16x16x32_bf16 a[68:71], v[24:27], v[108:111], a[68:71]\n\t"
    "v_perm_b32 v19, v172, v168, s60\n\t"
    "v_mfma_f32_16x16x32_bf16 a[100:103], v[28:31], v[108:111], a[100:103]\n\t"
    "buffer_load_dwordx4 v[120:123], v4, s[96:99], s62 offen offset:2048\n\t"
    "v_mfma_f32_16x16x32_bf16 a[96:99], v[28:31], v[104:107], a[96:99]\n\t"
    "v_perm_b32 v23, v172, v168, s61\n\t"
    "v_mfma_f32_16x16x32_bf16 a[64:67], v[24:27], v[104:107], a[64:67]\n\t"
    "v_mfma_f32_16x16x32_bf16 a[128:131], v[32:35], v[104:107], a[128:131]\n\t"
    "v_perm_b32 v24, v149, v145, s60\n\t"
    "v_mfma_f32_16x16x32_bf16 a[160:163], v[36:39], v[104:107], a[160:163]\n\t"
    "v_mfma_f32_16x16x32_bf16 a[164:167], v[36:39], v[108:111], a[164:167]\n\t"
    "v_perm_b32 v28, v149, v145, s61\n\t"
    "v_mfma_f32_16x16x32_bf16 a[132:135], v[32:35], v[108:111], a[132:135]\n\t"
    "buffer_load_dwordx4 v[124:127], v4, s[96:99], s62 offen offset:3072\n\t"
    "ds_read_b128 v[60:63], v8 offset:54016\n\t"
    "v_mfma_f32_16x16x32_bf16 a[136:139], v[32:35], v[12:15], a[136:139]\n\t"
    "v_perm_b32 v25, v157, v153, s60\n\t"
    "v_mfma_f32_16x16x32_bf16 a[168:171], v[36:39], v[12:15], a[168:171]\n\t"
    "v_mfma_f32_16x16x32_bf16 a[172:175], v[36:39], v[80:83], a[172:175]\n\t"
    "v_perm_b32 v29, v157, v153, s61\n\t"
    "v_mfma_f32_16x16x32_bf16 a[140:143], v[32:35], v[80:83], a[140:143]\n\t"
    "v_mfma_f32_16x16x32_bf16 a[144:147], v[32:35], v[84:87], a[144:147]\n\t"
    "v_perm_b32 v26, v165, v161, s60\n\t"
    "v_mfma_f32_16x16x32_bf16 a[176:179], v[36:39], v[84:87], a[176:179]\n\t"
    "buffer_load_dwordx4 v[128:131], v5, s[96:99], s62 offen\n\t"
    "v_mfma_f32_16x16x32_bf16 a[180:183], v[36:39], v[88:91], a[180:183]\n\t"
    "v_perm_b32 v30, v165, v161, s61\n\t"
    "v_mfma_f32_16x16x32_bf16 a[148:151], v[32:35], v[88:91], a[148:151]\n\t"
    "v_mfma_f32_16x16x32_bf16 a[152:155], v[32:35], v[92:95], a[152:155]\n\t"
    "v_perm_b32 v27, v173, v169, s60\n\t"
    "v_mfma_f32_16x16x32_bf16 a[184:187], v[36:39], v[92:95], a[184:187]\n\t"
    "v_mfma_f32_16x16x32_bf16 a[188:191], v[36:39], v[96:99], a[188:191]\n\t"
    "v_perm_b32 v31, v173, v169, s61\n\t"
    "v_mfma_f32_16x16x32_bf16 a[156:159], v[32:35], v[96:99], a[156:159]\n\t"
    "buffer_load_dwordx4 v[132:135], v5, s[96:99], s62 offen offset:1024\n\t"
    "v_mfma_f32_16x16x32_bf16 a[220:223], v[40:43], v[96:99], a[220:223]\n\t"
    "v_perm_b32 v32, v150, v146, s60\n\t"
    "v_mfma_f32_16x16x32_bf16 a[252:255], v[44:47], v[96:99], a[252:255]\n\t"
    "v_mfma_f32_16x16x32_bf16 a[248:251], v[44:47], v[92:95], a[248:251]\n\t"
    "v_perm_b32 v36, v150, v146, s61\n\t"
    "v_mfma_f32_16x16x32_bf16 a[216:219], v[40:43], v[92:95], a[216:219]\n\t"
    "v_mfma_f32_16x16x32_bf16 a[212:215], v[40:43], v[88:91], a[212:215]\n\t"
    "v_perm_b32 v33, v158, v154, s60\n\t"
    "v_mfma_f32_16x16x32_bf16 a[244:247], v[44:47], v[88:91], a[244:247]\n\t"
    "buffer_load_dwordx4 v[136:139], v5, s[96:99], s62 offen offset:2048\n\t"
    "v_mfma_f32_16x16x32_bf16 a[240:243], v[44:47], v[84:87], a[240:243]\n\t"
    "v_perm_b32 v37, v158, v154, s61\n\t"
    "v_mfma_f32_16x16x32_bf16 a[208:211], v[40:43], v[84:87], a[208:211]\n\t"
    "v_mfma_f32_16x16x32_bf16 a[204:207], v[40:43], v[80:83], a[204:207]\n\t"
    "v_perm_b32 v34, v166, v162, s60\n\t"
    "v_mfma_f32_16x16x32_bf16 a[236:239], v[44:47], v[80:83], a[236:239]\n\t"
    "v_mfma_f32_16x16x32_bf16 a[232:235], v[44:47], v[12:15], a[232:235]\n\t"
    "v_perm_b32 v38, v166, v162, s61\n\t"
    "v_mfma_f32_16x16x32_bf16 a[200:203], v[40:43], v[12:15], a[200:203]\n\t"
    "buffer_load_dwordx4 v[140:143], v5, s[96:99], s62 offen offset:3072\n\t"
    "v_mfma_f32_16x16x32_bf16 a[196:199], v[40:43], v[108:111], a[196:199]\n\t"
    "v_perm_b32 v35, v174, v170, s60\n\t"
    "v_mfma_f32_16x16x32_bf16 a[228:231], v[44:47], v[108:111], a[228:231]\n\t"
    "v_mfma_f32_16x16x32_bf16 a[224:227], v[44:47], v[104:107], a[224:227]\n\t"
    "v_perm_b32 v39, v174, v170, s61\n\t"
    "v_mfma_f32_16x16x32_bf16 a[192:195], v[40:43], v[104:107], a[192:195]\n\t"
    "s_waitcnt lgkmcnt(0)\n\t"
    "v_mfma_f32_16x16x32_bf16 a[0:3], v[16:19], v[100:103], a[0:3]\n\t"
    "v_perm_b32 v40, v151, v147, s60\n\t"
    "v_mfma_f32_16x16x32_bf16 a[32:35], v[20:23], v[100:103], a[32:35]\n\t"
    "v_mfma_f32_16x16x32_bf16 a[36:39], v[20:23], v[104:107], a[36:39]\n\t"
    "v_perm_b32 v44, v151, v147, s61\n\t"
    "v_mfma_f32_16x16x32_bf16 a[4:7], v[16:19], v[104:107], a[4:7]\n\t"
    "v_mfma_f32_16x16x32_bf16 a[8:11], v[16:19], v[108:111], a[8:11]\n\t"
    "v_perm_b32 v41, v159, v155, s60\n\t"
    "v_mfma_f32_16x16x32_bf16 a[40:43], v[20:23], v[108:111], a[40:43]\n\t"
    "v_mfma_f32_16x16x32_bf16 a[44:47], v[20:23], v[12:15], a[44:47]\n\t"
    "v_perm_b32 v45, v159, v155, s61\n\t"
    "v_mfma_f32_16x16x32_bf16 a[12:15], v[16:19], v[12:15], a[12:15]\n\t"
    "v_mfma_f32_16x16x32_bf16 a[16:19], v[16:19], v[80:83], a[16:19]\n\t"
    "v_perm_b32 v42, v167, v163, s60\n\t"
    "v_mfma_f32_16x16x32_bf16 a[48:51], v[20:23], v[80:83], a[48:51]\n\t"
    "v_mfma_f32_16x16x32_bf16 a[52:55], v[20:23], v[84:87], a[52:55]\n\t"
    "v_perm_b32 v46, v167, v163, s61\n\t"
    "v_mfma_f32_16x16x32_bf16 a[20:23], v[16:19], v[84:87], a[20:23]\n\t"
    "ds_read_b128 v[64:67], v8 offset:54272\n\t"
    "v_mfma_f32_16x16x32_bf16 a[24:27], v[16:19], v[88:91], a[24:27]\n\t"
    "v_perm_b32 v43, v175, v171, s60\n\t"
    "v_mfma_f32_16x16x32_bf16 a[56:59], v[20:23], v[88:91], a[56:59]\n\t"
    "v_mfma_f32_16x16x32_bf16 a[60:63], v[20:23], v[92:95], a[60:63]\n\t"
    "v_perm_b32 v47, v175, v171, s61\n\t"
    "v_mfma_f32_16x16x32_bf16 a[28:31], v[16:19], v[92:95], a[28:31]\n\t"
    "s_waitcnt vmcnt(16)\n\t"
    "s_add_u32 s62, s70, 0x30000\n\t"
    "v_mfma_f32_16x16x32_bf16 a[92:95], v[24:27], v[92:95], a[92:95]\n\t"
    "v_perm_b32 v16, v180, v176, s60\n\t"
    "v_mfma_f32_16x16x32_bf16 a[124:127], v[28:31], v[92:95], a[124:127]\n\t"
    "buffer_load_dwordx4 v[144:147], v4, s[96:99], s62 offen\n\t"
    "v_mfma_f32_16x16x32_bf16 a[120:123], v[28:31], v[88:91], a[120:123]\n\t"
    "v_perm_b32 v20, v180, v176, s61\n\t"
    "v_mfma_f32_16x16x32_bf16 a[88:91], v[24:27], v[88:91], a[88:91]\n\t"
    "ds_read_b128 v[68:71], v8 offset:54528\n\t"
    "v_mfma_f32_16x16x32_bf16 a[84:87], v[24:27], v[84:87], a[84:87]\n\t"
    "v_perm_b32 v17, v188, v184, s60\n\t"
    "v_mfma_f32_16x16x32_bf16 a[116:119], v[28:31], v[84:87], a[116:119]\n\t"
    "v_mfma_f32_16x16x32_bf16 a[112:115], v[28:31], v[80:83], a[112:115]\n\t"
    "v_perm_b32 v21, v188, v184, s61\n\t"
    "v_mfma_f32_16x16x32_bf16 a[80:83], v[24:27], v[80:83], a[80:83]\n\t"
    "buffer_load_dwordx4 v[148:151], v4, s[96:99], s62 offen offset:1024\n\t"
    "v_mfma_f32_16x16x32_bf16 a[76:79], v[24:27], v[12:15], a[76:79]\n\t"
    "v_perm_b32 v18, v196, v192, s60\n\t"
    "v_mfma_f32_16x16x32_bf16 a[108:111], v[28:31], v[12:15], a[108:111]\n\t"
    "v_mfma_f32_16x16x32_bf16 a[104:107], v[28:31], v[108:111], a[104:107]\n\t"
    "v_perm_b32 v22, v196, v192, s61\n\t"
    "v_mfma_f32_16x16x32_bf16 a[72:75], v[24:27], v[108:111], a[72:75]\n\t"
    "ds_read_b128 v[72:75], v8 offset:54784\n\t"
    "v_mfma_f32_16x16x32_bf16 a[68:71], v[24:27], v[104:107], a[68:71]\n\t"
    "v_perm_b32 v19, v204, v200, s60\n\t"
    "v_mfma_f32_16x16x32_bf16 a[100:103], v[28:31], v[104:107], a[100:103]\n\t"
    "buffer_load_dwordx4 v[152:155], v4, s[96:99], s62 offen offset:2048\n\t"
    "v_mfma_f32_16x16x32_bf16 a[96:99], v[28:31], v[100:103], a[96:99]\n\t"
    "v_perm_b32 v23, v204, v200, s61\n\t"
    "v_mfma_f32_16x16x32_bf16 a[64:67], v[24:27], v[100:103], a[64:67]\n\t"
    "v_mfma_f32_16x16x32_bf16 a[128:131], v[32:35], v[100:103], a[128:131]\n\t"
    "v_perm_b32 v24, v181, v177, s60\n\t"
    "v_mfma_f32_16x16x32_bf16 a[160:163], v[36:39], v[100:103], a[160:163]\n\t"
    "v_mfma_f32_16x16x32_bf16 a[164:167], v[36:39], v[104:107], a[164:167]\n\t"
    "v_perm_b32 v28, v181, v177, s61\n\t"
    "v_mfma_f32_16x16x32_bf16 a[132:135], v[32:35], v[104:107], a[132:135]\n\t"
    "buffer_load_dwordx4 v[156:159], v4, s[96:99], s62 offen offset:3072\n\t"
    "ds_read_b128 v[76:79], v8 offset:55040\n\t"
    "v_mfma_f32_16x16x32_bf16 a[136:139], v[32:35], v[108:111], a[136:139]\n\t"
    "v_perm_b32 v25, v189, v185, s60\n\t"
    "v_mfma_f32_16x16x32_bf16 a[168:171], v[36:39], v[108:111], a[168:171]\n\t"
    "v_mfma_f32_16x16x32_bf16 a[172:175], v[36:39], v[12:15], a[172:175]\n\t"
    "v_perm_b32 v29, v189, v185, s61\n\t"
    "v_mfma_f32_16x16x32_bf16 a[140:143], v[32:35], v[12:15], a[140:143]\n\t"
    "v_mfma_f32_16x16x32_bf16 a[144:147], v[32:35], v[80:83], a[144:147]\n\t"
    "v_perm_b32 v26, v197, v193, s60\n\t"
    "v_mfma_f32_16x16x32_bf16 a[176:179], v[36:39], v[80:83], a[176:179]\n\t"
    "buffer_load_dwordx4 v[160:163], v5, s[96:99], s62 offen\n\t"
    "v_mfma_f32_16x16x32_bf16 a[180:183], v[36:39], v[84:87], a[180:183]\n\t"
    "v_perm_b32 v30, v197, v193, s61\n\t"
    "v_mfma_f32_16x16x32_bf16 a[148:151], v[32:35], v[84:87], a[148:151]\n\t"
    "v_mfma_f32_16x16x32_bf16 a[152:155], v[32:35], v[88:91], a[152:155]\n\t"
    "v_perm_b32 v27, v205, v201, s60\n\t"
    "v_mfma_f32_16x16x32_bf16 a[184:187], v[36:39], v[88:91], a[184:187]\n\t"
    "v_mfma_f32_16x16x32_bf16 a[188:191], v[36:39], v[92:95], a[188:191]\n\t"
    "v_perm_b32 v31, v205, v201, s61\n\t"
    "v_mfma_f32_16x16x32_bf16 a[156:159], v[32:35], v[92:95], a[156:159]\n\t"
    "buffer_load_dwordx4 v[164:167], v5, s[96:99], s62 offen offset:1024\n\t"
    "v_mfma_f32_16x16x32_bf16 a[220:223], v[40:43], v[92:95], a[220:223]\n\t"
    "v_perm_b32 v32, v182, v178, s60\n\t"
    "v_mfma_f32_16x16x32_bf16 a[252:255], v[44:47], v[92:95], a[252:255]\n\t"
    "v_mfma_f32_16x16x32_bf16 a[248:251], v[44:47], v[88:91], a[248:251]\n\t"
    "v_perm_b32 v36, v182, v178, s61\n\t"
    "v_mfma_f32_16x16x32_bf16 a[216:219], v[40:43], v[88:91], a[216:219]\n\t"
    "v_mfma_f32_16x16x32_bf16 a[212:215], v[40:43], v[84:87], a[212:215]\n\t"
    "v_perm_b32 v33, v190, v186, s60\n\t"
    "v_mfma_f32_16x16x32_bf16 a[244:247], v[44:47], v[84:87], a[244:247]\n\t"
    "buffer_load_dwordx4 v[168:171], v5, s[96:99], s62 offen offset:2048\n\t"
    "v_mfma_f32_16x16x32_bf16 a[240:243], v[44:47], v[80:83], a[240:243]\n\t"
    "v_perm_b32 v37, v190, v186, s61\n\t"
    "v_mfma_f32_16x16x32_bf16 a[208:211], v[40:43], v[80:83], a[208:211]\n\t"
    "v_mfma_f32_16x16x32_bf16 a[204:207], v[40:43], v[12:15], a[204:207]\n\t"
    "v_perm_b32 v34, v198, v194, s60\n\t"
    "v_mfma_f32_16x16x32_bf16 a[236:239], v[44:47], v[12:15], a[236:239]\n\t"
    "v_mfma_f32_16x16x32_bf16 a[232:235], v[44:47], v[108:111], a[232:235]\n\t"
    "v_perm_b32 v38, v198, v194, s61\n\t"
    "v_mfma_f32_16x16x32_bf16 a[200:203], v[40:43], v[108:111], a[200:203]\n\t"
    "buffer_load_dwordx4 v[172:175], v5, s[96:99], s62 offen offset:3072\n\t"
    "v_mfma_f32_16x16x32_bf16 a[196:199], v[40:43], v[104:107], a[196:199]\n\t"
    "v_perm_b32 v35, v206, v202, s60\n\t"
    "v_mfma_f32_16x16x32_bf16 a[228:231], v[44:47], v[104:107], a[228:231]\n\t"
    "v_mfma_f32_16x16x32_bf16 a[224:227], v[44:47], v[100:103], a[224:227]\n\t"
    "v_perm_b32 v39, v206, v202, s61\n\t"
    "v_mfma_f32_16x16x32_bf16 a[192:195], v[40:43], v[100:103], a[192:195]\n\t"
    "s_waitcnt lgkmcnt(0)\n\t"
    "v_mfma_f32_16x16x32_bf16 a[0:3], v[16:19], v[48:51], a[0:3]\n\t"
    "v_perm_b32 v40, v183, v179, s60\n\t"
    "v_mfma_f32_16x16x32_bf16 a[32:35], v[20:23], v[48:51], a[32:35]\n\t"
    "v_mfma_f32_16x16x32_bf16 a[36:39], v[20:23], v[52:55], a[36:39]\n\t"
    "v_perm_b32 v44, v183, v179, s61\n\t"
    "v_mfma_f32_16x16x32_bf16 a[4:7], v[16:19], v[52:55], a[4:7]\n\t"
    "ds_read_b128 v[12:15], v9 offset:52992\n\t"
    "v_mfma_f32_16x16x32_bf16 a[8:11], v[16:19], v[56:59], a[8:11]\n\t"
    "v_perm_b32 v41, v191, v187, s60\n\t"
    "v_mfma_f32_16x16x32_bf16 a[40:43], v[20:23], v[56:59], a[40:43]\n\t"
    "v_mfma_f32_16x16x32_bf16 a[44:47], v[20:23], v[60:63], a[44:47]\n\t"
    "v_perm_b32 v45, v191, v187, s61\n\t"
    "v_mfma_f32_16x16x32_bf16 a[12:15], v[16:19], v[60:63], a[12:15]\n\t"
    "v_mfma_f32_16x16x32_bf16 a[16:19], v[16:19], v[64:67], a[16:19]\n\t"
    "v_perm_b32 v42, v199, v195, s60\n\t"
    "v_mfma_f32_16x16x32_bf16 a[48:51], v[20:23], v[64:67], a[48:51]\n\t"
    "v_mfma_f32_16x16x32_bf16 a[52:55], v[20:23], v[68:71], a[52:55]\n\t"
    "v_perm_b32 v46, v199, v195, s61\n\t"
    "v_mfma_f32_16x16x32_bf16 a[20:23], v[16:19], v[68:71], a[20:23]\n\t"
    "v_mfma_f32_16x16x32_bf16 a[24:27], v[16:19], v[72:75], a[24:27]\n\t"
    "v_perm_b32 v43, v207, v203, s60\n\t"
    "v_mfma_f32_16x16x32_bf16 a[56:59], v[20:23], v[72:75], a[56:59]\n\t"
    "v_mfma_f32_16x16x32_bf16 a[60:63], v[20:23], v[76:79], a[60:63]\n\t"
    "v_perm_b32 v47, v207, v203, s61\n\t"
    "v_mfma_f32_16x16x32_bf16 a[28:31], v[16:19], v[76:79], a[28:31]\n\t"
    "s_waitcnt vmcnt(16)\n\t"
    "s_add_u32 s62, s72, 0x30000\n\t"
    "v_mfma_f32_16x16x32_bf16 a[92:95], v[24:27], v[76:79], a[92:95]\n\t"
    "v_perm_b32 v16, v212, v208, s60\n\t"
    "v_mfma_f32_16x16x32_bf16 a[124:127], v[28:31], v[76:79], a[124:127]\n\t"
    "buffer_load_dwordx4 v[176:179], v4, s[96:99], s62 offen\n\t"
    "v_mfma_f32_16x16x32_bf16 a[120:123], v[28:31], v[72:75], a[120:123]\n\t"
    "v_perm_b32 v20, v212, v208, s61\n\t"
    "v_mfma_f32_16x16x32_bf16 a[88:91], v[24:27], v[72:75], a[88:91]\n\t"
    "v_mfma_f32_16x16x32_bf16 a[84:87], v[24:27], v[68:71], a[84:87]\n\t"
    "v_perm_b32 v17, v220, v216, s60\n\t"
    "v_mfma_f32_16x16x32_bf16 a[116:119], v[28:31], v[68:71], a[116:119]\n\t"
    "v_mfma_f32_16x16x32_bf16 a[112:115], v[28:31], v[64:67], a[112:115]\n\t"
    "v_perm_b32 v21, v220, v216, s61\n\t"
    "v_mfma_f32_16x16x32_bf16 a[80:83], v[24:27], v[64:67], a[80:83]\n\t"
    "buffer_load_dwordx4 v[180:183], v4, s[96:99], s62 offen offset:1024\n\t"
    "v_mfma_f32_16x16x32_bf16 a[76:79], v[24:27], v[60:63], a[76:79]\n\t"
    "v_perm_b32 v18, v228, v224, s60\n\t"
    "v_mfma_f32_16x16x32_bf16 a[108:111], v[28:31], v[60:63], a[108:111]\n\t"
    "v_mfma_f32_16x16x32_bf16 a[104:107], v[28:31], v[56:59], a[104:107]\n\t"
    "v_perm_b32 v22, v228, v224, s61\n\t"
    "v_mfma_f32_16x16x32_bf16 a[72:75], v[24:27], v[56:59], a[72:75]\n\t"
    "v_mfma_f32_16x16x32_bf16 a[68:71], v[24:27], v[52:55], a[68:71]\n\t"
    "v_perm_b32 v19, v236, v232, s60\n\t"
    "v_mfma_f32_16x16x32_bf16 a[100:103], v[28:31], v[52:55], a[100:103]\n\t"
    "buffer_load_dwordx4 v[184:187], v4, s[96:99], s62 offen offset:2048\n\t"
    "v_mfma_f32_16x16x32_bf16 a[96:99], v[28:31], v[48:51], a[96:99]\n\t"
    "v_perm_b32 v23, v236, v232, s61\n\t"
    "v_mfma_f32_16x16x32_bf16 a[64:67], v[24:27], v[48:51], a[64:67]\n\t"
    "v_mfma_f32_16x16x32_bf16 a[128:131], v[32:35], v[48:51], a[128:131]\n\t"
    "v_perm_b32 v24, v213, v209, s60\n\t"
    "v_mfma_f32_16x16x32_bf16 a[160:163], v[36:39], v[48:51], a[160:163]\n\t"
    "v_mfma_f32_16x16x32_bf16 a[164:167], v[36:39], v[52:55], a[164:167]\n\t"
    "v_perm_b32 v28, v213, v209, s61\n\t"
    "v_mfma_f32_16x16x32_bf16 a[132:135], v[32:35], v[52:55], a[132:135]\n\t"
    "buffer_load_dwordx4 v[188:191], v4, s[96:99], s62 offen offset:3072\n\t"
    "v_mfma_f32_16x16x32_bf16 a[136:139], v[32:35], v[56:59], a[136:139]\n\t"
    "v_perm_b32 v25, v221, v217, s60\n\t"
    "v_mfma_f32_16x16x32_bf16 a[168:171], v[36:39], v[56:59], a[168:171]\n\t"
    "v_mfma_f32_16x16x32_bf16 a[172:175], v[36:39], v[60:63], a[172:175]\n\t"
    "v_perm_b32 v29, v221, v217, s61\n\t"
    "v_mfma_f32_16x16x32_bf16 a[140:143], v[32:35], v[60:63], a[140:143]\n\t"
    "v_mfma_f32_16x16x32_bf16 a[144:147], v[32:35], v[64:67], a[144:147]\n\t"
    "v_perm_b32 v26, v229, v225, s60\n\t"
    "v_mfma_f32_16x16x32_bf16 a[176:179], v[36:39], v[64:67], a[176:179]\n\t"
    "buffer_load_dwordx4 v[192:195], v5, s[96:99], s62 offen\n\t"
    "v_mfma_f32_16x16x32_bf16 a[180:183], v[36:39], v[68:71], a[180:183]\n\t"
    "v_perm_b32 v30, v229, v225, s61\n\t"
    "v_mfma_f32_16x16x32_bf16 a[148:151], v[32:35], v[68:71], a[148:151]\n\t"
    "v_mfma_f32_16x16x32_bf16 a[152:155], v[32:35], v[72:75], a[152:155]\n\t"
    "v_perm_b32 v27, v237, v233, s60\n\t"
    "v_mfma_f32_16x16x32_bf16 a[184:187], v[36:39], v[72:75], a[184:187]\n\t"
    "v_mfma_f32_16x16x32_bf16 a[188:191], v[36:39], v[76:79], a[188:191]\n\t"
    "v_perm_b32 v31, v237, v233, s61\n\t"
    "v_mfma_f32_16x16x32_bf16 a[156:159], v[32:35], v[76:79], a[156:159]\n\t"
    "buffer_load_dwordx4 v[196:199], v5, s[96:99], s62 offen offset:1024\n\t"
    "v_mfma_f32_16x16x32_bf16 a[220:223], v[40:43], v[76:79], a[220:223]\n\t"
    "v_perm_b32 v32, v214, v210, s60\n\t"
    "v_mfma_f32_16x16x32_bf16 a[252:255], v[44:47], v[76:79], a[252:255]\n\t"
    "v_mfma_f32_16x16x32_bf16 a[248:251], v[44:47], v[72:75], a[248:251]\n\t"
    "v_perm_b32 v36, v214, v210, s61\n\t"
    "v_mfma_f32_16x16x32_bf16 a[216:219], v[40:43], v[72:75], a[216:219]\n\t"
    "v_mfma_f32_16x16x32_bf16 a[212:215], v[40:43], v[68:71], a[212:215]\n\t"
    "v_perm_b32 v33, v222, v218, s60\n\t"
    "v_mfma_f32_16x16x32_bf16 a[244:247], v[44:47], v[68:71], a[244:247]\n\t"
    "buffer_load_dwordx4 v[200:203], v5, s[96:99], s62 offen offset:2048\n\t"
    "v_mfma_f32_16x16x32_bf16 a[240:243], v[44:47], v[64:67], a[240:243]\n\t"
    "v_perm_b32 v37, v222, v218, s61\n\t"
    "v_mfma_f32_16x16x32_bf16 a[208:211], v[40:43], v[64:67], a[208:211]\n\t"
    "v_mfma_f32_16x16x32_bf16 a[204:207], v[40:43], v[60:63], a[204:207]\n\t"
    "v_perm_b32 v34, v230, v226, s60\n\t"
    "v_mfma_f32_16x16x32_bf16 a[236:239], v[44:47], v[60:63], a[236:239]\n\t"
    "v_mfma_f32_16x16x32_bf16 a[232:235], v[44:47], v[56:59], a[232:235]\n\t"
    "v_perm_b32 v38, v230, v226, s61\n\t"
    "v_mfma_f32_16x16x32_bf16 a[200:203], v[40:43], v[56:59], a[200:203]\n\t"
    "buffer_load_dwordx4 v[204:207], v5, s[96:99], s62 offen offset:3072\n\t"
    "v_mfma_f32_16x16x32_bf16 a[196:199], v[40:43], v[52:55], a[196:199]\n\t"
    "v_perm_b32 v35, v238, v234, s60\n\t"
    "v_mfma_f32_16x16x32_bf16 a[228:231], v[44:47], v[52:55], a[228:231]\n\t"
    "v_mfma_f32_16x16x32_bf16 a[224:227], v[44:47], v[48:51], a[224:227]\n\t"
    "v_perm_b32 v39, v238, v234, s61\n\t"
    "v_mfma_f32_16x16x32_bf16 a[192:195], v[40:43], v[48:51], a[192:195]\n\t"
    "s_waitcnt lgkmcnt(0)\n\t"
    "v_mfma_f32_16x16x32_bf16 a[0:3], v[16:19], v[12:15], a[0:3]\n\t"
    "v_perm_b32 v40, v215, v211, s60\n\t"
    "v_mfma_f32_16x16x32_bf16 a[32:35], v[20:23], v[12:15], a[32:35]\n\t"
    "v_mfma_f32_16x16x32_bf16 a[36:39], v[20:23], v[48:51], a[36:39]\n\t"
    "v_perm_b32 v44, v215, v211, s61\n\t"
    "v_mfma_f32_16x16x32_bf16 a[4:7], v[16:19], v[48:51], a[4:7]\n\t"
    "ds_read_b128 v[76:79], v9 offset:52736\n\t"
    "v_mfma_f32_16x16x32_bf16 a[8:11], v[16:19], v[52:55], a[8:11]\n\t"
    "v_perm_b32 v41, v223, v219, s60\n\t"
    "v_mfma_f32_16x16x32_bf16 a[40:43], v[20:23], v[52:55], a[40:43]\n\t"
    "v_mfma_f32_16x16x32_bf16 a[44:47], v[20:23], v[56:59], a[44:47]\n\t"
    "v_perm_b32 v45, v223, v219, s61\n\t"
    "v_mfma_f32_16x16x32_bf16 a[12:15], v[16:19], v[56:59], a[12:15]\n\t"
    "v_mfma_f32_16x16x32_bf16 a[16:19], v[16:19], v[60:63], a[16:19]\n\t"
    "v_perm_b32 v42, v231, v227, s60\n\t"
    "v_mfma_f32_16x16x32_bf16 a[48:51], v[20:23], v[60:63], a[48:51]\n\t"
    "v_mfma_f32_16x16x32_bf16 a[52:55], v[20:23], v[64:67], a[52:55]\n\t"
    "v_perm_b32 v46, v231, v227, s61\n\t"
    "v_mfma_f32_16x16x32_bf16 a[20:23], v[16:19], v[64:67], a[20:23]\n\t"
    "v_mfma_f32_16x16x32_bf16 a[24:27], v[16:19], v[68:71], a[24:27]\n\t"
    "v_perm_b32 v43, v239, v235, s60\n\t"
    "v_mfma_f32_16x16x32_bf16 a[56:59], v[20:23], v[68:71], a[56:59]\n\t"
    "v_mfma_f32_16x16x32_bf16 a[60:63], v[20:23], v[72:75], a[60:63]\n\t"
    "v_perm_b32 v47, v239, v235, s61\n\t"
    "v_mfma_f32_16x16x32_bf16 a[28:31], v[16:19], v[72:75], a[28:31]\n\t"
    "s_waitcnt vmcnt(16)\n\t"
    "s_add_u32 s62, %[tb], 0x38000\n\t"
    "v_mfma_f32_16x16x32_bf16 a[92:95], v[24:27], v[72:75], a[92:95]\n\t"
    "v_perm_b32 v16, v116, v112, s60\n\t"
    "v_mfma_f32_16x16x32_bf16 a[124:127], v[28:31], v[72:75], a[124:127]\n\t"
    "buffer_load_dwordx4 v[208:211], v4, s[96:99], s62 offen\n\t"
    "v_mfma_f32_16x16x32_bf16 a[120:123], v[28:31], v[68:71], a[120:123]\n\t"
    "v_perm_b32 v20, v116, v112, s61\n\t"
    "v_mfma_f32_16x16x32_bf16 a[88:91], v[24:27], v[68:71], a[88:91]\n\t"
    "v_mfma_f32_16x16x32_bf16 a[84:87], v[24:27], v[64:67], a[84:87]\n\t"
    "v_perm_b32 v17, v124, v120, s60\n\t"
    "v_mfma_f32_16x16x32_bf16 a[116:119], v[28:31], v[64:67], a[116:119]\n\t"
    "v_mfma_f32_16x16x32_bf16 a[112:115], v[28:31], v[60:63], a[112:115]\n\t"
    "v_perm_b32 v21, v124, v120, s61\n\t"
    "v_mfma_f32_16x16x32_bf16 a[80:83], v[24:27], v[60:63], a[80:83]\n\t"
    "buffer_load_dwordx4 v[212:215], v4, s[96:99], s62 offen offset:1024\n\t"
    "v_mfma_f32_16x16x32_bf16 a[76:79], v[24:27], v[56:59], a[76:79]\n\t"
    "v_perm_b32 v18, v132, v128, s60\n\t"
    "v_mfma_f32_16x16x32_bf16 a[108:111], v[28:31], v[56:59], a[108:111]\n\t"
    "v_mfma_f32_16x16x32_bf16 a[104:107], v[28:31], v[52:55], a[104:107]\n\t"
    "v_perm_b32 v22, v132, v128, s61\n\t"
    "v_mfma_f32_16x16x32_bf16 a[72:75], v[24:27], v[52:55], a[72:75]\n\t"
    "v_mfma_f32_16x16x32_bf16 a[68:71], v[24:27], v[48:51], a[68:71]\n\t"
    "v_perm_b32 v19, v140, v136, s60\n\t"
    "v_mfma_f32_16x16x32_bf16 a[100:103], v[28:31], v[48:51], a[100:103]\n\t"
    "buffer_load_dwordx4 v[216:219], v4, s[96:99], s62 offen offset:2048\n\t"
    "v_mfma_f32_16x16x32_bf16 a[96:99], v[28:31], v[12:15], a[96:99]\n\t"
    "v_perm_b32 v23, v140, v136, s61\n\t"
    "v_mfma_f32_16x16x32_bf16 a[64:67], v[24:27], v[12:15], a[64:67]\n\t"
    "v_mfma_f32_16x16x32_bf16 a[128:131], v[32:35], v[12:15], a[128:131]\n\t"
    "v_perm_b32 v24, v117, v113, s60\n\t"
    "v_mfma_f32_16x16x32_bf16 a[160:163], v[36:39], v[12:15], a[160:163]\n\t"
    "v_mfma_f32_16x16x32_bf16 a[164:167], v[36:39], v[48:51], a[164:167]\n\t"
    "v_perm_b32 v28, v117, v113, s61\n\t"
    "v_mfma_f32_16x16x32_bf16 a[132:135], v[32:35], v[48:51], a[132:135]\n\t"
    "buffer_load_dwordx4 v[220:223], v4, s[96:99], s62 offen offset:3072\n\t"
    "v_mfma_f32_16x16x32_bf16 a[136:139], v[32:35], v[52:55], a[136:139]\n\t"
    "v_perm_b32 v25, v125, v121, s60\n\t"
    "v_mfma_f32_16x16x32_bf16 a[168:171], v[36:39], v[52:55], a[168:171]\n\t"
    "v_mfma_f32_16x16x32_bf16 a[172:175], v[36:39], v[56:59], a[172:175]\n\t"
    "v_perm_b32 v29, v125, v121, s61\n\t"
    "v_mfma_f32_16x16x32_bf16 a[140:143], v[32:35], v[56:59], a[140:143]\n\t"
    "v_mfma_f32_16x16x32_bf16 a[144:147], v[32:35], v[60:63], a[144:147]\n\t"
    "v_perm_b32 v26, v133, v129, s60\n\t"
    "v_mfma_f32_16x16x32_bf16 a[176:179], v[36:39], v[60:63], a[176:179]\n\t"
    "buffer_load_dwordx4 v[224:227], v5, s[96:99], s62 offen\n\t"
    "v_mfma_f32_16x16x32_bf16 a[180:183], v[36:39], v[64:67], a[180:183]\n\t"
    "v_perm_b32 v30, v133, v129, s61\n\t"
    "v_mfma_f32_16x16x32_bf16 a[148:151], v[32:35], v[64:67], a[148:151]\n\t"
    "v_mfma_f32_16x16x32_bf16 a[152:155], v[32:35], v[68:71], a[152:155]\n\t"
    "v_perm_b32 v27, v141, v137, s60\n\t"
    "v_mfma_f32_16x16x32_bf16 a[184:187], v[36:39], v[68:71], a[184:187]\n\t"
    "v_mfma_f32_16x16x32_bf16 a[188:191], v[36:39], v[72:75], a[188:191]\n\t"
    "v_perm_b32 v31, v141, v137, s61\n\t"
    "v_mfma_f32_16x16x32_bf16 a[156:159], v[32:35], v[72:75], a[156:159]\n\t"
    "buffer_load_dwordx4 v[228:231], v5, s[96:99], s62 offen offset:1024\n\t"
    "v_mfma_f32_16x16x32_bf16 a[220:223], v[40:43], v[72:75], a[220:223]\n\t"
    "v_perm_b32 v32, v118, v114, s60\n\t"
    "v_mfma_f32_16x16x32_bf16 a[252:255], v[44:47], v[72:75], a[252:255]\n\t"
    "v_mfma_f32_16x16x32_bf16 a[248:251], v[44:47], v[68:71], a[248:251]\n\t"
    "v_perm_b32 v36, v118, v114, s61\n\t"
    "v_mfma_f32_16x16x32_bf16 a[216:219], v[40:43], v[68:71], a[216:219]\n\t"
    "v_mfma_f32_16x16x32_bf16 a[212:215], v[40:43], v[64:67], a[212:215]\n\t"
    "v_perm_b32 v33, v126, v122, s60\n\t"
    "v_mfma_f32_16x16x32_bf16 a[244:247], v[44:47], v[64:67], a[244:247]\n\t"
    "buffer_load_dwordx4 v[232:235], v5, s[96:99], s62 offen offset:2048\n\t"
    "v_mfma_f32_16x16x32_bf16 a[240:243], v[44:47], v[60:63], a[240:243]\n\t"
    "v_perm_b32 v37, v126, v122, s61\n\t"
    "v_mfma_f32_16x16x32_bf16 a[208:211], v[40:43], v[60:63], a[208:211]\n\t"
    "v_mfma_f32_16x16x32_bf16 a[204:207], v[40:43], v[56:59], a[204:207]\n\t"
    "v_perm_b32 v34, v134, v130, s60\n\t"
    "v_mfma_f32_16x16x32_bf16 a[236:239], v[44:47], v[56:59], a[236:239]\n\t"
    "v_mfma_f32_16x16x32_bf16 a[232:235], v[44:47], v[52:55], a[232:235]\n\t"
    "v_perm_b32 v38, v134, v130, s61\n\t"
    "v_mfma_f32_16x16x32_bf16 a[200:203], v[40:43], v[52:55], a[200:203]\n\t"
    "buffer_load_dwordx4 v[236:239], v5, s[96:99], s62 offen offset:3072\n\t"
    "v_mfma_f32_16x16x32_bf16 a[196:199], v[40:43], v[48:51], a[196:199]\n\t"
    "v_perm_b32 v35, v142, v138, s60\n\t"
    "v_mfma_f32_16x16x32_bf16 a[228:231], v[44:47], v[48:51], a[228:231]\n\t"
    "v_mfma_f32_16x16x32_bf16 a[224:227], v[44:47], v[12:15], a[224:227]\n\t"
    "v_perm_b32 v39, v142, v138, s61\n\t"
    "v_mfma_f32_16x16x32_bf16 a[192:195], v[40:43], v[12:15], a[192:195]\n\t"
    "s_waitcnt lgkmcnt(0)\n\t"
    "v_mfma_f32_16x16x32_bf16 a[0:3], v[16:19], v[76:79], a[0:3]\n\t"
    "v_perm_b32 v40, v119, v115, s60\n\t"
    "v_mfma_f32_16x16x32_bf16 a[32:35], v[20:23], v[76:79], a[32:35]\n\t"
    "v_mfma_f32_16x16x32_bf16 a[36:39], v[20:23], v[12:15], a[36:39]\n\t"
    "v_perm_b32 v44, v119, v115, s61\n\t"
    "v_mfma_f32_16x16x32_bf16 a[4:7], v[16:19], v[12:15], a[4:7]\n\t"
    "ds_read_b128 v[72:75], v9 offset:52480\n\t"
    "v_mfma_f32_16x16x32_bf16 a[8:11], v[16:19], v[48:51], a[8:11]\n\t"
    "v_perm_b32 v41, v127, v123, s60\n\t"
    "v_mfma_f32_16x16x32_bf16 a[40:43], v[20:23], v[48:51], a[40:43]\n\t"
    "v_mfma_f32_16x16x32_bf16 a[44:47], v[20:23], v[52:55], a[44:47]\n\t"
    "v_perm_b32 v45, v127, v123, s61\n\t"
    "v_mfma_f32_16x16x32_bf16 a[12:15], v[16:19], v[52:55], a[12:15]\n\t"
    "v_mfma_f32_16x16x32_bf16 a[16:19], v[16:19], v[56:59], a[16:19]\n\t"
    "v_perm_b32 v42, v135, v131, s60\n\t"
    "v_mfma_f32_16x16x32_bf16 a[48:51], v[20:23], v[56:59], a[48:51]\n\t"
    "v_mfma_f32_16x16x32_bf16 a[52:55], v[20:23], v[60:63], a[52:55]\n\t"
    "v_perm_b32 v46, v135, v131, s61\n\t"
    "v_mfma_f32_16x16x32_bf16 a[20:23], v[16:19], v[60:63], a[20:23]\n\t"
    "v_mfma_f32_16x16x32_bf16 a[24:27], v[16:19], v[64:67], a[24:27]\n\t"
    "v_perm_b32 v43, v143, v139, s60\n\t"
    "v_mfma_f32_16x16x32_bf16 a[56:59], v[20:23], v[64:67], a[56:59]\n\t"
    "v_mfma_f32_16x16x32_bf16 a[60:63], v[20:23], v[68:71], a[60:63]\n\t"
    "v_perm_b32 v47, v143, v139, s61\n\t"
    "v_mfma_f32_16x16x32_bf16 a[28:31], v[16:19], v[68:71], a[28:31]\n\t"
    "s_waitcnt vmcnt(16)\n\t"
    "s_add_u32 s62, s63, 0x38000\n\t"
    "v_mfma_f32_16x16x32_bf16 a[92:95], v[24:27], v[68:71], a[92:95]\n\t"
    "v_perm_b32 v16, v148, v144, s60\n\t"
    "v_mfma_f32_16x16x32_bf16 a[124:127], v[28:31], v[68:71], a[124:127]\n\t"
    "buffer_load_dwordx4 v[112:115], v4, s[96:99], s62 offen\n\t"
    "v_mfma_f32_16x16x32_bf16 a[120:123], v[28:31], v[64:67], a[120:123]\n\t"
    "v_perm_b32 v20, v148, v144, s61\n\t"
    "v_mfma_f32_16x16x32_bf16 a[88:91], v[24:27], v[64:67], a[88:91]\n\t"
    "v_mfma_f32_16x16x32_bf16 a[84:87], v[24:27], v[60:63], a[84:87]\n\t"
    "v_perm_b32 v17, v156, v152, s60\n\t"
    "v_mfma_f32_16x16x32_bf16 a[116:119], v[28:31], v[60:63], a[116:119]\n\t"
    "v_mfma_f32_16x16x32_bf16 a[112:115], v[28:31], v[56:59], a[112:115]\n\t"
    "v_perm_b32 v21, v156, v152, s61\n\t"
    "v_mfma_f32_16x16x32_bf16 a[80:83], v[24:27], v[56:59], a[80:83]\n\t"
    "buffer_load_dwordx4 v[116:119], v4, s[96:99], s62 offen offset:1024\n\t"
    "v_mfma_f32_16x16x32_bf16 a[76:79], v[24:27], v[52:55], a[76:79]\n\t"
    "v_perm_b32 v18, v164, v160, s60\n\t"
    "v_mfma_f32_16x16x32_bf16 a[108:111], v[28:31], v[52:55], a[108:111]\n\t"
    "v_mfma_f32_16x16x32_bf16 a[104:107], v[28:31], v[48:51], a[104:107]\n\t"
    "v_perm_b32 v22, v164, v160, s61\n\t"
    "v_mfma_f32_16x16x32_bf16 a[72:75], v[24:27], v[48:51], a[72:75]\n\t"
    "v_mfma_f32_16x16x32_bf16 a[68:71], v[24:27], v[12:15], a[68:71]\n\t"
    "v_perm_b32 v19, v172, v168, s60\n\t"
    "v_mfma_f32_16x16x32_bf16 a[100:103], v[28:31], v[12:15], a[100:103]\n\t"
    "buffer_load_dwordx4 v[120:123], v4, s[96:99], s62 offen offset:2048\n\t"
    "v_mfma_f32_16x16x32_bf16 a[96:99], v[28:31], v[76:79], a[96:99]\n\t"
    "v_perm_b32 v23, v172, v168, s61\n\t"
    "v_mfma_f32_16x16x32_bf16 a[64:67], v[24:27], v[76:79], a[64:67]\n\t"
    "v_mfma_f32_16x16x32_bf16 a[128:131], v[32:35], v[76:79], a[128:131]\n\t"
    "v_perm_b32 v24, v149, v145, s60\n\t"
    "v_mfma_f32_16x16x32_bf16 a[160:163], v[36:39], v[76:79], a[160:163]\n\t"
    "v_mfma_f32_16x16x32_bf16 a[164:167], v[36:39], v[12:15], a[164:167]\n\t"
    "v_perm_b32 v28, v149, v145, s61\n\t"
    "v_mfma_f32_16x16x32_bf16 a[132:135], v[32:35], v[12:15], a[132:135]\n\t"
    "buffer_load_dwordx4 v[124:127], v4, s[96:99], s62 offen offset:3072\n\t"
    "v_mfma_f32_16x16x32_bf16 a[136:139], v[32:35], v[48:51], a[136:139]\n\t"
    "v_perm_b32 v25, v157, v153, s60\n\t"
    "v_mfma_f32_16x16x32_bf16 a[168:171], v[36:39], v[48:51], a[168:171]\n\t"
    "v_mfma_f32_16x16x32_bf16 a[172:175], v[36:39], v[52:55], a[172:175]\n\t"
    "v_perm_b32 v29, v157, v153, s61\n\t"
    "v_mfma_f32_16x16x32_bf16 a[140:143], v[32:35], v[52:55], a[140:143]\n\t"
    "v_mfma_f32_16x16x32_bf16 a[144:147], v[32:35], v[56:59], a[144:147]\n\t"
    "v_perm_b32 v26, v165, v161, s60\n\t"
    "v_mfma_f32_16x16x32_bf16 a[176:179], v[36:39], v[56:59], a[176:179]\n\t"
    "buffer_load_dwordx4 v[128:131], v5, s[96:99], s62 offen\n\t"
    "v_mfma_f32_16x16x32_bf16 a[180:183], v[36:39], v[60:63], a[180:183]\n\t"
    "v_perm_b32 v30, v165, v161, s61\n\t"
    "v_mfma_f32_16x16x32_bf16 a[148:151], v[32:35], v[60:63], a[148:151]\n\t"
    "v_mfma_f32_16x16x32_bf16 a[152:155], v[32:35], v[64:67], a[152:155]\n\t"
    "v_perm_b32 v27, v173, v169, s60\n\t"
    "v_mfma_f32_16x16x32_bf16 a[184:187], v[36:39], v[64:67], a[184:187]\n\t"
    "v_mfma_f32_16x16x32_bf16 a[188:191], v[36:39], v[68:71], a[188:191]\n\t"
    "v_perm_b32 v31, v173, v169, s61\n\t"
    "v_mfma_f32_16x16x32_bf16 a[156:159], v[32:35], v[68:71], a[156:159]\n\t"
    "buffer_load_dwordx4 v[132:135], v5, s[96:99], s62 offen offset:1024\n\t"
    "v_mfma_f32_16x16x32_bf16 a[220:223], v[40:43], v[68:71], a[220:223]\n\t"
    "v_perm_b32 v32, v150, v146, s60\n\t"
    "v_mfma_f32_16x16x32_bf16 a[252:255], v[44:47], v[68:71], a[252:255]\n\t"
    "v_mfma_f32_16x16x32_bf16 a[248:251], v[44:47], v[64:67], a[248:251]\n\t"
    "v_perm_b32 v36, v150, v146, s61\n\t"
    "v_mfma_f32_16x16x32_bf16 a[216:219], v[40:43], v[64:67], a[216:219]\n\t"
    "v_mfma_f32_16x16x32_bf16 a[212:215], v[40:43], v[60:63], a[212:215]\n\t"
    "v_perm_b32 v33, v158, v154, s60\n\t"
    "v_mfma_f32_16x16x32_bf16 a[244:247], v[44:47], v[60:63], a[244:247]\n\t"
    "buffer_load_dwordx4 v[136:139], v5, s[96:99], s62 offen offset:2048\n\t"
    "v_mfma_f32_16x16x32_bf16 a[240:243], v[44:47], v[56:59], a[240:243]\n\t"
    "v_perm_b32 v37, v158, v154, s61\n\t"
    "v_mfma_f32_16x16x32_bf16 a[208:211], v[40:43], v[56:59], a[208:211]\n\t"
    "v_mfma_f32_16x16x32_bf16 a[204:207], v[40:43], v[52:55], a[204:207]\n\t"
    "v_perm_b32 v34, v166, v162, s60\n\t"
    "v_mfma_f32_16x16x32_bf16 a[236:239], v[44:47], v[52:55], a[236:239]\n\t"
    "v_mfma_f32_16x16x32_bf16 a[232:235], v[44:47], v[48:51], a[232:235]\n\t"
    "v_perm_b32 v38, v166, v162, s61\n\t"
    "v_mfma_f32_16x16x32_bf16 a[200:203], v[40:43], v[48:51], a[200:203]\n\t"
    "buffer_load_dwordx4 v[140:143], v5, s[96:99], s62 offen offset:3072\n\t"
    "v_mfma_f32_16x16x32_bf16 a[196:199], v[40:43], v[12:15], a[196:199]\n\t"
    "v_perm_b32 v35, v174, v170, s60\n\t"
    "v_mfma_f32_16x16x32_bf16 a[228:231], v[44:47], v[12:15], a[228:231]\n\t"
    "v_mfma_f32_16x16x32_bf16 a[224:227], v[44:47], v[76:79], a[224:227]\n\t"
    "v_perm_b32 v39, v174, v170, s61\n\t"
    "v_mfma_f32_16x16x32_bf16 a[192:195], v[40:43], v[76:79], a[192:195]\n\t"
    "s_waitcnt lgkmcnt(0)\n\t"
    "v_mfma_f32_16x16x32_bf16 a[0:3], v[16:19], v[72:75], a[0:3]\n\t"
    "v_perm_b32 v40, v151, v147, s60\n\t"
    "v_mfma_f32_16x16x32_bf16 a[32:35], v[20:23], v[72:75], a[32:35]\n\t"
    "v_mfma_f32_16x16x32_bf16 a[36:39], v[20:23], v[76:79], a[36:39]\n\t"
    "v_perm_b32 v44, v151, v147, s61\n\t"
    "v_mfma_f32_16x16x32_bf16 a[4:7], v[16:19], v[76:79], a[4:7]\n\t"
    "ds_read_b128 v[68:71], v9 offset:52224\n\t"
    "v_mfma_f32_16x16x32_bf16 a[8:11], v[16:19], v[12:15], a[8:11]\n\t"
    "v_perm_b32 v41, v159, v155, s60\n\t"
    "v_mfma_f32_16x16x32_bf16 a[40:43], v[20:23], v[12:15], a[40:43]\n\t"
    "v_mfma_f32_16x16x32_bf16 a[44:47], v[20:23], v[48:51], a[44:47]\n\t"
    "v_perm_b32 v45, v159, v155, s61\n\t"
    "v_mfma_f32_16x16x32_bf16 a[12:15], v[16:19], v[48:51], a[12:15]\n\t"
    "v_mfma_f32_16x16x32_bf16 a[16:19], v[16:19], v[52:55], a[16:19]\n\t"
    "v_perm_b32 v42, v167, v163, s60\n\t"
    "v_mfma_f32_16x16x32_bf16 a[48:51], v[20:23], v[52:55], a[48:51]\n\t"
    "v_mfma_f32_16x16x32_bf16 a[52:55], v[20:23], v[56:59], a[52:55]\n\t"
    "v_perm_b32 v46, v167, v163, s61\n\t"
    "v_mfma_f32_16x16x32_bf16 a[20:23], v[16:19], v[56:59], a[20:23]\n\t"
    "ds_read_b128 v[80:83], v10 offset:53248\n\t"
    "v_mfma_f32_16x16x32_bf16 a[24:27], v[16:19], v[60:63], a[24:27]\n\t"
    "v_perm_b32 v43, v175, v171, s60\n\t"
    "v_mfma_f32_16x16x32_bf16 a[56:59], v[20:23], v[60:63], a[56:59]\n\t"
    "v_mfma_f32_16x16x32_bf16 a[60:63], v[20:23], v[64:67], a[60:63]\n\t"
    "v_perm_b32 v47, v175, v171, s61\n\t"
    "v_mfma_f32_16x16x32_bf16 a[28:31], v[16:19], v[64:67], a[28:31]\n\t"
    "s_waitcnt vmcnt(16)\n\t"
    "s_mov_b32 s62, 0xb8000\n\t"
    "v_mfma_f32_16x16x32_bf16 a[92:95], v[24:27], v[64:67], a[92:95]\n\t"
    "v_perm_b32 v16, v180, v176, s60\n\t"
    "v_mfma_f32_16x16x32_bf16 a[124:127], v[28:31], v[64:67], a[124:127]\n\t"
    "buffer_load_dwordx4 v[144:147], v4, s[96:99], s62 offen\n\t"
    "v_mfma_f32_16x16x32_bf16 a[120:123], v[28:31], v[60:63], a[120:123]\n\t"
    "v_perm_b32 v20, v180, v176, s61\n\t"
    "v_mfma_f32_16x16x32_bf16 a[88:91], v[24:27], v[60:63], a[88:91]\n\t"
    "ds_read_b128 v[84:87], v10 offset:53504\n\t"
    "v_mfma_f32_16x16x32_bf16 a[84:87], v[24:27], v[56:59], a[84:87]\n\t"
    "v_perm_b32 v17, v188, v184, s60\n\t"
    "v_mfma_f32_16x16x32_bf16 a[116:119], v[28:31], v[56:59], a[116:119]\n\t"
    "v_mfma_f32_16x16x32_bf16 a[112:115], v[28:31], v[52:55], a[112:115]\n\t"
    "v_perm_b32 v21, v188, v184, s61\n\t"
    "v_mfma_f32_16x16x32_bf16 a[80:83], v[24:27], v[52:55], a[80:83]\n\t"
    "buffer_load_dwordx4 v[148:151], v4, s[96:99], s62 offen offset:1024\n\t"
    "v_mfma_f32_16x16x32_bf16 a[76:79], v[24:27], v[48:51], a[76:79]\n\t"
    "v_perm_b32 v18, v196, v192, s60\n\t"
    "v_mfma_f32_16x16x32_bf16 a[108:111], v[28:31], v[48:51], a[108:111]\n\t"
    "v_mfma_f32_16x16x32_bf16 a[104:107], v[28:31], v[12:15], a[104:107]\n\t"
    "v_perm_b32 v22, v196, v192, s61\n\t"
    "v_mfma_f32_16x16x32_bf16 a[72:75], v[24:27], v[12:15], a[72:75]\n\t"
    "ds_read_b128 v[88:91], v10 offset:53760\n\t"
    "v_mfma_f32_16x16x32_bf16 a[68:71], v[24:27], v[76:79], a[68:71]\n\t"
    "v_perm_b32 v19, v204, v200, s60\n\t"
    "v_mfma_f32_16x16x32_bf16 a[100:103], v[28:31], v[76:79], a[100:103]\n\t"
    "buffer_load_dwordx4 v[152:155], v4, s[96:99], s62 offen offset:2048\n\t"
    "v_mfma_f32_16x16x32_bf16 a[96:99], v[28:31], v[72:75], a[96:99]\n\t"
    "v_perm_b32 v23, v204, v200, s61\n\t"
    "v_mfma_f32_16x16x32_bf16 a[64:67], v[24:27], v[72:75], a[64:67]\n\t"
    "v_mfma_f32_16x16x32_bf16 a[128:131], v[32:35], v[72:75], a[128:131]\n\t"
    "v_perm_b32 v24, v181, v177, s60\n\t"
    "v_mfma_f32_16x16x32_bf16 a[160:163], v[36:39], v[72:75], a[160:163]\n\t"
    "v_mfma_f32_16x16x32_bf16 a[164:167], v[36:39], v[76:79], a[164:167]\n\t"
    "v_perm_b32 v28, v181, v177, s61\n\t"
    "v_mfma_f32_16x16x32_bf16 a[132:135], v[32:35], v[76:79], a[132:135]\n\t"
    "buffer_load_dwordx4 v[156:159], v4, s[96:99], s62 offen offset:3072\n\t"
    "ds_read_b128 v[92:95], v10 offset:54016\n\t"
    "v_mfma_f32_16x16x32_bf16 a[136:139], v[32:35], v[12:15], a[136:139]\n\t"
    "v_perm_b32 v25, v189, v185, s60\n\t"
    "v_mfma_f32_16x16x32_bf16 a[168:171], v[36:39], v[12:15], a[168:171]\n\t"
    "v_mfma_f32_16x16x32_bf16 a[172:175], v[36:39], v[48:51], a[172:175]\n\t"
    "v_perm_b32 v29, v189, v185, s61\n\t"
    "v_mfma_f32_16x16x32_bf16 a[140:143], v[32:35], v[48:51], a[140:143]\n\t"
    "v_mfma_f32_16x16x32_bf16 a[144:147], v[32:35], v[52:55], a[144:147]\n\t"
    "v_perm_b32 v26, v197, v193, s60\n\t"
    "v_mfma_f32_16x16x32_bf16 a[176:179], v[36:39], v[52:55], a[176:179]\n\t"
    "buffer_load_dwordx4 v[160:163], v5, s[96:99], s62 offen\n\t"
    "v_mfma_f32_16x16x32_bf16 a[180:183], v[36:39], v[56:59], a[180:183]\n\t"
    "v_perm_b32 v30, v197, v193, s61\n\t"
    "v_mfma_f32_16x16x32_bf16 a[148:151], v[32:35], v[56:59], a[148:151]\n\t"
    "v_mfma_f32_16x16x32_bf16 a[152:155], v[32:35], v[60:63], a[152:155]\n\t"
    "v_perm_b32 v27, v205, v201, s60\n\t"
    "v_mfma_f32_16x16x32_bf16 a[184:187], v[36:39], v[60:63], a[184:187]\n\t"
    "v_mfma_f32_16x16x32_bf16 a[188:191], v[36:39], v[64:67], a[188:191]\n\t"
    "v_perm_b32 v31, v205, v201, s61\n\t"
    "v_mfma_f32_16x16x32_bf16 a[156:159], v[32:35], v[64:67], a[156:159]\n\t"
    "buffer_load_dwordx4 v[164:167], v5, s[96:99], s62 offen offset:1024\n\t"
    "v_mfma_f32_16x16x32_bf16 a[220:223], v[40:43], v[64:67], a[220:223]\n\t"
    "v_perm_b32 v32, v182, v178, s60\n\t"
    "v_mfma_f32_16x16x32_bf16 a[252:255], v[44:47], v[64:67], a[252:255]\n\t"
    "v_mfma_f32_16x16x32_bf16 a[248:251], v[44:47], v[60:63], a[248:251]\n\t"
    "v_perm_b32 v36, v182, v178, s61\n\t"
    "v_mfma_f32_16x16x32_bf16 a[216:219], v[40:43], v[60:63], a[216:219]\n\t"
    "v_mfma_f32_16x16x32_bf16 a[212:215], v[40:43], v[56:59], a[212:215]\n\t"
    "v_perm_b32 v33, v190, v186, s60\n\t"
    "v_mfma_f32_16x16x32_bf16 a[244:247], v[44:47], v[56:59], a[244:247]\n\t"
    "buffer_load_dwordx4 v[168:171], v5, s[96:99], s62 offen offset:2048\n\t"
    "v_mfma_f32_16x16x32_bf16 a[240:243], v[44:47], v[52:55], a[240:243]\n\t"
    "v_perm_b32 v37, v190, v186, s61\n\t"
    "v_mfma_f32_16x16x32_bf16 a[208:211], v[40:43], v[52:55], a[208:211]\n\t"
    "v_mfma_f32_16x16x32_bf16 a[204:207], v[40:43], v[48:51], a[204:207]\n\t"
    "v_perm_b32 v34, v198, v194, s60\n\t"
    "v_mfma_f32_16x16x32_bf16 a[236:239], v[44:47], v[48:51], a[236:239]\n\t"
    "v_mfma_f32_16x16x32_bf16 a[232:235], v[44:47], v[12:15], a[232:235]\n\t"
    "v_perm_b32 v38, v198, v194, s61\n\t"
    "v_mfma_f32_16x16x32_bf16 a[200:203], v[40:43], v[12:15], a[200:203]\n\t"
    "buffer_load_dwordx4 v[172:175], v5, s[96:99], s62 offen offset:3072\n\t"
    "v_mfma_f32_16x16x32_bf16 a[196:199], v[40:43], v[76:79], a[196:199]\n\t"
    "v_perm_b32 v35, v206, v202, s60\n\t"
    "v_mfma_f32_16x16x32_bf16 a[228:231], v[44:47], v[76:79], a[228:231]\n\t"
    "v_mfma_f32_16x16x32_bf16 a[224:227], v[44:47], v[72:75], a[224:227]\n\t"
    "v_perm_b32 v39, v206, v202, s61\n\t"
    "v_mfma_f32_16x16x32_bf16 a[192:195], v[40:43], v[72:75], a[192:195]\n\t"
    "s_waitcnt lgkmcnt(0)\n\t"
    "v_mfma_f32_16x16x32_bf16 a[0:3], v[16:19], v[68:71], a[0:3]\n\t"
    "v_perm_b32 v40, v183, v179, s60\n\t"
    "v_mfma_f32_16x16x32_bf16 a[32:35], v[20:23], v[68:71], a[32:35]\n\t"
    "v_mfma_f32_16x16x32_bf16 a[36:39], v[20:23], v[72:75], a[36:39]\n\t"
    "v_perm_b32 v44, v183, v179, s61\n\t"
    "v_mfma_f32_16x16x32_bf16 a[4:7], v[16:19], v[72:75], a[4:7]\n\t"
    "v_mfma_f32_16x16x32_bf16 a[8:11], v[16:19], v[76:79], a[8:11]\n\t"
    "v_perm_b32 v41, v191, v187, s60\n\t"
    "v_mfma_f32_16x16x32_bf16 a[40:43], v[20:23], v[76:79], a[40:43]\n\t"
    "v_mfma_f32_16x16x32_bf16 a[44:47], v[20:23], v[12:15], a[44:47]\n\t"
    "v_perm_b32 v45, v191, v187, s61\n\t"
    "v_mfma_f32_16x16x32_bf16 a[12:15], v[16:19], v[12:15], a[12:15]\n\t"
    "v_mfma_f32_16x16x32_bf16 a[16:19], v[16:19], v[48:51], a[16:19]\n\t"
    "v_perm_b32 v42, v199, v195, s60\n\t"
    "v_mfma_f32_16x16x32_bf16 a[48:51], v[20:23], v[48:51], a[48:51]\n\t"
    "v_mfma_f32_16x16x32_bf16 a[52:55], v[20:23], v[52:55], a[52:55]\n\t"
    "v_perm_b32 v46, v199, v195, s61\n\t"
    "v_mfma_f32_16x16x32_bf16 a[20:23], v[16:19], v[52:55], a[20:23]\n\t"
    "ds_read_b128 v[96:99], v10 offset:54272\n\t"
    "v_mfma_f32_16x16x32_bf16 a[24:27], v[16:19], v[56:59], a[24:27]\n\t"
    "v_perm_b32 v43, v207, v203, s60\n\t"
    "v_mfma_f32_16x16x32_bf16 a[56:59], v[20:23], v[56:59], a[56:59]\n\t"
    "v_mfma_f32_16x16x32_bf16 a[60:63], v[20:23], v[60:63], a[60:63]\n\t"
    "v_perm_b32 v47, v207, v203, s61\n\t"
    "v_mfma_f32_16x16x32_bf16 a[28:31], v[16:19], v[60:63], a[28:31]\n\t"
    "s_waitcnt vmcnt(16)\n\t"
    "s_add_u32 s62, s70, 0x38000\n\t"
    "v_mfma_f32_16x16x32_bf16 a[92:95], v[24:27], v[60:63], a[92:95]\n\t"
    "v_perm_b32 v16, v212, v208, s60\n\t"
    "v_mfma_f32_16x16x32_bf16 a[124:127], v[28:31], v[60:63], a[124:127]\n\t"
    "buffer_load_dwordx4 v[176:179], v4, s[96:99], s62 offen\n\t"
    "v_mfma_f32_16x16x32_bf16 a[120:123], v[28:31], v[56:59], a[120:123]\n\t"
    "v_perm_b32 v20, v212, v208, s61\n\t"
    "v_mfma_f32_16x16x32_bf16 a[88:91], v[24:27], v[56:59], a[88:91]\n\t"
    "ds_read_b128 v[100:103], v10 offset:54528\n\t"
    "v_mfma_f32_16x16x32_bf16 a[84:87], v[24:27], v[52:55], a[84:87]\n\t"
    "v_perm_b32 v17, v220, v216, s60\n\t"
    "v_mfma_f32_16x16x32_bf16 a[116:119], v[28:31], v[52:55], a[116:119]\n\t"
    "v_mfma_f32_16x16x32_bf16 a[112:115], v[28:31], v[48:51], a[112:115]\n\t"
    "v_perm_b32 v21, v220, v216, s61\n\t"
    "v_mfma_f32_16x16x32_bf16 a[80:83], v[24:27], v[48:51], a[80:83]\n\t"
    "buffer_load_dwordx4 v[180:183], v4, s[96:99], s62 offen offset:1024\n\t"
    "v_mfma_f32_16x16x32_bf16 a[76:79], v[24:27], v[12:15], a[76:79]\n\t"
    "v_perm_b32 v18, v228, v224, s60\n\t"
    "v_mfma_f32_16x16x32_bf16 a[108:111], v[28:31], v[12:15], a[108:111]\n\t"
    "v_mfma_f32_16x16x32_bf16 a[104:107], v[28:31], v[76:79], a[104:107]\n\t"
    "v_perm_b32 v22, v228, v224, s61\n\t"
    "v_mfma_f32_16x16x32_bf16 a[72:75], v[24:27], v[76:79], a[72:75]\n\t"
    "ds_read_b128 v[104:107], v10 offset:54784\n\t"
    "v_mfma_f32_16x16x32_bf16 a[68:71], v[24:27], v[72:75], a[68:71]\n\t"
    "v_perm_b32 v19, v236, v232, s60\n\t"
    "v_mfma_f32_16x16x32_bf16 a[100:103], v[28:31], v[72:75], a[100:103]\n\t"
    "buffer_load_dwordx4 v[184:187], v4, s[96:99], s62 offen offset:2048\n\t"
    "v_mfma_f32_16x16x32_bf16 a[96:99], v[28:31], v[68:71], a[96:99]\n\t"
    "v_perm_b32 v23, v236, v232, s61\n\t"
    "v_mfma_f32_16x16x32_bf16 a[64:67], v[24:27], v[68:71], a[64:67]\n\t"
    "v_mfma_f32_16x16x32_bf16 a[128:131], v[32:35], v[68:71], a[128:131]\n\t"
    "v_perm_b32 v24, v213, v209, s60\n\t"
    "v_mfma_f32_16x16x32_bf16 a[160:163], v[36:39], v[68:71], a[160:163]\n\t"
    "v_mfma_f32_16x16x32_bf16 a[164:167], v[36:39], v[72:75], a[164:167]\n\t"
    "v_perm_b32 v28, v213, v209, s61\n\t"
    "v_mfma_f32_16x16x32_bf16 a[132:135], v[32:35], v[72:75], a[132:135]\n\t"
    "buffer_load_dwordx4 v[188:191], v4, s[96:99], s62 offen offset:3072\n\t"
    "ds_read_b128 v[108:111], v10 offset:55040\n\t"
    "v_mfma_f32_16x16x32_bf16 a[136:139], v[32:35], v[76:79], a[136:139]\n\t"
    "v_perm_b32 v25, v221, v217, s60\n\t"
    "v_mfma_f32_16x16x32_bf16 a[168:171], v[36:39], v[76:79], a[168:171]\n\t"
    "v_mfma_f32_16x16x32_bf16 a[172:175], v[36:39], v[12:15], a[172:175]\n\t"
    "v_perm_b32 v29, v221, v217, s61\n\t"
    "v_mfma_f32_16x16x32_bf16 a[140:143], v[32:35], v[12:15], a[140:143]\n\t"
    "v_mfma_f32_16x16x32_bf16 a[144:147], v[32:35], v[48:51], a[144:147]\n\t"
    "v_perm_b32 v26, v229, v225, s60\n\t"
    "v_mfma_f32_16x16x32_bf16 a[176:179], v[36:39], v[48:51], a[176:179]\n\t"
    "buffer_load_dwordx4 v[192:195], v5, s[96:99], s62 offen\n\t"
    "v_mfma_f32_16x16x32_bf16 a[180:183], v[36:39], v[52:55], a[180:183]\n\t"
    "v_perm_b32 v30, v229, v225, s61\n\t"
    "v_mfma_f32_16x16x32_bf16 a[148:151], v[32:35], v[52:55], a[148:151]\n\t"
    "v_mfma_f32_16x16x32_bf16 a[152:155], v[32:35], v[56:59], a[152:155]\n\t"
    "v_perm_b32 v27, v237, v233, s60\n\t"
    "v_mfma_f32_16x16x32_bf16 a[184:187], v[36:39], v[56:59], a[184:187]\n\t"
    "v_mfma_f32_16x16x32_bf16 a[188:191], v[36:39], v[60:63], a[188:191]\n\t"
    "v_perm_b32 v31, v237, v233, s61\n\t"
    "v_mfma_f32_16x16x32_bf16 a[156:159], v[32:35], v[60:63], a[156:159]\n\t"
    "buffer_load_dwordx4 v[196:199], v5, s[96:99], s62 offen offset:1024\n\t"
    "v_mfma_f32_16x16x32_bf16 a[220:223], v[40:43], v[60:63], a[220:223]\n\t"
    "v_perm_b32 v32, v214, v210, s60\n\t"
    "v_mfma_f32_16x16x32_bf16 a[252:255], v[44:47], v[60:63], a[252:255]\n\t"
    "v_mfma_f32_16x16x32_bf16 a[248:251], v[44:47], v[56:59], a[248:251]\n\t"
    "v_perm_b32 v36, v214, v210, s61\n\t"
    "v_mfma_f32_16x16x32_bf16 a[216:219], v[40:43], v[56:59], a[216:219]\n\t"
    "v_mfma_f32_16x16x32_bf16 a[212:215], v[40:43], v[52:55], a[212:215]\n\t"
    "v_perm_b32 v33, v222, v218, s60\n\t"
    "v_mfma_f32_16x16x32_bf16 a[244:247], v[44:47], v[52:55], a[244:247]\n\t"
    "buffer_load_dwordx4 v[200:203], v5, s[96:99], s62 offen offset:2048\n\t"
    "v_mfma_f32_16x16x32_bf16 a[240:243], v[44:47], v[48:51], a[240:243]\n\t"
    "v_perm_b32 v37, v222, v218, s61\n\t"
    "v_mfma_f32_16x16x32_bf16 a[208:211], v[40:43], v[48:51], a[208:211]\n\t"
    "v_mfma_f32_16x16x32_bf16 a[204:207], v[40:43], v[12:15], a[204:207]\n\t"
    "v_perm_b32 v34, v230, v226, s60\n\t"
    "v_mfma_f32_16x16x32_bf16 a[236:239], v[44:47], v[12:15], a[236:239]\n\t"
    "v_mfma_f32_16x16x32_bf16 a[232:235], v[44:47], v[76:79], a[232:235]\n\t"
    "v_perm_b32 v38, v230, v226, s61\n\t"
    "v_mfma_f32_16x16x32_bf16 a[200:203], v[40:43], v[76:79], a[200:203]\n\t"
    "buffer_load_dwordx4 v[204:207], v5, s[96:99], s62 offen offset:3072\n\t"
    "v_mfma_f32_16x16x32_bf16 a[196:199], v[40:43], v[72:75], a[196:199]\n\t"
    "v_perm_b32 v35, v238, v234, s60\n\t"
    "v_mfma_f32_16x16x32_bf16 a[228:231], v[44:47], v[72:75], a[228:231]\n\t"
    "v_mfma_f32_16x16x32_bf16 a[224:227], v[44:47], v[68:71], a[224:227]\n\t"
    "v_perm_b32 v39, v238, v234, s61\n\t"
    "v_mfma_f32_16x16x32_bf16 a[192:195], v[40:43], v[68:71], a[192:195]\n\t"
    "s_waitcnt lgkmcnt(0)\n\t"
    "v_mfma_f32_16x16x32_bf16 a[0:3], v[16:19], v[80:83], a[0:3]\n\t"
    "v_perm_b32 v40, v215, v211, s60\n\t"
    "v_mfma_f32_16x16x32_bf16 a[32:35], v[20:23], v[80:83], a[32:35]\n\t"
    "v_mfma_f32_16x16x32_bf16 a[36:39], v[20:23], v[84:87], a[36:39]\n\t"
    "v_perm_b32 v44, v215, v211, s61\n\t"
    "v_mfma_f32_16x16x32_bf16 a[4:7], v[16:19], v[84:87], a[4:7]\n\t"
    "ds_read_b128 v[12:15], v11 offset:52992\n\t"
    "v_mfma_f32_16x16x32_bf16 a[8:11], v[16:19], v[88:91], a[8:11]\n\t"
    "v_perm_b32 v41, v223, v219, s60\n\t"
    "v_mfma_f32_16x16x32_bf16 a[40:43], v[20:23], v[88:91], a[40:43]\n\t"
    "v_mfma_f32_16x16x32_bf16 a[44:47], v[20:23], v[92:95], a[44:47]\n\t"
    "v_perm_b32 v45, v223, v219, s61\n\t"
    "v_mfma_f32_16x16x32_bf16 a[12:15], v[16:19], v[92:95], a[12:15]\n\t"
    "v_mfma_f32_16x16x32_bf16 a[16:19], v[16:19], v[96:99], a[16:19]\n\t"
    "v_perm_b32 v42, v231, v227, s60\n\t"
    "v_mfma_f32_16x16x32_bf16 a[48:51], v[20:23], v[96:99], a[48:51]\n\t"
    "v_mfma_f32_16x16x32_bf16 a[52:55], v[20:23], v[100:103], a[52:55]\n\t"
    "v_perm_b32 v46, v231, v227, s61\n\t"
    "v_mfma_f32_16x16x32_bf16 a[20:23], v[16:19], v[100:103], a[20:23]\n\t"
    "v_mfma_f32_16x16x32_bf16 a[24:27], v[16:19], v[104:107], a[24:27]\n\t"
    "v_perm_b32 v43, v239, v235, s60\n\t"
    "v_mfma_f32_16x16x32_bf16 a[56:59], v[20:23], v[104:107], a[56:59]\n\t"
    "v_mfma_f32_16x16x32_bf16 a[60:63], v[20:23], v[108:111], a[60:63]\n\t"
    "v_perm_b32 v47, v239, v235, s61\n\t"
    "v_mfma_f32_16x16x32_bf16 a[28:31], v[16:19], v[108:111], a[28:31]\n\t"
    "s_waitcnt vmcnt(16)\n\t"
    "s_add_u32 s62, s72, 0x38000\n\t"
    "v_mfma_f32_16x16x32_bf16 a[92:95], v[24:27], v[108:111], a[92:95]\n\t"
    "v_perm_b32 v16, v116, v112, s60\n\t"
    "v_mfma_f32_16x16x32_bf16 a[124:127], v[28:31], v[108:111], a[124:127]\n\t"
    "buffer_load_dwordx4 v[208:211], v4, s[96:99], s62 offen\n\t"
    "v_mfma_f32_16x16x32_bf16 a[120:123], v[28:31], v[104:107], a[120:123]\n\t"
    "v_perm_b32 v20, v116, v112, s61\n\t"
    "v_mfma_f32_16x16x32_bf16 a[88:91], v[24:27], v[104:107], a[88:91]\n\t"
    "v_mfma_f32_16x16x32_bf16 a[84:87], v[24:27], v[100:103], a[84:87]\n\t"
    "v_perm_b32 v17, v124, v120, s60\n\t"
    "v_mfma_f32_16x16x32_bf16 a[116:119], v[28:31], v[100:103], a[116:119]\n\t"
    "v_mfma_f32_16x16x32_bf16 a[112:115], v[28:31], v[96:99], a[112:115]\n\t"
    "v_perm_b32 v21, v124, v120, s61\n\t"
    "v_mfma_f32_16x16x32_bf16 a[80:83], v[24:27], v[96:99], a[80:83]\n\t"
    "buffer_load_dwordx4 v[212:215], v4, s[96:99], s62 offen offset:1024\n\t"
    "v_mfma_f32_16x16x32_bf16 a[76:79], v[24:27], v[92:95], a[76:79]\n\t"
    "v_perm_b32 v18, v132, v128, s60\n\t"
    "v_mfma_f32_16x16x32_bf16 a[108:111], v[28:31], v[92:95], a[108:111]\n\t"
    "v_mfma_f32_16x16x32_bf16 a[104:107], v[28:31], v[88:91], a[104:107]\n\t"
    "v_perm_b32 v22, v132, v128, s61\n\t"
    "v_mfma_f32_16x16x32_bf16 a[72:75], v[24:27], v[88:91], a[72:75]\n\t"
    "v_mfma_f32_16x16x32_bf16 a[68:71], v[24:27], v[84:87], a[68:71]\n\t"
    "v_perm_b32 v19, v140, v136, s60\n\t"
    "v_mfma_f32_16x16x32_bf16 a[100:103], v[28:31], v[84:87], a[100:103]\n\t"
    "buffer_load_dwordx4 v[216:219], v4, s[96:99], s62 offen offset:2048\n\t"
    "v_mfma_f32_16x16x32_bf16 a[96:99], v[28:31], v[80:83], a[96:99]\n\t"
    "v_perm_b32 v23, v140, v136, s61\n\t"
    "v_mfma_f32_16x16x32_bf16 a[64:67], v[24:27], v[80:83], a[64:67]\n\t"
    "v_mfma_f32_16x16x32_bf16 a[128:131], v[32:35], v[80:83], a[128:131]\n\t"
    "v_perm_b32 v24, v117, v113, s60\n\t"
    "v_mfma_f32_16x16x32_bf16 a[160:163], v[36:39], v[80:83], a[160:163]\n\t"
    "v_mfma_f32_16x16x32_bf16 a[164:167], v[36:39], v[84:87], a[164:167]\n\t"
    "v_perm_b32 v28, v117, v113, s61\n\t"
    "v_mfma_f32_16x16x32_bf16 a[132:135], v[32:35], v[84:87], a[132:135]\n\t"
    "buffer_load_dwordx4 v[220:223], v4, s[96:99], s62 offen offset:3072\n\t"
    "v_mfma_f32_16x16x32_bf16 a[136:139], v[32:35], v[88:91], a[136:139]\n\t"
    "v_perm_b32 v25, v125, v121, s60\n\t"
    "v_mfma_f32_16x16x32_bf16 a[168:171], v[36:39], v[88:91], a[168:171]\n\t"
    "v_mfma_f32_16x16x32_bf16 a[172:175], v[36:39], v[92:95], a[172:175]\n\t"
    "v_perm_b32 v29, v125, v121, s61\n\t"
    "v_mfma_f32_16x16x32_bf16 a[140:143], v[32:35], v[92:95], a[140:143]\n\t"
    "v_mfma_f32_16x16x32_bf16 a[144:147], v[32:35], v[96:99], a[144:147]\n\t"
    "v_perm_b32 v26, v133, v129, s60\n\t"
    "v_mfma_f32_16x16x32_bf16 a[176:179], v[36:39], v[96:99], a[176:179]\n\t"
    "buffer_load_dwordx4 v[224:227], v5, s[96:99], s62 offen\n\t"
    "v_mfma_f32_16x16x32_bf16 a[180:183], v[36:39], v[100:103], a[180:183]\n\t"
    "v_perm_b32 v30, v133, v129, s61\n\t"
    "v_mfma_f32_16x16x32_bf16 a[148:151], v[32:35], v[100:103], a[148:151]\n\t"
    "v_mfma_f32_16x16x32_bf16 a[152:155], v[32:35], v[104:107], a[152:155]\n\t"
    "v_perm_b32 v27, v141, v137, s60\n\t"
    "v_mfma_f32_16x16x32_bf16 a[184:187], v[36:39], v[104:107], a[184:187]\n\t"
    "v_mfma_f32_16x16x32_bf16 a[188:191], v[36:39], v[108:111], a[188:191]\n\t"
    "v_perm_b32 v31, v141, v137, s61\n\t"
    "v_mfma_f32_16x16x32_bf16 a[156:159], v[32:35], v[108:111], a[156:159]\n\t"
    "buffer_load_dwordx4 v[228:231], v5, s[96:99], s62 offen offset:1024\n\t"
    "v_mfma_f32_16x16x32_bf16 a[220:223], v[40:43], v[108:111], a[220:223]\n\t"
    "v_perm_b32 v32, v118, v114, s60\n\t"
    "v_mfma_f32_16x16x32_bf16 a[252:255], v[44:47], v[108:111], a[252:255]\n\t"
    "v_mfma_f32_16x16x32_bf16 a[248:251], v[44:47], v[104:107], a[248:251]\n\t"
    "v_perm_b32 v36, v118, v114, s61\n\t"
    "v_mfma_f32_16x16x32_bf16 a[216:219], v[40:43], v[104:107], a[216:219]\n\t"
    "v_mfma_f32_16x16x32_bf16 a[212:215], v[40:43], v[100:103], a[212:215]\n\t"
    "v_perm_b32 v33, v126, v122, s60\n\t"
    "v_mfma_f32_16x16x32_bf16 a[244:247], v[44:47], v[100:103], a[244:247]\n\t"
    "buffer_load_dwordx4 v[232:235], v5, s[96:99], s62 offen offset:2048\n\t"
    "v_mfma_f32_16x16x32_bf16 a[240:243], v[44:47], v[96:99], a[240:243]\n\t"
    "v_perm_b32 v37, v126, v122, s61\n\t"
    "v_mfma_f32_16x16x32_bf16 a[208:211], v[40:43], v[96:99], a[208:211]\n\t"
    "v_mfma_f32_16x16x32_bf16 a[204:207], v[40:43], v[92:95], a[204:207]\n\t"
    "v_perm_b32 v34, v134, v130, s60\n\t"
    "v_mfma_f32_16x16x32_bf16 a[236:239], v[44:47], v[92:95], a[236:239]\n\t"
    "v_mfma_f32_16x16x32_bf16 a[232:235], v[44:47], v[88:91], a[232:235]\n\t"
    "v_perm_b32 v38, v134, v130, s61\n\t"
    "v_mfma_f32_16x16x32_bf16 a[200:203], v[40:43], v[88:91], a[200:203]\n\t"
    "buffer_load_dwordx4 v[236:239], v5, s[96:99], s62 offen offset:3072\n\t"
    "v_mfma_f32_16x16x32_bf16 a[196:199], v[40:43], v[84:87], a[196:199]\n\t"
    "v_perm_b32 v35, v142, v138, s60\n\t"
    "v_mfma_f32_16x16x32_bf16 a[228:231], v[44:47], v[84:87], a[228:231]\n\t"
    "v_mfma_f32_16x16x32_bf16 a[224:227], v[44:47], v[80:83], a[224:227]\n\t"
    "v_perm_b32 v39, v142, v138, s61\n\t"
    "v_mfma_f32_16x16x32_bf16 a[192:195], v[40:43], v[80:83], a[192:195]\n\t"
    "s_waitcnt lgkmcnt(0)\n\t"
    "v_mfma_f32_16x16x32_bf16 a[0:3], v[16:19], v[12:15], a[0:3]\n\t"
    "v_perm_b32 v40, v119, v115, s60\n\t"
    "v_mfma_f32_16x16x32_bf16 a[32:35], v[20:23], v[12:15], a[32:35]\n\t"
    "v_mfma_f32_16x16x32_bf16 a[36:39], v[20:23], v[80:83], a[36:39]\n\t"
    "v_perm_b32 v44, v119, v115, s61\n\t"
    "v_mfma_f32_16x16x32_bf16 a[4:7], v[16:19], v[80:83], a[4:7]\n\t"
    "ds_read_b128 v[108:111], v11 offset:52736\n\t"
    "v_mfma_f32_16x16x32_bf16 a[8:11], v[16:19], v[84:87], a[8:11]\n\t"
    "v_perm_b32 v41, v127, v123, s60\n\t"
    "v_mfma_f32_16x16x32_bf16 a[40:43], v[20:23], v[84:87], a[40:43]\n\t"
    "v_mfma_f32_16x16x32_bf16 a[44:47], v[20:23], v[88:91], a[44:47]\n\t"
    "v_perm_b32 v45, v127, v123, s61\n\t"
    "v_mfma_f32_16x16x32_bf16 a[12:15], v[16:19], v[88:91], a[12:15]\n\t"
    "v_mfma_f32_16x16x32_bf16 a[16:19], v[16:19], v[92:95], a[16:19]\n\t"
    "v_perm_b32 v42, v135, v131, s60\n\t"
    "v_mfma_f32_16x16x32_bf16 a[48:51], v[20:23], v[92:95], a[48:51]\n\t"
    "v_mfma_f32_16x16x32_bf16 a[52:55], v[20:23], v[96:99], a[52:55]\n\t"
    "v_perm_b32 v46, v135, v131, s61\n\t"
    "v_mfma_f32_16x16x32_bf16 a[20:23], v[16:19], v[96:99], a[20:23]\n\t"
    "v_mfma_f32_16x16x32_bf16 a[24:27], v[16:19], v[100:103], a[24:27]\n\t"
    "v_perm_b32 v43, v143, v139, s60\n\t"
    "v_mfma_f32_16x16x32_bf16 a[56:59], v[20:23], v[100:103], a[56:59]\n\t"
    "v_mfma_f32_16x16x32_bf16 a[60:63], v[20:23], v[104:107], a[60:63]\n\t"
    "v_perm_b32 v47, v143, v139, s61\n\t"
    "v_mfma_f32_16x16x32_bf16 a[28:31], v[16:19], v[104:107], a[28:31]\n\t"
    "s_waitcnt vmcnt(16)\n\t"
    "s_mov_b32 s62, 0\n\t"
    "v_mfma_f32_16x16x32_bf16 a[92:95], v[24:27], v[104:107], a[92:95]\n\t"
    "v_perm_b32 v16, v148, v144, s60\n\t"
    "v_mfma_f32_16x16x32_bf16 a[124:127], v[28:31], v[104:107], a[124:127]\n\t"
    "buffer_load_dwordx2 v[112:113], %[vwo2], s[88:91], s62 offen\n\t"
    "v_mfma_f32_16x16x32_bf16 a[120:123], v[28:31], v[100:103], a[120:123]\n\t"
    "v_perm_b32 v20, v148, v144, s61\n\t"
    "v_mfma_f32_16x16x32_bf16 a[88:91], v[24:27], v[100:103], a[88:91]\n\t"
    "v_mfma_f32_16x16x32_bf16 a[84:87], v[24:27], v[96:99], a[84:87]\n\t"
    "v_perm_b32 v17, v156, v152, s60\n\t"
    "buffer_load_dwordx2 v[114:115], %[vwo2], s[88:91], s62 offen offset:512\n\t"
    "v_mfma_f32_16x16x32_bf16 a[116:119], v[28:31], v[96:99], a[116:119]\n\t"
    "v_mfma_f32_16x16x32_bf16 a[112:115], v[28:31], v[92:95], a[112:115]\n\t"
    "v_perm_b32 v21, v156, v152, s61\n\t"
    "v_mfma_f32_16x16x32_bf16 a[80:83], v[24:27], v[92:95], a[80:83]\n\t"
    "buffer_load_dwordx2 v[116:117], %[vwo2], s[88:91], s62 offen offset:1024\n\t"
    "v_mfma_f32_16x16x32_bf16 a[76:79], v[24:27], v[88:91], a[76:79]\n\t"
    "v_perm_b32 v18, v164, v160, s60\n\t"
    "v_mfma_f32_16x16x32_bf16 a[108:111], v[28:31], v[88:91], a[108:111]\n\t"
    "v_mfma_f32_16x16x32_bf16 a[104:107], v[28:31], v[84:87], a[104:107]\n\t"
    "v_perm_b32 v22, v164, v160, s61\n\t"
    "buffer_load_dwordx2 v[118:119], %[vwo2], s[88:91], s62 offen offset:1536\n\t"
    "v_mfma_f32_16x16x32_bf16 a[72:75], v[24:27], v[84:87], a[72:75]\n\t"
    "v_mfma_f32_16x16x32_bf16 a[68:71], v[24:27], v[80:83], a[68:71]\n\t"
    "v_perm_b32 v19, v172, v168, s60\n\t"
    "v_mfma_f32_16x16x32_bf16 a[100:103], v[28:31], v[80:83], a[100:103]\n\t"
    "buffer_load_dwordx2 v[120:121], %[vwo2], s[88:91], s62 offen offset:2048\n\t"
    "v_mfma_f32_16x16x32_bf16 a[96:99], v[28:31], v[12:15], a[96:99]\n\t"
    "v_perm_b32 v23, v172, v168, s61\n\t"
    "v_mfma_f32_16x16x32_bf16 a[64:67], v[24:27], v[12:15], a[64:67]\n\t"
    "v_mfma_f32_16x16x32_bf16 a[128:131], v[32:35], v[12:15], a[128:131]\n\t"
    "v_perm_b32 v24, v149, v145, s60\n\t"
    "buffer_load_dwordx2 v[122:123], %[vwo2], s[88:91], s62 offen offset:2560\n\t"
    "v_mfma_f32_16x16x32_bf16 a[160:163], v[36:39], v[12:15], a[160:163]\n\t"
    "v_mfma_f32_16x16x32_bf16 a[164:167], v[36:39], v[80:83], a[164:167]\n\t"
    "v_perm_b32 v28, v149, v145, s61\n\t"
    "v_mfma_f32_16x16x32_bf16 a[132:135], v[32:35], v[80:83], a[132:135]\n\t"
    "buffer_load_dwordx2 v[124:125], %[vwo2], s[88:91], s62 offen offset:3072\n\t"
    "v_mfma_f32_16x16x32_bf16 a[136:139], v[32:35], v[84:87], a[136:139]\n\t"
    "v_perm_b32 v25, v157, v153, s60\n\t"
    "v_mfma_f32_16x16x32_bf16 a[168:171], v[36:39], v[84:87], a[168:171]\n\t"
    "v_mfma_f32_16x16x32_bf16 a[172:175], v[36:39], v[88:91], a[172:175]\n\t"
    "v_perm_b32 v29, v157, v153, s61\n\t"
    "buffer_load_dwordx2 v[126:127], %[vwo2], s[88:91], s62 offen offset:3584\n\t"
    "v_mfma_f32_16x16x32_bf16 a[140:143], v[32:35], v[88:91], a[140:143]\n\t"
    "s_mov_b32 s62, 16384\n\t"
    "v_mfma_f32_16x16x32_bf16 a[144:147], v[32:35], v[92:95], a[144:147]\n\t"
    "v_perm_b32 v26, v165, v161, s60\n\t"
    "v_mfma_f32_16x16x32_bf16 a[176:179], v[36:39], v[92:95], a[176:179]\n\t"
    "buffer_load_dwordx2 v[128:129], %[vwo2], s[88:91], s62 offen\n\t"
    "v_mfma_f32_16x16x32_bf16 a[180:183], v[36:39], v[96:99], a[180:183]\n\t"
    "v_perm_b32 v30, v165, v161, s61\n\t"
    "v_mfma_f32_16x16x32_bf16 a[148:151], v[32:35], v[96:99], a[148:151]\n\t"
    "v_mfma_f32_16x16x32_bf16 a[152:155], v[32:35], v[100:103], a[152:155]\n\t"
    "v_perm_b32 v27, v173, v169, s60\n\t"
    "buffer_load_dwordx2 v[130:131], %[vwo2], s[88:91], s62 offen offset:512\n\t"
    "v_mfma_f32_16x16x32_bf16 a[184:187], v[36:39], v[100:103], a[184:187]\n\t"
    "v_mfma_f32_16x16x32_bf16 a[188:191], v[36:39], v[104:107], a[188:191]\n\t"
    "v_perm_b32 v31, v173, v169, s61\n\t"
    "v_mfma_f32_16x16x32_bf16 a[156:159], v[32:35], v[104:107], a[156:159]\n\t"
    "buffer_load_dwordx2 v[132:133], %[vwo2], s[88:91], s62 offen offset:1024\n\t"
    "v_mfma_f32_16x16x32_bf16 a[220:223], v[40:43], v[104:107], a[220:223]\n\t"
    "v_perm_b32 v32, v150, v146, s60\n\t"
    "v_mfma_f32_16x16x32_bf16 a[252:255], v[44:47], v[104:107], a[252:255]\n\t"
    "v_mfma_f32_16x16x32_bf16 a[248:251], v[44:47], v[100:103], a[248:251]\n\t"
    "v_perm_b32 v36, v150, v146, s61\n\t"
    "buffer_load_dwordx2 v[134:135], %[vwo2], s[88:91], s62 offen offset:1536\n\t"
    "v_mfma_f32_16x16x32_bf16 a[216:219], v[40:43], v[100:103], a[216:219]\n\t"
    "v_mfma_f32_16x16x32_bf16 a[212:215], v[40:43], v[96:99], a[212:215]\n\t"
    "v_perm_b32 v33, v158, v154, s60\n\t"
    "v_mfma_f32_16x16x32_bf16 a[244:247], v[44:47], v[96:99], a[244:247]\n\t"
    "buffer_load_dwordx2 v[136:137], %[vwo2], s[88:91], s62 offen offset:2048\n\t"
    "v_mfma_f32_16x16x32_bf16 a[240:243], v[44:47], v[92:95], a[240:243]\n\t"
    "v_perm_b32 v37, v158, v154, s61\n\t"
    "v_mfma_f32_16x16x32_bf16 a[208:211], v[40:43], v[92:95], a[208:211]\n\t"
    "v_mfma_f32_16x16x32_bf16 a[204:207], v[40:43], v[88:91], a[204:207]\n\t"
    "v_perm_b32 v34, v166, v162, s60\n\t"
    "buffer_load_dwordx2 v[138:139], %[vwo2], s[88:91], s62 offen offset:2560\n\t"
    "v_mfma_f32_16x16x32_bf16 a[236:239], v[44:47], v[88:91], a[236:239]\n\t"
    "v_mfma_f32_16x16x32_bf16 a[232:235], v[44:47], v[84:87], a[232:235]\n\t"
    "v_perm_b32 v38, v166, v162, s61\n\t"
    "v_mfma_f32_16x16x32_bf16 a[200:203], v[40:43], v[84:87], a[200:203]\n\t"
    "buffer_load_dwordx2 v[140:141], %[vwo2], s[88:91], s62 offen offset:3072\n\t"
    "v_mfma_f32_16x16x32_bf16 a[196:199], v[40:43], v[80:83], a[196:199]\n\t"
    "v_perm_b32 v35, v174, v170, s60\n\t"
    "v_mfma_f32_16x16x32_bf16 a[228:231], v[44:47], v[80:83], a[228:231]\n\t"
    "v_mfma_f32_16x16x32_bf16 a[224:227], v[44:47], v[12:15], a[224:227]\n\t"
    "v_perm_b32 v39, v174, v170, s61\n\t"
    "buffer_load_dwordx2 v[142:143], %[vwo2], s[88:91], s62 offen offset:3584\n\t"
    "v_mfma_f32_16x16x32_bf16 a[192:195], v[40:43], v[12:15], a[192:195]\n\t"
    "s_waitcnt lgkmcnt(0)\n\t"
    "v_mfma_f32_16x16x32_bf16 a[0:3], v[16:19], v[108:111], a[0:3]\n\t"
    "v_perm_b32 v40, v151, v147, s60\n\t"
    "v_mfma_f32_16x16x32_bf16 a[32:35], v[20:23], v[108:111], a[32:35]\n\t"
    "v_mfma_f32_16x16x32_bf16 a[36:39], v[20:23], v[12:15], a[36:39]\n\t"
    "v_perm_b32 v44, v151, v147, s61\n\t"
    "v_mfma_f32_16x16x32_bf16 a[4:7], v[16:19], v[12:15], a[4:7]\n\t"
    "ds_read_b128 v[104:107], v11 offset:52480\n\t"
    "v_mfma_f32_16x16x32_bf16 a[8:11], v[16:19], v[80:83], a[8:11]\n\t"
    "v_perm_b32 v41, v159, v155, s60\n\t"
    "v_mfma_f32_16x16x32_bf16 a[40:43], v[20:23], v[80:83], a[40:43]\n\t"
    "v_mfma_f32_16x16x32_bf16 a[44:47], v[20:23], v[84:87], a[44:47]\n\t"
    "v_perm_b32 v45, v159, v155, s61\n\t"
    "v_mfma_f32_16x16x32_bf16 a[12:15], v[16:19], v[84:87], a[12:15]\n\t"
    "v_mfma_f32_16x16x32_bf16 a[16:19], v[16:19], v[88:91], a[16:19]\n\t"
    "v_perm_b32 v42, v167, v163, s60\n\t"
    "v_mfma_f32_16x16x32_bf16 a[48:51], v[20:23], v[88:91], a[48:51]\n\t"
    "v_mfma_f32_16x16x32_bf16 a[52:55], v[20:23], v[92:95], a[52:55]\n\t"
    "v_perm_b32 v46, v167, v163, s61\n\t"
    "v_mfma_f32_16x16x32_bf16 a[20:23], v[16:19], v[92:95], a[20:23]\n\t"
    "v_mfma_f32_16x16x32_bf16 a[24:27], v[16:19], v[96:99], a[24:27]\n\t"
    "v_perm_b32 v43, v175, v171, s60\n\t"
    "v_mfma_f32_16x16x32_bf16 a[56:59], v[20:23], v[96:99], a[56:59]\n\t"
    "v_mfma_f32_16x16x32_bf16 a[60:63], v[20:23], v[100:103], a[60:63]\n\t"
    "v_perm_b32 v47, v175, v171, s61\n\t"
    "v_mfma_f32_16x16x32_bf16 a[28:31], v[16:19], v[100:103], a[28:31]\n\t"
    "s_waitcnt vmcnt(24)\n\t"
    "s_mov_b32 s62, 32768\n\t"
    "v_mfma_f32_16x16x32_bf16 a[92:95], v[24:27], v[100:103], a[92:95]\n\t"
    "v_perm_b32 v16, v180, v176, s60\n\t"
    "v_mfma_f32_16x16x32_bf16 a[124:127], v[28:31], v[100:103], a[124:127]\n\t"
    "buffer_load_dwordx2 v[144:145], %[vwo2], s[88:91], s62 offen\n\t"
    "v_mfma_f32_16x16x32_bf16 a[120:123], v[28:31], v[96:99], a[120:123]\n\t"
    "v_perm_b32 v20, v180, v176, s61\n\t"
    "v_mfma_f32_16x16x32_bf16 a[88:91], v[24:27], v[96:99], a[88:91]\n\t"
    "v_mfma_f32_16x16x32_bf16 a[84:87], v[24:27], v[92:95], a[84:87]\n\t"
    "v_perm_b32 v17, v188, v184, s60\n\t"
    "buffer_load_dwordx2 v[146:147], %[vwo2], s[88:91], s62 offen offset:512\n\t"
    "v_mfma_f32_16x16x32_bf16 a[116:119], v[28:31], v[92:95], a[116:119]\n\t"
    "v_mfma_f32_16x16x32_bf16 a[112:115], v[28:31], v[88:91], a[112:115]\n\t"
    "v_perm_b32 v21, v188, v184, s61\n\t"
    "v_mfma_f32_16x16x32_bf16 a[80:83], v[24:27], v[88:91], a[80:83]\n\t"
    "buffer_load_dwordx2 v[148:149], %[vwo2], s[88:91], s62 offen offset:1024\n\t"
    "v_mfma_f32_16x16x32_bf16 a[76:79], v[24:27], v[84:87], a[76:79]\n\t"
    "v_perm_b32 v18, v196, v192, s60\n\t"
    "v_mfma_f32_16x16x32_bf16 a[108:111], v[28:31], v[84:87], a[108:111]\n\t"
    "v_mfma_f32_16x16x32_bf16 a[104:107], v[28:31], v[80:83], a[104:107]\n\t"
    "v_perm_b32 v22, v196, v192, s61\n\t"
    "buffer_load_dwordx2 v[150:151], %[vwo2], s[88:91], s62 offen offset:1536\n\t"
    "v_mfma_f32_16x16x32_bf16 a[72:75], v[24:27], v[80:83], a[72:75]\n\t"
    "v_mfma_f32_16x16x32_bf16 a[68:71], v[24:27], v[12:15], a[68:71]\n\t"
    "v_perm_b32 v19, v204, v200, s60\n\t"
    "v_mfma_f32_16x16x32_bf16 a[100:103], v[28:31], v[12:15], a[100:103]\n\t"
    "buffer_load_dwordx2 v[152:153], %[vwo2], s[88:91], s62 offen offset:2048\n\t"
    "v_mfma_f32_16x16x32_bf16 a[96:99], v[28:31], v[108:111], a[96:99]\n\t"
    "v_perm_b32 v23, v204, v200, s61\n\t"
    "v_mfma_f32_16x16x32_bf16 a[64:67], v[24:27], v[108:111], a[64:67]\n\t"
    "v_mfma_f32_16x16x32_bf16 a[128:131], v[32:35], v[108:111], a[128:131]\n\t"
    "v_perm_b32 v24, v181, v177, s60\n\t"
    "buffer_load_dwordx2 v[154:155], %[vwo2], s[88:91], s62 offen offset:2560\n\t"
    "v_mfma_f32_16x16x32_bf16 a[160:163], v[36:39], v[108:111], a[160:163]\n\t"
    "v_mfma_f32_16x16x32_bf16 a[164:167], v[36:39], v[12:15], a[164:167]\n\t"
    "v_perm_b32 v28, v181, v177, s61\n\t"
    "v_mfma_f32_16x16x32_bf16 a[132:135], v[32:35], v[12:15], a[132:135]\n\t"
    "buffer_load_dwordx2 v[156:157], %[vwo2], s[88:91], s62 offen offset:3072\n\t"
    "v_mfma_f32_16x16x32_bf16 a[136:139], v[32:35], v[80:83], a[136:139]\n\t"
    "v_perm_b32 v25, v189, v185, s60\n\t"
    "v_mfma_f32_16x16x32_bf16 a[168:171], v[36:39], v[80:83], a[168:171]\n\t"
    "v_mfma_f32_16x16x32_bf16 a[172:175], v[36:39], v[84:87], a[172:175]\n\t"
    "v_perm_b32 v29, v189, v185, s61\n\t"
    "buffer_load_dwordx2 v[158:159], %[vwo2], s[88:91], s62 offen offset:3584\n\t"
    "v_mfma_f32_16x16x32_bf16 a[140:143], v[32:35], v[84:87], a[140:143]\n\t"
    "s_mov_b32 s62, 49152\n\t"
    "v_mfma_f32_16x16x32_bf16 a[144:147], v[32:35], v[88:91], a[144:147]\n\t"
    "v_perm_b32 v26, v197, v193, s60\n\t"
    "v_mfma_f32_16x16x32_bf16 a[176:179], v[36:39], v[88:91], a[176:179]\n\t"
    "buffer_load_dwordx2 v[160:161], %[vwo2], s[88:91], s62 offen\n\t"
    "v_mfma_f32_16x16x32_bf16 a[180:183], v[36:39], v[92:95], a[180:183]\n\t"
    "v_perm_b32 v30, v197, v193, s61\n\t"
    "v_mfma_f32_16x16x32_bf16 a[148:151], v[32:35], v[92:95], a[148:151]\n\t"
    "v_mfma_f32_16x16x32_bf16 a[152:155], v[32:35], v[96:99], a[152:155]\n\t"
    "v_perm_b32 v27, v205, v201, s60\n\t"
    "buffer_load_dwordx2 v[162:163], %[vwo2], s[88:91], s62 offen offset:512\n\t"
    "v_mfma_f32_16x16x32_bf16 a[184:187], v[36:39], v[96:99], a[184:187]\n\t"
    "v_mfma_f32_16x16x32_bf16 a[188:191], v[36:39], v[100:103], a[188:191]\n\t"
    "v_perm_b32 v31, v205, v201, s61\n\t"
    "v_mfma_f32_16x16x32_bf16 a[156:159], v[32:35], v[100:103], a[156:159]\n\t"
    "buffer_load_dwordx2 v[164:165], %[vwo2], s[88:91], s62 offen offset:1024\n\t"
    "v_mfma_f32_16x16x32_bf16 a[220:223], v[40:43], v[100:103], a[220:223]\n\t"
    "v_perm_b32 v32, v182, v178, s60\n\t"
    "v_mfma_f32_16x16x32_bf16 a[252:255], v[44:47], v[100:103], a[252:255]\n\t"
    "v_mfma_f32_16x16x32_bf16 a[248:251], v[44:47], v[96:99], a[248:251]\n\t"
    "v_perm_b32 v36, v182, v178, s61\n\t"
    "buffer_load_dwordx2 v[166:167], %[vwo2], s[88:91], s62 offen offset:1536\n\t"
    "v_mfma_f32_16x16x32_bf16 a[216:219], v[40:43], v[96:99], a[216:219]\n\t"
    "v_mfma_f32_16x16x32_bf16 a[212:215], v[40:43], v[92:95], a[212:215]\n\t"
    "v_perm_b32 v33, v190, v186, s60\n\t"
    "v_mfma_f32_16x16x32_bf16 a[244:247], v[44:47], v[92:95], a[244:247]\n\t"
    "buffer_load_dwordx2 v[168:169], %[vwo2], s[88:91], s62 offen offset:2048\n\t"
    "v_mfma_f32_16x16x32_bf16 a[240:243], v[44:47], v[88:91], a[240:243]\n\t"
    "v_perm_b32 v37, v190, v186, s61\n\t"
    "v_mfma_f32_16x16x32_bf16 a[208:211], v[40:43], v[88:91], a[208:211]\n\t"
    "v_mfma_f32_16x16x32_bf16 a[204:207], v[40:43], v[84:87], a[204:207]\n\t"
    "v_perm_b32 v34, v198, v194, s60\n\t"
    "buffer_load_dwordx2 v[170:171], %[vwo2], s[88:91], s62 offen offset:2560\n\t"
    "v_mfma_f32_16x16x32_bf16 a[236:239], v[44:47], v[84:87], a[236:239]\n\t"
    "v_mfma_f32_16x16x32_bf16 a[232:235], v[44:47], v[80:83], a[232:235]\n\t"
    "v_perm_b32 v38, v198, v194, s61\n\t"
    "v_mfma_f32_16x16x32_bf16 a[200:203], v[40:43], v[80:83], a[200:203]\n\t"
    "buffer_load_dwordx2 v[172:173], %[vwo2], s[88:91], s62 offen offset:3072\n\t"
    "v_mfma_f32_16x16x32_bf16 a[196:199], v[40:43], v[12:15], a[196:199]\n\t"
    "v_perm_b32 v35, v206, v202, s60\n\t"
    "v_mfma_f32_16x16x32_bf16 a[228:231], v[44:47], v[12:15], a[228:231]\n\t"
    "v_mfma_f32_16x16x32_bf16 a[224:227], v[44:47], v[108:111], a[224:227]\n\t"
    "v_perm_b32 v39, v206, v202, s61\n\t"
    "buffer_load_dwordx2 v[174:175], %[vwo2], s[88:91], s62 offen offset:3584\n\t"
    "v_mfma_f32_16x16x32_bf16 a[192:195], v[40:43], v[108:111], a[192:195]\n\t"
    "s_waitcnt lgkmcnt(0)\n\t"
    "v_mfma_f32_16x16x32_bf16 a[0:3], v[16:19], v[104:107], a[0:3]\n\t"
    "v_perm_b32 v40, v183, v179, s60\n\t"
    "s_waitcnt vmcnt(24)\n\t"
    "v_mfma_f32_16x16x32_bf16 a[32:35], v[20:23], v[104:107], a[32:35]\n\t"
    "v_perm_b32 v48, v114, v112, s60\n\t"
    "v_mfma_f32_16x16x32_bf16 a[36:39], v[20:23], v[108:111], a[36:39]\n\t"
    "v_perm_b32 v44, v183, v179, s61\n\t"
    "v_mfma_f32_16x16x32_bf16 a[4:7], v[16:19], v[108:111], a[4:7]\n\t"
    "ds_read_b128 v[100:103], v11 offset:52224\n\t"
    "v_mfma_f32_16x16x32_bf16 a[8:11], v[16:19], v[12:15], a[8:11]\n\t"
    "v_perm_b32 v41, v191, v187, s60\n\t"
    "v_mfma_f32_16x16x32_bf16 a[40:43], v[20:23], v[12:15], a[40:43]\n\t"
    "v_perm_b32 v52, v114, v112, s61\n\t"
    "v_mfma_f32_16x16x32_bf16 a[44:47], v[20:23], v[80:83], a[44:47]\n\t"
    "v_perm_b32 v45, v191, v187, s61\n\t"
    "v_mfma_f32_16x16x32_bf16 a[12:15], v[16:19], v[80:83], a[12:15]\n\t"
    "v_mfma_f32_16x16x32_bf16 a[16:19], v[16:19], v[84:87], a[16:19]\n\t"
    "v_perm_b32 v42, v199, v195, s60\n\t"
    "v_mfma_f32_16x16x32_bf16 a[48:51], v[20:23], v[84:87], a[48:51]\n\t"
    "v_perm_b32 v56, v115, v113, s60\n\t"
    "v_mfma_f32_16x16x32_bf16 a[52:55], v[20:23], v[88:91], a[52:55]\n\t"
    "v_perm_b32 v46, v199, v195, s61\n\t"
    "v_mfma_f32_16x16x32_bf16 a[20:23], v[16:19], v[88:91], a[20:23]\n\t"
    "v_mfma_f32_16x16x32_bf16 a[24:27], v[16:19], v[92:95], a[24:27]\n\t"
    "v_perm_b32 v43, v207, v203, s60\n\t"
    "v_mfma_f32_16x16x32_bf16 a[56:59], v[20:23], v[92:95], a[56:59]\n\t"
    "v_perm_b32 v60, v115, v113, s61\n\t"
    "v_mfma_f32_16x16x32_bf16 a[60:63], v[20:23], v[96:99], a[60:63]\n\t"
    "v_perm_b32 v47, v207, v203, s61\n\t"
    "v_mfma_f32_16x16x32_bf16 a[28:31], v[16:19], v[96:99], a[28:31]\n\t"
    "s_waitcnt vmcnt(32)\n\t"
    "s_mov_b32 s62, 65536\n\t"
    "v_mfma_f32_16x16x32_bf16 a[92:95], v[24:27], v[96:99], a[92:95]\n\t"
    "v_perm_b32 v16, v212, v208, s60\n\t"
    "v_mfma_f32_16x16x32_bf16 a[124:127], v[28:31], v[96:99], a[124:127]\n\t"
    "v_perm_b32 v49, v118, v116, s60\n\t"
    "buffer_load_dwordx2 v[176:177], %[vwo2], s[88:91], s62 offen\n\t"
    "v_mfma_f32_16x16x32_bf16 a[120:123], v[28:31], v[92:95], a[120:123]\n\t"
    "v_perm_b32 v20, v212, v208, s61\n\t"
    "v_mfma_f32_16x16x32_bf16 a[88:91], v[24:27], v[92:95], a[88:91]\n\t"
    "v_mfma_f32_16x16x32_bf16 a[84:87], v[24:27], v[88:91], a[84:87]\n\t"
    "v_perm_b32 v17, v220, v216, s60\n\t"
    "buffer_load_dwordx2 v[178:179], %[vwo2], s[88:91], s62 offen offset:512\n\t"
    "v_mfma_f32_16x16x32_bf16 a[116:119], v[28:31], v[88:91], a[116:119]\n\t"
    "v_perm_b32 v53, v118, v116, s61\n\t"
    "v_mfma_f32_16x16x32_bf16 a[112:115], v[28:31], v[84:87], a[112:115]\n\t"
    "v_perm_b32 v21, v220, v216, s61\n\t"
    "v_mfma_f32_16x16x32_bf16 a[80:83], v[24:27], v[84:87], a[80:83]\n\t"
    "buffer_load_dwordx2 v[180:181], %[vwo2], s[88:91], s62 offen offset:1024\n\t"
    "v_mfma_f32_16x16x32_bf16 a[76:79], v[24:27], v[80:83], a[76:79]\n\t"
    "v_perm_b32 v18, v228, v224, s60\n\t"
    "v_mfma_f32_16x16x32_bf16 a[108:111], v[28:31], v[80:83], a[108:111]\n\t"
    "v_perm_b32 v57, v119, v117, s60\n\t"
    "v_mfma_f32_16x16x32_bf16 a[104:107], v[28:31], v[12:15], a[104:107]\n\t"
    "v_perm_b32 v22, v228, v224, s61\n\t"
    "buffer_load_dwordx2 v[182:183], %[vwo2], s[88:91], s62 offen offset:1536\n\t"
    "v_mfma_f32_16x16x32_bf16 a[72:75], v[24:27], v[12:15], a[72:75]\n\t"
    "v_mfma_f32_16x16x32_bf16 a[68:71], v[24:27], v[108:111], a[68:71]\n\t"
    "v_perm_b32 v19, v236, v232, s60\n\t"
    "v_mfma_f32_16x16x32_bf16 a[100:103], v[28:31], v[108:111], a[100:103]\n\t"
    "v_perm_b32 v61, v119, v117, s61\n\t"
    "buffer_load_dwordx2 v[184:185], %[vwo2], s[88:91], s62 offen offset:2048\n\t"
    "v_mfma_f32_16x16x32_bf16 a[96:99], v[28:31], v[104:107], a[96:99]\n\t"
    "v_perm_b32 v23, v236, v232, s61\n\t"
    "v_mfma_f32_16x16x32_bf16 a[64:67], v[24:27], v[104:107], a[64:67]\n\t"
    "v_mfma_f32_16x16x32_bf16 a[128:131], v[32:35], v[104:107], a[128:131]\n\t"
    "v_perm_b32 v24, v213, v209, s60\n\t"
    "buffer_load_dwordx2 v[186:187], %[vwo2], s[88:91], s62 offen offset:2560\n\t"
    "v_mfma_f32_16x16x32_bf16 a[160:163], v[36:39], v[104:107], a[160:163]\n\t"
    "v_perm_b32 v50, v122, v120, s60\n\t"
    "v_mfma_f32_16x16x32_bf16 a[164:167], v[36:39], v[108:111], a[164:167]\n\t"
    "v_perm_b32 v28, v213, v209, s61\n\t"
    "v_mfma_f32_16x16x32_bf16 a[132:135], v[32:35], v[108:111], a[132:135]\n\t"
    "buffer_load_dwordx2 v[188:189], %[vwo2], s[88:91], s62 offen offset:3072\n\t"
    "v_mfma_f32_16x16x32_bf16 a[136:139], v[32:35], v[12:15], a[136:139]\n\t"
    "v_perm_b32 v25, v221, v217, s60\n\t"
    "v_mfma_f32_16x16x32_bf16 a[168:171], v[36:39], v[12:15], a[168:171]\n\t"
    "v_perm_b32 v54, v122, v120, s61\n\t"
    "v_mfma_f32_16x16x32_bf16 a[172:175], v[36:39], v[80:83], a[172:175]\n\t"
    "v_perm_b32 v29, v221, v217, s61\n\t"
    "buffer_load_dwordx2 v[190:191], %[vwo2], s[88:91], s62 offen offset:3584\n\t"
    "v_mfma_f32_16x16x32_bf16 a[140:143], v[32:35], v[80:83], a[140:143]\n\t"
    "s_mov_b32 s62, 81920\n\t"
    "v_mfma_f32_16x16x32_bf16 a[144:147], v[32:35], v[84:87], a[144:147]\n\t"
    "v_perm_b32 v26, v229, v225, s60\n\t"
    "v_mfma_f32_16x16x32_bf16 a[176:179], v[36:39], v[84:87], a[176:179]\n\t"
    "v_perm_b32 v58, v123, v121, s60\n\t"
    "buffer_load_dwordx2 v[192:193], %[vwo2], s[88:91], s62 offen\n\t"
    "v_mfma_f32_16x16x32_bf16 a[180:183], v[36:39], v[88:91], a[180:183]\n\t"
    "v_perm_b32 v30, v229, v225, s61\n\t"
    "v_mfma_f32_16x16x32_bf16 a[148:151], v[32:35], v[88:91], a[148:151]\n\t"
    "v_mfma_f32_16x16x32_bf16 a[152:155], v[32:35], v[92:95], a[152:155]\n\t"
    "v_perm_b32 v27, v237, v233, s60\n\t"
    "buffer_load_dwordx2 v[194:195], %[vwo2], s[88:91], s62 offen offset:512\n\t"
    "v_mfma_f32_16x16x32_bf16 a[184:187], v[36:39], v[92:95], a[184:187]\n\t"
    "v_perm_b32 v62, v123, v121, s61\n\t"
    "v_mfma_f32_16x16x32_bf16 a[188:191], v[36:39], v[96:99], a[188:191]\n\t"
    "v_perm_b32 v31, v237, v233, s61\n\t"
    "v_mfma_f32_16x16x32_bf16 a[156:159], v[32:35], v[96:99], a[156:159]\n\t"
    "buffer_load_dwordx2 v[196:197], %[vwo2], s[88:91], s62 offen offset:1024\n\t"
    "v_mfma_f32_16x16x32_bf16 a[220:223], v[40:43], v[96:99], a[220:223]\n\t"
    "v_perm_b32 v32, v214, v210, s60\n\t"
    "v_mfma_f32_16x16x32_bf16 a[252:255], v[44:47], v[96:99], a[252:255]\n\t"
    "v_perm_b32 v51, v126, v124, s60\n\t"
    "v_mfma_f32_16x16x32_bf16 a[248:251], v[44:47], v[92:95], a[248:251]\n\t"
    "v_perm_b32 v36, v214, v210, s61\n\t"
    "buffer_load_dwordx2 v[198:199], %[vwo2], s[88:91], s62 offen offset:1536\n\t"
    "v_mfma_f32_16x16x32_bf16 a[216:219], v[40:43], v[92:95], a[216:219]\n\t"
    "v_mfma_f32_16x16x32_bf16 a[212:215], v[40:43], v[88:91], a[212:215]\n\t"
    "v_perm_b32 v33, v222, v218, s60\n\t"
    "v_mfma_f32_16x16x32_bf16 a[244:247], v[44:47], v[88:91], a[244:247]\n\t"
    "v_perm_b32 v55, v126, v124, s61\n\t"
    "buffer_load_dwordx2 v[200:201], %[vwo2], s[88:91], s62 offen offset:2048\n\t"
    "v_mfma_f32_16x16x32_bf16 a[240:243], v[44:47], v[84:87], a[240:243]\n\t"
    "v_perm_b32 v37, v222, v218, s61\n\t"
    "v_mfma_f32_16x16x32_bf16 a[208:211], v[40:43], v[84:87], a[208:211]\n\t"
    "v_mfma_f32_16x16x32_bf16 a[204:207], v[40:43], v[80:83], a[204:207]\n\t"
    "v_perm_b32 v34, v230, v226, s60\n\t"
    "buffer_load_dwordx2 v[202:203], %[vwo2], s[88:91], s62 offen offset:2560\n\t"
    "v_mfma_f32_16x16x32_bf16 a[236:239], v[44:47], v[80:83], a[236:239]\n\t"
    "v_perm_b32 v59, v127, v125, s60\n\t"
    "v_mfma_f32_16x16x32_bf16 a[232:235], v[44:47], v[12:15], a[232:235]\n\t"
    "v_perm_b32 v38, v230, v226, s61\n\t"
    "v_mfma_f32_16x16x32_bf16 a[200:203], v[40:43], v[12:15], a[200:203]\n\t"
    "buffer_load_dwordx2 v[204:205], %[vwo2], s[88:91], s62 offen offset:3072\n\t"
    "v_mfma_f32_16x16x32_bf16 a[196:199], v[40:43], v[108:111], a[196:199]\n\t"
    "v_perm_b32 v35, v238, v234, s60\n\t"
    "v_mfma_f32_16x16x32_bf16 a[228:231], v[44:47], v[108:111], a[228:231]\n\t"
    "v_perm_b32 v63, v127, v125, s61\n\t"
    "v_mfma_f32_16x16x32_bf16 a[224:227], v[44:47], v[104:107], a[224:227]\n\t"
    "v_perm_b32 v39, v238, v234, s61\n\t"
    "buffer_load_dwordx2 v[206:207], %[vwo2], s[88:91], s62 offen offset:3584\n\t"
    "v_mfma_f32_16x16x32_bf16 a[192:195], v[40:43], v[104:107], a[192:195]\n\t"
    "s_waitcnt lgkmcnt(0)\n\t"
    "v_mfma_f32_16x16x32_bf16 a[0:3], v[16:19], v[100:103], a[0:3]\n\t"
    "v_perm_b32 v40, v215, v211, s60\n\t"
    "s_waitcnt vmcnt(32)\n\t"
    "v_mfma_f32_16x16x32_bf16 a[32:35], v[20:23], v[100:103], a[32:35]\n\t"
    "v_perm_b32 v64, v130, v128, s60\n\t"
    "v_mfma_f32_16x16x32_bf16 a[36:39], v[20:23], v[104:107], a[36:39]\n\t"
    "v_perm_b32 v44, v215, v211, s61\n\t"
    "v_mfma_f32_16x16x32_bf16 a[4:7], v[16:19], v[104:107], a[4:7]\n\t"
    "v_perm_b32 v68, v130, v128, s61\n\t"
    "v_mfma_f32_16x16x32_bf16 a[8:11], v[16:19], v[108:111], a[8:11]\n\t"
    "v_perm_b32 v41, v223, v219, s60\n\t"
    "v_mfma_f32_16x16x32_bf16 a[40:43], v[20:23], v[108:111], a[40:43]\n\t"
    "v_perm_b32 v72, v131, v129, s60\n\t"
    "v_mfma_f32_16x16x32_bf16 a[44:47], v[20:23], v[12:15], a[44:47]\n\t"
    "v_perm_b32 v45, v223, v219, s61\n\t"
    "v_mfma_f32_16x16x32_bf16 a[12:15], v[16:19], v[12:15], a[12:15]\n\t"
    "v_perm_b32 v76, v131, v129, s61\n\t"
    "v_mfma_f32_16x16x32_bf16 a[16:19], v[16:19], v[80:83], a[16:19]\n\t"
    "v_perm_b32 v42, v231, v227, s60\n\t"
    "v_mfma_f32_16x16x32_bf16 a[48:51], v[20:23], v[80:83], a[48:51]\n\t"
    "v_perm_b32 v65, v134, v132, s60\n\t"
    "v_mfma_f32_16x16x32_bf16 a[52:55], v[20:23], v[84:87], a[52:55]\n\t"
    "v_perm_b32 v46, v231, v227, s61\n\t"
    "v_mfma_f32_16x16x32_bf16 a[20:23], v[16:19], v[84:87], a[20:23]\n\t"
    "v_perm_b32 v69, v134, v132, s61\n\t"
    "v_mfma_f32_16x16x32_bf16 a[24:27], v[16:19], v[88:91], a[24:27]\n\t"
    "v_perm_b32 v43, v239, v235, s60\n\t"
    "v_mfma_f32_16x16x32_bf16 a[56:59], v[20:23], v[88:91], a[56:59]\n\t"
    "v_perm_b32 v73, v135, v133, s60\n\t"
    "v_mfma_f32_16x16x32_bf16 a[60:63], v[20:23], v[92:95], a[60:63]\n\t"
    "v_perm_b32 v47, v239, v235, s61\n\t"
    "v_mfma_f32_16x16x32_bf16 a[28:31], v[16:19], v[92:95], a[28:31]\n\t"
    "s_waitcnt vmcnt(16)\n\t"
    "v_perm_b32 v77, v135, v133, s61\n\t"
    "s_mov_b32 s62, 98304\n\t"
    "v_mfma_f32_16x16x32_bf16 a[92:95], v[24:27], v[92:95], a[92:95]\n\t"
    "v_perm_b32 v66, v138, v136, s60\n\t"
    "v_mfma_f32_16x16x32_bf16 a[124:127], v[28:31], v[92:95], a[124:127]\n\t"
    "v_perm_b32 v70, v138, v136, s61\n\t"
    "buffer_load_dwordx2 v[208:209], %[vwo2], s[88:91], s62 offen\n\t"
    "v_mfma_f32_16x16x32_bf16 a[120:123], v[28:31], v[88:91], a[120:123]\n\t"
    "v_perm_b32 v74, v139, v137, s60\n\t"
    "v_mfma_f32_16x16x32_bf16 a[88:91], v[24:27], v[88:91], a[88:91]\n\t"
    "v_perm_b32 v78, v139, v137, s61\n\t"
    "v_mfma_f32_16x16x32_bf16 a[84:87], v[24:27], v[84:87], a[84:87]\n\t"
    "v_perm_b32 v67, v142, v140, s60\n\t"
    "buffer_load_dwordx2 v[210:211], %[vwo2], s[88:91], s62 offen offset:512\n\t"
    "v_mfma_f32_16x16x32_bf16 a[116:119], v[28:31], v[84:87], a[116:119]\n\t"
    "v_perm_b32 v71, v142, v140, s61\n\t"
    "v_mfma_f32_16x16x32_bf16 a[112:115], v[28:31], v[80:83], a[112:115]\n\t"
    "v_perm_b32 v75, v143, v141, s60\n\t"
    "v_mfma_f32_16x16x32_bf16 a[80:83], v[24:27], v[80:83], a[80:83]\n\t"
    "v_perm_b32 v79, v143, v141, s61\n\t"
    "buffer_load_dwordx2 v[212:213], %[vwo2], s[88:91], s62 offen offset:1024\n\t"
    "v_mfma_f32_16x16x32_bf16 a[76:79], v[24:27], v[12:15], a[76:79]\n\t"
    "v_perm_b32 v112, v146, v144, s60\n\t"
    "v_mfma_f32_16x16x32_bf16 a[108:111], v[28:31], v[12:15], a[108:111]\n\t"
    "v_perm_b32 v116, v146, v144, s61\n\t"
    "v_mfma_f32_16x16x32_bf16 a[104:107], v[28:31], v[108:111], a[104:107]\n\t"
    "v_perm_b32 v120, v147, v145, s60\n\t"
    "buffer_load_dwordx2 v[214:215], %[vwo2], s[88:91], s62 offen offset:1536\n\t"
    "v_mfma_f32_16x16x32_bf16 a[72:75], v[24:27], v[108:111], a[72:75]\n\t"
    "v_perm_b32 v124, v147, v145, s61\n\t"
    "v_mfma_f32_16x16x32_bf16 a[68:71], v[24:27], v[104:107], a[68:71]\n\t"
    "v_perm_b32 v113, v150, v148, s60\n\t"
    "v_mfma_f32_16x16x32_bf16 a[100:103], v[28:31], v[104:107], a[100:103]\n\t"
    "v_perm_b32 v117, v150, v148, s61\n\t"
    "buffer_load_dwordx2 v[216:217], %[vwo2], s[88:91], s62 offen offset:2048\n\t"
    "v_mfma_f32_16x16x32_bf16 a[96:99], v[28:31], v[100:103], a[96:99]\n\t"
    "v_perm_b32 v121, v151, v149, s60\n\t"
    "v_mfma_f32_16x16x32_bf16 a[64:67], v[24:27], v[100:103], a[64:67]\n\t"
    "v_perm_b32 v125, v151, v149, s61\n\t"
    "v_mfma_f32_16x16x32_bf16 a[128:131], v[32:35], v[100:103], a[128:131]\n\t"
    "v_perm_b32 v114, v154, v152, s60\n\t"
    "buffer_load_dwordx2 v[218:219], %[vwo2], s[88:91], s62 offen offset:2560\n\t"
    "v_mfma_f32_16x16x32_bf16 a[160:163], v[36:39], v[100:103], a[160:163]\n\t"
    "v_perm_b32 v118, v154, v152, s61\n\t"
    "v_mfma_f32_16x16x32_bf16 a[164:167], v[36:39], v[104:107], a[164:167]\n\t"
    "v_perm_b32 v122, v155, v153, s60\n\t"
    "v_mfma_f32_16x16x32_bf16 a[132:135], v[32:35], v[104:107], a[132:135]\n\t"
    "v_perm_b32 v126, v155, v153, s61\n\t"
    "buffer_load_dwordx2 v[220:221], %[vwo2], s[88:91], s62 offen offset:3072\n\t"
    "v_mfma_f32_16x16x32_bf16 a[136:139], v[32:35], v[108:111], a[136:139]\n\t"
    "v_perm_b32 v115, v158, v156, s60\n\t"
    "v_mfma_f32_16x16x32_bf16 a[168:171], v[36:39], v[108:111], a[168:171]\n\t"
    "v_perm_b32 v119, v158, v156, s61\n\t"
    "v_mfma_f32_16x16x32_bf16 a[172:175], v[36:39], v[12:15], a[172:175]\n\t"
    "v_perm_b32 v123, v159, v157, s60\n\t"
    "buffer_load_dwordx2 v[222:223], %[vwo2], s[88:91], s62 offen offset:3584\n\t"
    "v_mfma_f32_16x16x32_bf16 a[140:143], v[32:35], v[12:15], a[140:143]\n\t"
    "v_perm_b32 v127, v159, v157, s61\n\t"
    "s_mov_b32 s62, 114688\n\t"
    "v_mfma_f32_16x16x32_bf16 a[144:147], v[32:35], v[80:83], a[144:147]\n\t"
    "v_perm_b32 v128, v162, v160, s60\n\t"
    "v_mfma_f32_16x16x32_bf16 a[176:179], v[36:39], v[80:83], a[176:179]\n\t"
    "v_perm_b32 v132, v162, v160, s61\n\t"
    "buffer_load_dwordx2 v[224:225], %[vwo2], s[88:91], s62 offen\n\t"
    "v_mfma_f32_16x16x32_bf16 a[180:183], v[36:39], v[84:87], a[180:183]\n\t"
    "v_perm_b32 v136, v163, v161, s60\n\t"
    "v_mfma_f32_16x16x32_bf16 a[148:151], v[32:35], v[84:87], a[148:151]\n\t"
    "v_perm_b32 v140, v163, v161, s61\n\t"
    "v_mfma_f32_16x16x32_bf16 a[152:155], v[32:35], v[88:91], a[152:155]\n\t"
    "v_perm_b32 v129, v166, v164, s60\n\t"
    "buffer_load_dwordx2 v[226:227], %[vwo2], s[88:91], s62 offen offset:512\n\t"
    "v_mfma_f32_16x16x32_bf16 a[184:187], v[36:39], v[88:91], a[184:187]\n\t"
    "v_perm_b32 v133, v166, v164, s61\n\t"
    "v_mfma_f32_16x16x32_bf16 a[188:191], v[36:39], v[92:95], a[188:191]\n\t"
    "v_perm_b32 v137, v167, v165, s60\n\t"
    "v_mfma_f32_16x16x32_bf16 a[156:159], v[32:35], v[92:95], a[156:159]\n\t"
    "v_perm_b32 v141, v167, v165, s61\n\t"
    "buffer_load_dwordx2 v[228:229], %[vwo2], s[88:91], s62 offen offset:1024\n\t"
    "v_mfma_f32_16x16x32_bf16 a[220:223], v[40:43], v[92:95], a[220:223]\n\t"
    "v_perm_b32 v130, v170, v168, s60\n\t"
    "v_mfma_f32_16x16x32_bf16 a[252:255], v[44:47], v[92:95], a[252:255]\n\t"
    "v_perm_b32 v134, v170, v168, s61\n\t"
    "v_mfma_f32_16x16x32_bf16 a[248:251], v[44:47], v[88:91], a[248:251]\n\t"
    "v_perm_b32 v138, v171, v169, s60\n\t"
    "buffer_load_dwordx2 v[230:231], %[vwo2], s[88:91], s62 offen offset:1536\n\t"
    "v_mfma_f32_16x16x32_bf16 a[216:219], v[40:43], v[88:91], a[216:219]\n\t"
    "v_perm_b32 v142, v171, v169, s61\n\t"
    "v_mfma_f32_16x16x32_bf16 a[212:215], v[40:43], v[84:87], a[212:215]\n\t"
    "v_perm_b32 v131, v174, v172, s60\n\t"
    "v_mfma_f32_16x16x32_bf16 a[244:247], v[44:47], v[84:87], a[244:247]\n\t"
    "v_perm_b32 v135, v174, v172, s61\n\t"
    "buffer_load_dwordx2 v[232:233], %[vwo2], s[88:91], s62 offen offset:2048\n\t"
    "v_mfma_f32_16x16x32_bf16 a[240:243], v[44:47], v[80:83], a[240:243]\n\t"
    "v_perm_b32 v139, v175, v173, s60\n\t"
    "v_mfma_f32_16x16x32_bf16 a[208:211], v[40:43], v[80:83], a[208:211]\n\t"
    "v_perm_b32 v143, v175, v173, s61\n\t"
    "v_mfma_f32_16x16x32_bf16 a[204:207], v[40:43], v[12:15], a[204:207]\n\t"
    "buffer_load_dwordx2 v[234:235], %[vwo2], s[88:91], s62 offen offset:2560\n\t"
    "v_mfma_f32_16x16x32_bf16 a[236:239], v[44:47], v[12:15], a[236:239]\n\t"
    "v_mfma_f32_16x16x32_bf16 a[232:235], v[44:47], v[108:111], a[232:235]\n\t"
    "v_mfma_f32_16x16x32_bf16 a[200:203], v[40:43], v[108:111], a[200:203]\n\t"
    "buffer_load_dwordx2 v[236:237], %[vwo2], s[88:91], s62 offen offset:3072\n\t"
    "v_mfma_f32_16x16x32_bf16 a[196:199], v[40:43], v[104:107], a[196:199]\n\t"
    "v_mfma_f32_16x16x32_bf16 a[228:231], v[44:47], v[104:107], a[228:231]\n\t"
    "v_mfma_f32_16x16x32_bf16 a[224:227], v[44:47], v[100:103], a[224:227]\n\t"
    "buffer_load_dwordx2 v[238:239], %[vwo2], s[88:91], s62 offen offset:3584\n\t"
    "v_mfma_f32_16x16x32_bf16 a[192:195], v[40:43], v[100:103], a[192:195]\n\t"
    "s_nop 7\n\t"
    "v_accvgpr_read_b32 v80, a0\n\t"
    "v_accvgpr_read_b32 v81, a1\n\t"
    "v_accvgpr_read_b32 v82, a2\n\t"
    "v_accvgpr_read_b32 v83, a3\n\t"
    "v_accvgpr_read_b32 v84, a32\n\t"
    "v_accvgpr_read_b32 v85, a33\n\t"
    "v_accvgpr_read_b32 v86, a34\n\t"
    "v_accvgpr_read_b32 v87, a35\n\t"
    "v_permlane32_swap_b32 v80, v82\n\t"
    "v_permlane32_swap_b32 v81, v83\n\t"
    "v_permlane32_swap_b32 v84, v86\n\t"
    "v_permlane32_swap_b32 v85, v87\n\t"
    "v_pk_mul_f32 v[80:81], v[80:81], s[74:75]\n\t"
    "v_pk_mul_f32 v[82:83], v[82:83], s[76:77]\n\t"
    "v_pk_mul_f32 v[84:85], v[84:85], s[74:75]\n\t"
    "v_pk_mul_f32 v[86:87], v[86:87], s[76:77]\n\t"
    "v_exp_f32_e64 v88, -|v80|\n\t"
    "v_exp_f32_e64 v89, -|v81|\n\t"
    "v_exp_f32_e64 v90, -|v84|\n\t"
    "v_exp_f32_e64 v91, -|v85|\n\t"
    "v_exp_f32 v82, v82\n\t"
    "v_exp_f32 v83, v83\n\t"
    "v_exp_f32 v86, v86\n\t"
    "v_exp_f32 v87, v87\n\t"
    "v_pk_add_f32 v[92:93], v[88:89], 1.0 op_sel_hi:[1,0]\n\t"
    "v_pk_add_f32 v[94:95], v[90:91], 1.0 op_sel_hi:[1,0]\n\t"
    "v_pk_add_f32 v[88:89], v[88:89], 1.0 op_sel_hi:[1,0] neg_lo:[1,0] neg_hi:[1,0]\n\t"
    "v_pk_add_f32 v[90:91], v[90:91], 1.0 op_sel_hi:[1,0] neg_lo:[1,0] neg_hi:[1,0]\n\t"
    "v_pk_fma_f32 v[92:93], v[92:93], v[82:83], v[92:93]\n\t"
    "v_pk_fma_f32 v[94:95], v[94:95], v[86:87], v[94:95]\n\t"
    "v_rcp_f32 v92, v92\n\t"
    "v_rcp_f32 v93, v93\n\t"
    "v_rcp_f32 v94, v94\n\t"
    "v_rcp_f32 v95, v95\n\t"
    "v_pk_mul_f32 v[88:89], v[88:89], v[92:93]\n\t"
    "v_pk_mul_f32 v[90:91], v[90:91], v[94:95]\n\t"
    "v_bfi_b32 v88, s73, v88, v80\n\t"
    "v_bfi_b32 v89, s73, v89, v81\n\t"
    "v_bfi_b32 v90, s73, v90, v84\n\t"
    "v_bfi_b32 v91, s73, v91, v85\n\t"
    "v_cvt_pk_bf16_f32 v40, v88, v90\n\t"
    "v_cvt_pk_bf16_f32 v44, v89, v91\n\t"
    "s_waitcnt vmcnt(16)\n\t"
    "v_perm_b32 v144, v178, v176, s60\n\t"
    "v_perm_b32 v148, v178, v176, s61\n\t"
    "v_perm_b32 v152, v179, v177, s60\n\t"
    "v_perm_b32 v156, v179, v177, s61\n\t"
    "v_perm_b32 v145, v182, v180, s60\n\t"
    "v_perm_b32 v149, v182, v180, s61\n\t"
    "v_perm_b32 v153, v183, v181, s60\n\t"
    "v_perm_b32 v157, v183, v181, s61\n\t"
    "v_perm_b32 v146, v186, v184, s60\n\t"
    "v_perm_b32 v150, v186, v184, s61\n\t"
    "v_perm_b32 v154, v187, v185, s60\n\t"
    "v_perm_b32 v158, v187, v185, s61\n\t"
    "v_perm_b32 v147, v190, v188, s60\n\t"
    "v_perm_b32 v151, v190, v188, s61\n\t"
    "v_perm_b32 v155, v191, v189, s60\n\t"
    "v_perm_b32 v159, v191, v189, s61\n\t"
    "v_perm_b32 v160, v194, v192, s60\n\t"
    "v_perm_b32 v164, v194, v192, s61\n\t"
    "v_perm_b32 v168, v195, v193, s60\n\t"
    "v_perm_b32 v172, v195, v193, s61\n\t"
    "v_perm_b32 v161, v198, v196, s60\n\t"
    "v_perm_b32 v165, v198, v196, s61\n\t"
    "v_perm_b32 v169, v199, v197, s60\n\t"
    "v_perm_b32 v173, v199, v197, s61\n\t"
    "v_perm_b32 v162, v202, v200, s60\n\t"
    "v_perm_b32 v166, v202, v200, s61\n\t"
    "v_perm_b32 v170, v203, v201, s60\n\t"
    "v_perm_b32 v174, v203, v201, s61\n\t"
    "v_perm_b32 v163, v206, v204, s60\n\t"
    "v_perm_b32 v167, v206, v204, s61\n\t"
    "v_perm_b32 v171, v207, v205, s60\n\t"
    "v_perm_b32 v175, v207, v205, s61\n\t"
    "v_accvgpr_read_b32 v96, a64\n\t"
    "v_accvgpr_read_b32 v97, a65\n\t"
    "v_accvgpr_read_b32 v98, a66\n\t"
    "v_accvgpr_read_b32 v99, a67\n\t"
    "v_accvgpr_read_b32 v100, a96\n\t"
    "v_accvgpr_read_b32 v101, a97\n\t"
    "v_accvgpr_read_b32 v102, a98\n\t"
    "v_accvgpr_read_b32 v103, a99\n\t"
    "v_permlane32_swap_b32 v96, v98\n\t"
    "v_permlane32_swap_b32 v97, v99\n\t"
    "v_permlane32_swap_b32 v100, v102\n\t"
    "v_permlane32_swap_b32 v101, v103\n\t"
    "v_pk_mul_f32 v[96:97], v[96:97], s[74:75]\n\t"
    "v_pk_mul_f32 v[98:99], v[98:99], s[76:77]\n\t"
    "v_pk_mul_f32 v[100:101], v[100:101], s[74:75]\n\t"
    "v_pk_mul_f32 v[102:103], v[102:103], s[76:77]\n\t"
    "v_exp_f32_e64 v104, -|v96|\n\t"
    "v_exp_f32_e64 v105, -|v97|\n\t"
    "v_exp_f32_e64 v106, -|v100|\n\t"
    "v_exp_f32_e64 v107, -|v101|\n\t"
    "v_exp_f32 v98, v98\n\t"
    "v_exp_f32 v99, v99\n\t"
    "v_exp_f32 v102, v102\n\t"
    "v_exp_f32 v103, v103\n\t"
    "v_pk_add_f32 v[108:109], v[104:105], 1.0 op_sel_hi:[1,0]\n\t"
    "v_pk_add_f32 v[110:111], v[106:107], 1.0 op_sel_hi:[1,0]\n\t"
    "v_pk_add_f32 v[104:105], v[104:105], 1.0 op_sel_hi:[1,0] neg_lo:[1,0] neg_hi:[1,0]\n\t"
    "v_pk_add_f32 v[106:107], v[106:107], 1.0 op_sel_hi:[1,0] neg_lo:[1,0] neg_hi:[1,0]\n\t"
    "v_pk_fma_f32 v[108:109], v[108:109], v[98:99], v[108:109]\n\t"
    "v_pk_fma_f32 v[110:111], v[110:111], v[102:103], v[110:111]\n\t"
    "v_rcp_f32 v108, v108\n\t"
    "v_rcp_f32 v109, v109\n\t"
    "v_rcp_f32 v110, v110\n\t"
    "v_rcp_f32 v111, v111\n\t"
    "v_pk_mul_f32 v[104:105], v[104:105], v[108:109]\n\t"
    "v_pk_mul_f32 v[106:107], v[106:107], v[110:111]\n\t"
    "v_bfi_b32 v104, s73, v104, v96\n\t"
    "v_bfi_b32 v105, s73, v105, v97\n\t"
    "v_bfi_b32 v106, s73, v106, v100\n\t"
    "v_bfi_b32 v107, s73, v107, v101\n\t"
    "v_cvt_pk_bf16_f32 v41, v104, v106\n\t"
    "v_cvt_pk_bf16_f32 v45, v105, v107\n\t"
    "v_accvgpr_read_b32 v80, a128\n\t"
    "v_accvgpr_read_b32 v81, a129\n\t"
    "v_accvgpr_read_b32 v82, a130\n\t"
    "v_accvgpr_read_b32 v83, a131\n\t"
    "v_accvgpr_read_b32 v84, a160\n\t"
    "v_accvgpr_read_b32 v85, a161\n\t"
    "v_accvgpr_read_b32 v86, a162\n\t"
    "v_accvgpr_read_b32 v87, a163\n\t"
    "v_permlane32_swap_b32 v80, v82\n\t"
    "v_permlane32_swap_b32 v81, v83\n\t"
    "v_permlane32_swap_b32 v84, v86\n\t"
    "v_permlane32_swap_b32 v85, v87\n\t"
    "v_pk_mul_f32 v[80:81], v[80:81], s[74:75]\n\t"
    "v_pk_mul_f32 v[82:83], v[82:83], s[76:77]\n\t"
    "v_pk_mul_f32 v[84:85], v[84:85], s[74:75]\n\t"
    "v_pk_mul_f32 v[86:87], v[86:87], s[76:77]\n\t"
    "v_exp_f32_e64 v88, -|v80|\n\t"
    "v_exp_f32_e64 v89, -|v81|\n\t"
    "v_exp_f32_e64 v90, -|v84|\n\t"
    "v_exp_f32_e64 v91, -|v85|\n\t"
    "v_exp_f32 v82, v82\n\t"
    "v_exp_f32 v83, v83\n\t"
    "v_exp_f32 v86, v86\n\t"
    "v_exp_f32 v87, v87\n\t"
    "v_pk_add_f32 v[92:93], v[88:89], 1.0 op_sel_hi:[1,0]\n\t"
    "v_pk_add_f32 v[94:95], v[90:91], 1.0 op_sel_hi:[1,0]\n\t"
    "v_pk_add_f32 v[88:89], v[88:89], 1.0 op_sel_hi:[1,0] neg_lo:[1,0] neg_hi:[1,0]\n\t"
    "v_pk_add_f32 v[90:91], v[90:91], 1.0 op_sel_hi:[1,0] neg_lo:[1,0] neg_hi:[1,0]\n\t"
    "v_pk_fma_f32 v[92:93], v[92:93], v[82:83], v[92:93]\n\t"
    "v_pk_fma_f32 v[94:95], v[94:95], v[86:87], v[94:95]\n\t"
    "v_rcp_f32 v92, v92\n\t"
    "v_rcp_f32 v93, v93\n\t"
    "v_rcp_f32 v94, v94\n\t"
    "v_rcp_f32 v95, v95\n\t"
    "v_pk_mul_f32 v[88:89], v[88:89], v[92:93]\n\t"
    "v_pk_mul_f32 v[90:91], v[90:91], v[94:95]\n\t"
    "v_bfi_b32 v88, s73, v88, v80\n\t"
    "v_bfi_b32 v89, s73, v89, v81\n\t"
    "v_bfi_b32 v90, s73, v90, v84\n\t"
    "v_bfi_b32 v91, s73, v91, v85\n\t"
    "v_cvt_pk_bf16_f32 v42, v88, v90\n\t"
    "v_cvt_pk_bf16_f32 v46, v89, v91\n\t"
    "s_waitcnt vmcnt(0)\n\t"
    "v_perm_b32 v176, v210, v208, s60\n\t"
    "v_perm_b32 v180, v210, v208, s61\n\t"
    "v_perm_b32 v184, v211, v209, s60\n\t"
    "v_perm_b32 v188, v211, v209, s61\n\t"
    "v_perm_b32 v177, v214, v212, s60\n\t"
    "v_perm_b32 v181, v214, v212, s61\n\t"
    "v_perm_b32 v185, v215, v213, s60\n\t"
    "v_perm_b32 v189, v215, v213, s61\n\t"
    "v_perm_b32 v178, v218, v216, s60\n\t"
    "v_perm_b32 v182, v218, v216, s61\n\t"
    "v_perm_b32 v186, v219, v217, s60\n\t"
    "v_perm_b32 v190, v219, v217, s61\n\t"
    "v_perm_b32 v179, v222, v220, s60\n\t"
    "v_perm_b32 v183, v222, v220, s61\n\t"
    "v_perm_b32 v187, v223, v221, s60\n\t"
    "v_perm_b32 v191, v223, v221, s61\n\t"
    "v_perm_b32 v192, v226, v224, s60\n\t"
    "v_perm_b32 v196, v226, v224, s61\n\t"
    "v_perm_b32 v200, v227, v225, s60\n\t"
    "v_perm_b32 v204, v227, v225, s61\n\t"
    "v_perm_b32 v193, v230, v228, s60\n\t"
    "v_perm_b32 v197, v230, v228, s61\n\t"
    "v_perm_b32 v201, v231, v229, s60\n\t"
    "v_perm_b32 v205, v231, v229, s61\n\t"
    "v_perm_b32 v194, v234, v232, s60\n\t"
    "v_perm_b32 v198, v234, v232, s61\n\t"
    "v_perm_b32 v202, v235, v233, s60\n\t"
    "v_perm_b32 v206, v235, v233, s61\n\t"
    "v_perm_b32 v195, v238, v236, s60\n\t"
    "v_perm_b32 v199, v238, v236, s61\n\t"
    "v_perm_b32 v203, v239, v237, s60\n\t"
    "v_perm_b32 v207, v239, v237, s61\n\t"
    "v_accvgpr_read_b32 v96, a192\n\t"
    "v_accvgpr_read_b32 v97, a193\n\t"
    "v_accvgpr_read_b32 v98, a194\n\t"
    "v_accvgpr_read_b32 v99, a195\n\t"
    "v_accvgpr_read_b32 v100, a224\n\t"
    "v_accvgpr_read_b32 v101, a225\n\t"
    "v_accvgpr_read_b32 v102, a226\n\t"
    "v_accvgpr_read_b32 v103, a227\n\t"
    "v_permlane32_swap_b32 v96, v98\n\t"
    "v_permlane32_swap_b32 v97, v99\n\t"
    "v_permlane32_swap_b32 v100, v102\n\t"
    "v_permlane32_swap_b32 v101, v103\n\t"
    "v_pk_mul_f32 v[96:97], v[96:97], s[74:75]\n\t"
    "v_pk_mul_f32 v[98:99], v[98:99], s[76:77]\n\t"
    "v_pk_mul_f32 v[100:101], v[100:101], s[74:75]\n\t"
    "v_pk_mul_f32 v[102:103], v[102:103], s[76:77]\n\t"
    "v_exp_f32_e64 v104, -|v96|\n\t"
    "v_exp_f32_e64 v105, -|v97|\n\t"
    "v_exp_f32_e64 v106, -|v100|\n\t"
    "v_exp_f32_e64 v107, -|v101|\n\t"
    "v_exp_f32 v98, v98\n\t"
    "v_exp_f32 v99, v99\n\t"
    "v_exp_f32 v102, v102\n\t"
    "v_exp_f32 v103, v103\n\t"
    "v_pk_add_f32 v[108:109], v[104:105], 1.0 op_sel_hi:[1,0]\n\t"
    "v_pk_add_f32 v[110:111], v[106:107], 1.0 op_sel_hi:[1,0]\n\t"
    "v_pk_add_f32 v[104:105], v[104:105], 1.0 op_sel_hi:[1,0] neg_lo:[1,0] neg_hi:[1,0]\n\t"
    "v_pk_add_f32 v[106:107], v[106:107], 1.0 op_sel_hi:[1,0] neg_lo:[1,0] neg_hi:[1,0]\n\t"
    "v_pk_fma_f32 v[108:109], v[108:109], v[98:99], v[108:109]\n\t"
    "v_pk_fma_f32 v[110:111], v[110:111], v[102:103], v[110:111]\n\t"
    "v_rcp_f32 v108, v108\n\t"
    "v_rcp_f32 v109, v109\n\t"
    "v_rcp_f32 v110, v110\n\t"
    "v_rcp_f32 v111, v111\n\t"
    "v_pk_mul_f32 v[104:105], v[104:105], v[108:109]\n\t"
    "v_pk_mul_f32 v[106:107], v[106:107], v[110:111]\n\t"
    "v_bfi_b32 v104, s73, v104, v96\n\t"
    "v_bfi_b32 v105, s73, v105, v97\n\t"
    "v_bfi_b32 v106, s73, v106, v100\n\t"
    "v_bfi_b32 v107, s73, v107, v101\n\t"
    "v_cvt_pk_bf16_f32 v43, v104, v106\n\t"
    "v_cvt_pk_bf16_f32 v47, v105, v107\n\t"
    "ds_write_b128 %[vzw0], v[40:43]\n\t"
    "ds_write_b128 %[vzw1], v[44:47]\n\t"
    "v_accvgpr_read_b32 v208, a4\n\t"
    "v_accvgpr_read_b32 v209, a5\n\t"
    "v_accvgpr_read_b32 v210, a6\n\t"
    "v_accvgpr_read_b32 v211, a7\n\t"
    "v_accvgpr_read_b32 v212, a36\n\t"
    "v_accvgpr_read_b32 v213, a37\n\t"
    "v_accvgpr_read_b32 v214, a38\n\t"
    "v_accvgpr_read_b32 v215, a39\n\t"
    "v_permlane32_swap_b32 v208, v210\n\t"
    "v_permlane32_swap_b32 v209, v211\n\t"
    "v_permlane32_swap_b32 v212, v214\n\t"
    "v_permlane32_swap_b32 v213, v215\n\t"
    "v_pk_mul_f32 v[208:209], v[208:209], s[74:75]\n\t"
    "v_pk_mul_f32 v[210:211], v[210:211], s[76:77]\n\t"
    "v_pk_mul_f32 v[212:213], v[212:213], s[74:75]\n\t"
    "v_pk_mul_f32 v[214:215], v[214:215], s[76:77]\n\t"
    "v_exp_f32_e64 v216, -|v208|\n\t"
    "v_exp_f32_e64 v217, -|v209|\n\t"
    "v_exp_f32_e64 v218, -|v212|\n\t"
    "v_exp_f32_e64 v219, -|v213|\n\t"
    "v_exp_f32 v210, v210\n\t"
    "v_exp_f32 v211, v211\n\t"
    "v_exp_f32 v214, v214\n\t"
    "v_exp_f32 v215, v215\n\t"
    "s_waitcnt lgkmcnt(0)\n\t"
    "s_barrier\n\t"
    "ds_read_b128 v[80:83], %[vzr0] offset:0\n\t"
    "v_pk_add_f32 v[220:221], v[216:217], 1.0 op_sel_hi:[1,0]\n\t"
    "v_pk_add_f32 v[222:223], v[218:219], 1.0 op_sel_hi:[1,0]\n\t"
    "v_pk_add_f32 v[216:217], v[216:217], 1.0 op_sel_hi:[1,0] neg_lo:[1,0] neg_hi:[1,0]\n\t"
    "ds_read_b128 v[84:87], %[vzr1] offset:0\n\t"
    "v_pk_add_f32 v[218:219], v[218:219], 1.0 op_sel_hi:[1,0] neg_lo:[1,0] neg_hi:[1,0]\n\t"
    "v_pk_fma_f32 v[220:221], v[220:221], v[210:211], v[220:221]\n\t"
    "v_pk_fma_f32 v[222:223], v[222:223], v[214:215], v[222:223]\n\t"
    "ds_read_b128 v[88:91], %[vzr0] offset:16384\n\t"
    "v_rcp_f32 v220, v220\n\t"
    "v_rcp_f32 v221, v221\n\t"
    "ds_read_b128 v[92:95], %[vzr1] offset:16384\n\t"
    "v_rcp_f32 v222, v222\n\t"
    "v_rcp_f32 v223, v223\n\t"
    "v_pk_mul_f32 v[216:217], v[216:217], v[220:221]\n\t"
    "v_pk_mul_f32 v[218:219], v[218:219], v[222:223]\n\t"
    "ds_read_b128 v[96:99], %[vzr0] offset:32768\n\t"
    "v_bfi_b32 v216, s73, v216, v208\n\t"
    "v_bfi_b32 v217, s73, v217, v209\n\t"
    "v_bfi_b32 v218, s73, v218, v212\n\t"
    "ds_read_b128 v[100:103], %[vzr1] offset:32768\n\t"
    "v_bfi_b32 v219, s73, v219, v213\n\t"
    "v_cvt_pk_bf16_f32 v40, v216, v218\n\t"
    "v_cvt_pk_bf16_f32 v44, v217, v219\n\t"
    "v_accvgpr_read_b32 v224, a68\n\t"
    "ds_read_b128 v[104:107], %[vzr0] offset:49152\n\t"
    "v_accvgpr_read_b32 v225, a69\n\t"
    "v_accvgpr_read_b32 v226, a70\n\t"
    "v_accvgpr_read_b32 v227, a71\n\t"
    "v_accvgpr_read_b32 v228, a100\n\t"
    "ds_read_b128 v[108:111], %[vzr1] offset:49152\n\t"
    "v_accvgpr_read_b32 v229, a101\n\t"
    "v_accvgpr_read_b32 v230, a102\n\t"
    "v_accvgpr_read_b32 v231, a103\n\t"
    "v_permlane32_swap_b32 v224, v226\n\t"
    "v_permlane32_swap_b32 v225, v227\n\t"
    "v_permlane32_swap_b32 v228, v230\n\t"
    "v_permlane32_swap_b32 v229, v231\n\t"
    "v_pk_mul_f32 v[224:225], v[224:225], s[74:75]\n\t"
    "v_pk_mul_f32 v[226:227], v[226:227], s[76:77]\n\t"
    "v_pk_mul_f32 v[228:229], v[228:229], s[74:75]\n\t"
    "v_pk_mul_f32 v[230:231], v[230:231], s[76:77]\n\t"
    "s_waitcnt lgkmcnt(7)\n\t"
    "v_mfma_f32_16x16x32_bf16 v[16:19], v[80:83], v[48:51], 0\n\t"
    "v_exp_f32_e64 v232, -|v224|\n\t"
    "v_mfma_f32_16x16x32_bf16 v[20:23], v[80:83], v[52:55], 0\n\t"
    "v_exp_f32_e64 v233, -|v225|\n\t"
    "v_mfma_f32_16x16x32_bf16 v[24:27], v[80:83], v[56:59], 0\n\t"
    "v_exp_f32_e64 v234, -|v228|\n\t"
    "v_mfma_f32_16x16x32_bf16 v[28:31], v[80:83], v[60:63], 0\n\t"
    "v_exp_f32_e64 v235, -|v229|\n\t"
    "s_waitcnt lgkmcnt(5)\n\t"
    "v_mfma_f32_16x16x32_bf16 v[16:19], v[84:87], v[64:67], v[16:19]\n\t"
    "v_exp_f32 v226, v226\n\t"
    "v_mfma_f32_16x16x32_bf16 v[20:23], v[84:87], v[68:71], v[20:23]\n\t"
    "v_exp_f32 v227, v227\n\t"
    "v_exp_f32 v230, v230\n\t"
    "v_exp_f32 v231, v231\n\t"
    "v_pk_add_f32 v[236:237], v[232:233], 1.0 op_sel_hi:[1,0]\n\t"
    "v_pk_add_f32 v[238:239], v[234:235], 1.0 op_sel_hi:[1,0]\n\t"
    "v_pk_add_f32 v[232:233], v[232:233], 1.0 op_sel_hi:[1,0] neg_lo:[1,0] neg_hi:[1,0]\n\t"
    "v_pk_add_f32 v[234:235], v[234:235], 1.0 op_sel_hi:[1,0] neg_lo:[1,0] neg_hi:[1,0]\n\t"
    "v_pk_fma_f32 v[236:237], v[236:237], v[226:227], v[236:237]\n\t"
    "v_pk_fma_f32 v[238:239], v[238:239], v[230:231], v[238:239]\n\t"
    "v_rcp_f32 v236, v236\n\t"
    "v_rcp_f32 v237, v237\n\t"
    "v_rcp_f32 v238, v238\n\t"
    "v_rcp_f32 v239, v239\n\t"
    "v_pk_mul_f32 v[232:233], v[232:233], v[236:237]\n\t"
    "v_pk_mul_f32 v[234:235], v[234:235], v[238:239]\n\t"
    "v_mfma_f32_16x16x32_bf16 v[24:27], v[84:87], v[72:75], v[24:27]\n\t"
    "v_bfi_b32 v232, s73, v232, v224\n\t"
    "v_bfi_b32 v233, s73, v233, v225\n\t"
    "v_bfi_b32 v234, s73, v234, v228\n\t"
    "v_mfma_f32_16x16x32_bf16 v[28:31], v[84:87], v[76:79], v[28:31]\n\t"
    "v_bfi_b32 v235, s73, v235, v229\n\t"
    "v_cvt_pk_bf16_f32 v41, v232, v234\n\t"
    "v_cvt_pk_bf16_f32 v45, v233, v235\n\t"
    "s_waitcnt lgkmcnt(4)\n\t"
    "v_mfma_f32_16x16x32_bf16 v[16:19], v[88:91], v[112:115], v[16:19]\n\t"
    "v_accvgpr_read_b32 v208, a132\n\t"
    "v_accvgpr_read_b32 v209, a133\n\t"
    "v_mfma_f32_16x16x32_bf16 v[20:23], v[88:91], v[116:119], v[20:23]\n\t"
    "v_accvgpr_read_b32 v210, a134\n\t"
    "v_accvgpr_read_b32 v211, a135\n\t"
    "v_mfma_f32_16x16x32_bf16 v[24:27], v[88:91], v[120:123], v[24:27]\n\t"
    "v_accvgpr_read_b32 v212, a164\n\t"
    "v_accvgpr_read_b32 v213, a165\n\t"
    "v_accvgpr_read_b32 v214, a166\n\t"
    "v_accvgpr_read_b32 v215, a167\n\t"
    "v_permlane32_swap_b32 v208, v210\n\t"
    "v_permlane32_swap_b32 v209, v211\n\t"
    "v_permlane32_swap_b32 v212, v214\n\t"
    "v_permlane32_swap_b32 v213, v215\n\t"
    "v_pk_mul_f32 v[208:209], v[208:209], s[74:75]\n\t"
    "v_pk_mul_f32 v[210:211], v[210:211], s[76:77]\n\t"
    "v_pk_mul_f32 v[212:213], v[212:213], s[74:75]\n\t"
    "v_pk_mul_f32 v[214:215], v[214:215], s[76:77]\n\t"
    "v_mfma_f32_16x16x32_bf16 v[28:31], v[88:91], v[124:127], v[28:31]\n\t"
    "v_exp_f32_e64 v216, -|v208|\n\t"
    "s_waitcnt lgkmcnt(3)\n\t"
    "v_mfma_f32_16x16x32_bf16 v[16:19], v[92:95], v[128:131], v[16:19]\n\t"
    "v_exp_f32_e64 v217, -|v209|\n\t"
    "v_mfma_f32_16x16x32_bf16 v[20:23], v[92:95], v[132:135], v[20:23]\n\t"
    "v_exp_f32_e64 v218, -|v212|\n\t"
    "v_mfma_f32_16x16x32_bf16 v[24:27], v[92:95], v[136:139], v[24:27]\n\t"
    "v_exp_f32_e64 v219, -|v213|\n\t"
    "v_mfma_f32_16x16x32_bf16 v[28:31], v[92:95], v[140:143], v[28:31]\n\t"
    "v_exp_f32 v210, v210\n\t"
    "s_waitcnt lgkmcnt(2)\n\t"
    "v_mfma_f32_16x16x32_bf16 v[16:19], v[96:99], v[144:147], v[16:19]\n\t"
    "v_exp_f32 v211, v211\n\t"
    "v_exp_f32 v214, v214\n\t"
    "v_exp_f32 v215, v215\n\t"
    "v_pk_add_f32 v[220:221], v[216:217], 1.0 op_sel_hi:[1,0]\n\t"
    "v_pk_add_f32 v[222:223], v[218:219], 1.0 op_sel_hi:[1,0]\n\t"
    "v_pk_add_f32 v[216:217], v[216:217], 1.0 op_sel_hi:[1,0] neg_lo:[1,0] neg_hi:[1,0]\n\t"
    "v_pk_add_f32 v[218:219], v[218:219], 1.0 op_sel_hi:[1,0] neg_lo:[1,0] neg_hi:[1,0]\n\t"
    "v_pk_fma_f32 v[220:221], v[220:221], v[210:211], v[220:221]\n\t"
    "v_pk_fma_f32 v[222:223], v[222:223], v[214:215], v[222:223]\n\t"
    "v_rcp_f32 v220, v220\n\t"
    "v_rcp_f32 v221, v221\n\t"
    "v_rcp_f32 v222, v222\n\t"
    "v_rcp_f32 v223, v223\n\t"
    "v_pk_mul_f32 v[216:217], v[216:217], v[220:221]\n\t"
    "v_pk_mul_f32 v[218:219], v[218:219], v[222:223]\n\t"
    "v_mfma_f32_16x16x32_bf16 v[20:23], v[96:99], v[148:151], v[20:23]\n\t"
    "v_bfi_b32 v216, s73, v216, v208\n\t"
    "v_bfi_b32 v217, s73, v217, v209\n\t"
    "v_bfi_b32 v218, s73, v218, v212\n\t"
    "v_mfma_f32_16x16x32_bf16 v[24:27], v[96:99], v[152:155], v[24:27]\n\t"
    "v_bfi_b32 v219, s73, v219, v213\n\t"
    "v_cvt_pk_bf16_f32 v42, v216, v218\n\t"
    "v_cvt_pk_bf16_f32 v46, v217, v219\n\t"
    "v_mfma_f32_16x16x32_bf16 v[28:31], v[96:99], v[156:159], v[28:31]\n\t"
    "v_accvgpr_read_b32 v224, a196\n\t"
    "v_accvgpr_read_b32 v225, a197\n\t"
    "s_waitcnt lgkmcnt(1)\n\t"
    "v_mfma_f32_16x16x32_bf16 v[16:19], v[100:103], v[160:163], v[16:19]\n\t"
    "v_accvgpr_read_b32 v226, a198\n\t"
    "v_accvgpr_read_b32 v227, a199\n\t"
    "v_mfma_f32_16x16x32_bf16 v[20:23], v[100:103], v[164:167], v[20:23]\n\t"
    "v_accvgpr_read_b32 v228, a228\n\t"
    "v_accvgpr_read_b32 v229, a229\n\t"
    "v_accvgpr_read_b32 v230, a230\n\t"
    "v_accvgpr_read_b32 v231, a231\n\t"
    "v_permlane32_swap_b32 v224, v226\n\t"
    "v_permlane32_swap_b32 v225, v227\n\t"
    "v_permlane32_swap_b32 v228, v230\n\t"
    "v_permlane32_swap_b32 v229, v231\n\t"
    "v_pk_mul_f32 v[224:225], v[224:225], s[74:75]\n\t"
    "v_pk_mul_f32 v[226:227], v[226:227], s[76:77]\n\t"
    "v_pk_mul_f32 v[228:229], v[228:229], s[74:75]\n\t"
    "v_pk_mul_f32 v[230:231], v[230:231], s[76:77]\n\t"
    "v_mfma_f32_16x16x32_bf16 v[24:27], v[100:103], v[168:171], v[24:27]\n\t"
    "v_exp_f32_e64 v232, -|v224|\n\t"
    "v_mfma_f32_16x16x32_bf16 v[28:31], v[100:103], v[172:175], v[28:31]\n\t"
    "v_exp_f32_e64 v233, -|v225|\n\t"
    "s_waitcnt lgkmcnt(0)\n\t"
    "v_mfma_f32_16x16x32_bf16 v[16:19], v[104:107], v[176:179], v[16:19]\n\t"
    "v_exp_f32_e64 v234, -|v228|\n\t"
    "v_mfma_f32_16x16x32_bf16 v[20:23], v[104:107], v[180:183], v[20:23]\n\t"
    "v_exp_f32_e64 v235, -|v229|\n\t"
    "v_mfma_f32_16x16x32_bf16 v[24:27], v[104:107], v[184:187], v[24:27]\n\t"
    "v_exp_f32 v226, v226\n\t"
    "v_mfma_f32_16x16x32_bf16 v[28:31], v[104:107], v[188:191], v[28:31]\n\t"
    "v_exp_f32 v227, v227\n\t"
    "v_exp_f32 v230, v230\n\t"
    "v_exp_f32 v231, v231\n\t"
    "v_pk_add_f32 v[236:237], v[232:233], 1.0 op_sel_hi:[1,0]\n\t"
    "v_pk_add_f32 v[238:239], v[234:235], 1.0 op_sel_hi:[1,0]\n\t"
    "v_pk_add_f32 v[232:233], v[232:233], 1.0 op_sel_hi:[1,0] neg_lo:[1,0] neg_hi:[1,0]\n\t"
    "v_pk_add_f32 v[234:235], v[234:235], 1.0 op_sel_hi:[1,0] neg_lo:[1,0] neg_hi:[1,0]\n\t"
    "v_pk_fma_f32 v[236:237], v[236:237], v[226:227], v[236:237]\n\t"
    "v_pk_fma_f32 v[238:239], v[238:239], v[230:231], v[238:239]\n\t"
    "v_rcp_f32 v236, v236\n\t"
    "v_rcp_f32 v237, v237\n\t"
    "v_rcp_f32 v238, v238\n\t"
    "v_rcp_f32 v239, v239\n\t"
    "v_pk_mul_f32 v[232:233], v[232:233], v[236:237]\n\t"
    "v_pk_mul_f32 v[234:235], v[234:235], v[238:239]\n\t"
    "v_mfma_f32_16x16x32_bf16 v[16:19], v[108:111], v[192:195], v[16:19]\n\t"
    "v_bfi_b32 v232, s73, v232, v224\n\t"
    "v_bfi_b32 v233, s73, v233, v225\n\t"
    "v_bfi_b32 v234, s73, v234, v228\n\t"
    "v_bfi_b32 v235, s73, v235, v229\n\t"
    "v_cvt_pk_bf16_f32 v43, v232, v234\n\t"
    "v_cvt_pk_bf16_f32 v47, v233, v235\n\t"
    "ds_write_b128 %[vzw0], v[40:43] offset:2048\n\t"
    "ds_write_b128 %[vzw1], v[44:47] offset:2048\n\t"
    "v_mfma_f32_16x16x32_bf16 v[20:23], v[108:111], v[196:199], v[20:23]\n\t"
    "v_accvgpr_read_b32 v208, a8\n\t"
    "v_accvgpr_read_b32 v209, a9\n\t"
    "v_mfma_f32_16x16x32_bf16 v[24:27], v[108:111], v[200:203], v[24:27]\n\t"
    "v_accvgpr_read_b32 v210, a10\n\t"
    "v_accvgpr_read_b32 v211, a11\n\t"
    "v_mfma_f32_16x16x32_bf16 v[28:31], v[108:111], v[204:207], v[28:31]\n\t"
    "v_accvgpr_read_b32 v212, a40\n\t"
    "v_accvgpr_read_b32 v213, a41\n\t"
    "v_accvgpr_read_b32 v214, a42\n\t"
    "v_accvgpr_read_b32 v215, a43\n\t"
    "v_permlane32_swap_b32 v208, v210\n\t"
    "v_permlane32_swap_b32 v209, v211\n\t"
    "v_permlane32_swap_b32 v212, v214\n\t"
    "v_permlane32_swap_b32 v213, v215\n\t"
    "v_pk_mul_f32 v[208:209], v[208:209], s[74:75]\n\t"
    "v_pk_mul_f32 v[210:211], v[210:211], s[76:77]\n\t"
    "v_pk_mul_f32 v[212:213], v[212:213], s[74:75]\n\t"
    "v_pk_mul_f32 v[214:215], v[214:215], s[76:77]\n\t"
    "v_exp_f32_e64 v216, -|v208|\n\t"
    "v_exp_f32_e64 v217, -|v209|\n\t"
    "s_xor_b32 s64, %[mx], 0\n\t"
    "v_cvt_pk_bf16_f32 v32, v16, v20\n\t"
    "v_cvt_pk_bf16_f32 v33, v24, v28\n\t"
    "buffer_store_dwordx2 v[32:33], %[vout], s[56:59], s64 offen sc1\n\t"
    "v_exp_f32_e64 v218, -|v212|\n\t"
    "v_exp_f32_e64 v219, -|v213|\n\t"
    "v_cvt_pk_bf16_f32 v34, v17, v21\n\t"
    "v_cvt_pk_bf16_f32 v35, v25, v29\n\t"
    "buffer_store_dwordx2 v[34:35], %[vout], s[56:59], s64 offen offset:512 sc1\n\t"
    "v_exp_f32 v210, v210\n\t"
    "v_exp_f32 v211, v211\n\t"
    "v_cvt_pk_bf16_f32 v36, v18, v22\n\t"
    "v_cvt_pk_bf16_f32 v37, v26, v30\n\t"
    "s_xor_b32 s64, %[mx], 8192\n\t"
    "buffer_store_dwordx2 v[36:37], %[vout], s[56:59], s64 offen sc1\n\t"
    "v_exp_f32 v214, v214\n\t"
    "v_cvt_pk_bf16_f32 v38, v19, v23\n\t"
    "v_cvt_pk_bf16_f32 v39, v27, v31\n\t"
    "buffer_store_dwordx2 v[38:39], %[vout], s[56:59], s64 offen offset:512 sc1\n\t"
    "v_exp_f32 v215, v215\n\t"
    "s_waitcnt lgkmcnt(0)\n\t"
    "s_barrier\n\t"
    "ds_read_b128 v[80:83], %[vzr0] offset:2048\n\t"
    "v_pk_add_f32 v[220:221], v[216:217], 1.0 op_sel_hi:[1,0]\n\t"
    "v_pk_add_f32 v[222:223], v[218:219], 1.0 op_sel_hi:[1,0]\n\t"
    "v_pk_add_f32 v[216:217], v[216:217], 1.0 op_sel_hi:[1,0] neg_lo:[1,0] neg_hi:[1,0]\n\t"
    "ds_read_b128 v[84:87], %[vzr1] offset:2048\n\t"
    "v_pk_add_f32 v[218:219], v[218:219], 1.0 op_sel_hi:[1,0] neg_lo:[1,0] neg_hi:[1,0]\n\t"
    "v_pk_fma_f32 v[220:221], v[220:221], v[210:211], v[220:221]\n\t"
    "v_pk_fma_f32 v[222:223], v[222:223], v[214:215], v[222:223]\n\t"
    "ds_read_b128 v[88:91], %[vzr0] offset:18432\n\t"
    "v_rcp_f32 v220, v220\n\t"
    "v_rcp_f32 v221, v221\n\t"
    "ds_read_b128 v[92:95], %[vzr1] offset:18432\n\t"
    "v_rcp_f32 v222, v222\n\t"
    "v_rcp_f32 v223, v223\n\t"
    "v_pk_mul_f32 v[216:217], v[216:217], v[220:221]\n\t"
    "v_pk_mul_f32 v[218:219], v[218:219], v[222:223]\n\t"
    "ds_read_b128 v[96:99], %[vzr0] offset:34816\n\t"
    "v_bfi_b32 v216, s73, v216, v208\n\t"
    "v_bfi_b32 v217, s73, v217, v209\n\t"
    "v_bfi_b32 v218, s73, v218, v212\n\t"
    "ds_read_b128 v[100:103], %[vzr1] offset:34816\n\t"
    "v_bfi_b32 v219, s73, v219, v213\n\t"
    "v_cvt_pk_bf16_f32 v40, v216, v218\n\t"
    "v_cvt_pk_bf16_f32 v44, v217, v219\n\t"
    "v_accvgpr_read_b32 v224, a72\n\t"
    "ds_read_b128 v[104:107], %[vzr0] offset:51200\n\t"
    "v_accvgpr_read_b32 v225, a73\n\t"
    "v_accvgpr_read_b32 v226, a74\n\t"
    "v_accvgpr_read_b32 v227, a75\n\t"
    "v_accvgpr_read_b32 v228, a104\n\t"
    "ds_read_b128 v[108:111], %[vzr1] offset:51200\n\t"
    "v_accvgpr_read_b32 v229, a105\n\t"
    "v_accvgpr_read_b32 v230, a106\n\t"
    "v_accvgpr_read_b32 v231, a107\n\t"
    "v_permlane32_swap_b32 v224, v226\n\t"
    "v_permlane32_swap_b32 v225, v227\n\t"
    "v_permlane32_swap_b32 v228, v230\n\t"
    "v_permlane32_swap_b32 v229, v231\n\t"
    "v_pk_mul_f32 v[224:225], v[224:225], s[74:75]\n\t"
    "v_pk_mul_f32 v[226:227], v[226:227], s[76:77]\n\t"
    "v_pk_mul_f32 v[228:229], v[228:229], s[74:75]\n\t"
    "v_pk_mul_f32 v[230:231], v[230:231], s[76:77]\n\t"
    "s_waitcnt lgkmcnt(7)\n\t"
    "v_mfma_f32_16x16x32_bf16 v[16:19], v[80:83], v[48:51], 0\n\t"
    "v_exp_f32_e64 v232, -|v224|\n\t"
    "v_mfma_f32_16x16x32_bf16 v[20:23], v[80:83], v[52:55], 0\n\t"
    "v_exp_f32_e64 v233, -|v225|\n\t"
    "v_mfma_f32_16x16x32_bf16 v[24:27], v[80:83], v[56:59], 0\n\t"
    "v_exp_f32_e64 v234, -|v228|\n\t"
    "v_mfma_f32_16x16x32_bf16 v[28:31], v[80:83], v[60:63], 0\n\t"
    "v_exp_f32_e64 v235, -|v229|\n\t"
    "s_waitcnt lgkmcnt(5)\n\t"
    "v_mfma_f32_16x16x32_bf16 v[16:19], v[84:87], v[64:67], v[16:19]\n\t"
    "v_exp_f32 v226, v226\n\t"
    "v_mfma_f32_16x16x32_bf16 v[20:23], v[84:87], v[68:71], v[20:23]\n\t"
    "v_exp_f32 v227, v227\n\t"
    "v_exp_f32 v230, v230\n\t"
    "v_exp_f32 v231, v231\n\t"
    "v_pk_add_f32 v[236:237], v[232:233], 1.0 op_sel_hi:[1,0]\n\t"
    "v_pk_add_f32 v[238:239], v[234:235], 1.0 op_sel_hi:[1,0]\n\t"
    "v_pk_add_f32 v[232:233], v[232:233], 1.0 op_sel_hi:[1,0] neg_lo:[1,0] neg_hi:[1,0]\n\t"
    "v_pk_add_f32 v[234:235], v[234:235], 1.0 op_sel_hi:[1,0] neg_lo:[1,0] neg_hi:[1,0]\n\t"
    "v_pk_fma_f32 v[236:237], v[236:237], v[226:227], v[236:237]\n\t"
    "v_pk_fma_f32 v[238:239], v[238:239], v[230:231], v[238:239]\n\t"
    "v_rcp_f32 v236, v236\n\t"
    "v_rcp_f32 v237, v237\n\t"
    "v_rcp_f32 v238, v238\n\t"
    "v_rcp_f32 v239, v239\n\t"
    "v_pk_mul_f32 v[232:233], v[232:233], v[236:237]\n\t"
    "v_pk_mul_f32 v[234:235], v[234:235], v[238:239]\n\t"
    "v_mfma_f32_16x16x32_bf16 v[24:27], v[84:87], v[72:75], v[24:27]\n\t"
    "v_bfi_b32 v232, s73, v232, v224\n\t"
    "v_bfi_b32 v233, s73, v233, v225\n\t"
    "v_bfi_b32 v234, s73, v234, v228\n\t"
    "v_mfma_f32_16x16x32_bf16 v[28:31], v[84:87], v[76:79], v[28:31]\n\t"
    "v_bfi_b32 v235, s73, v235, v229\n\t"
    "v_cvt_pk_bf16_f32 v41, v232, v234\n\t"
    "v_cvt_pk_bf16_f32 v45, v233, v235\n\t"
    "s_waitcnt lgkmcnt(4)\n\t"
    "v_mfma_f32_16x16x32_bf16 v[16:19], v[88:91], v[112:115], v[16:19]\n\t"
    "v_accvgpr_read_b32 v208, a136\n\t"
    "v_accvgpr_read_b32 v209, a137\n\t"
    "v_mfma_f32_16x16x32_bf16 v[20:23], v[88:91], v[116:119], v[20:23]\n\t"
    "v_accvgpr_read_b32 v210, a138\n\t"
    "v_accvgpr_read_b32 v211, a139\n\t"
    "v_mfma_f32_16x16x32_bf16 v[24:27], v[88:91], v[120:123], v[24:27]\n\t"
    "v_accvgpr_read_b32 v212, a168\n\t"
    "v_accvgpr_read_b32 v213, a169\n\t"
    "v_accvgpr_read_b32 v214, a170\n\t"
    "v_accvgpr_read_b32 v215, a171\n\t"
    "v_permlane32_swap_b32 v208, v210\n\t"
    "v_permlane32_swap_b32 v209, v211\n\t"
    "v_permlane32_swap_b32 v212, v214\n\t"
    "v_permlane32_swap_b32 v213, v215\n\t"
    "v_pk_mul_f32 v[208:209], v[208:209], s[74:75]\n\t"
    "v_pk_mul_f32 v[210:211], v[210:211], s[76:77]\n\t"
    "v_pk_mul_f32 v[212:213], v[212:213], s[74:75]\n\t"
    "v_pk_mul_f32 v[214:215], v[214:215], s[76:77]\n\t"
    "v_mfma_f32_16x16x32_bf16 v[28:31], v[88:91], v[124:127], v[28:31]\n\t"
    "v_exp_f32_e64 v216, -|v208|\n\t"
    "s_waitcnt lgkmcnt(3)\n\t"
    "v_mfma_f32_16x16x32_bf16 v[16:19], v[92:95], v[128:131], v[16:19]\n\t"
    "v_exp_f32_e64 v217, -|v209|\n\t"
    "v_mfma_f32_16x16x32_bf16 v[20:23], v[92:95], v[132:135], v[20:23]\n\t"
    "v_exp_f32_e64 v218, -|v212|\n\t"
    "v_mfma_f32_16x16x32_bf16 v[24:27], v[92:95], v[136:139], v[24:27]\n\t"
    "v_exp_f32_e64 v219, -|v213|\n\t"
    "v_mfma_f32_16x16x32_bf16 v[28:31], v[92:95], v[140:143], v[28:31]\n\t"
    "v_exp_f32 v210, v210\n\t"
    "s_waitcnt lgkmcnt(2)\n\t"
    "v_mfma_f32_16x16x32_bf16 v[16:19], v[96:99], v[144:147], v[16:19]\n\t"
    "v_exp_f32 v211, v211\n\t"
    "v_exp_f32 v214, v214\n\t"
    "v_exp_f32 v215, v215\n\t"
    "v_pk_add_f32 v[220:221], v[216:217], 1.0 op_sel_hi:[1,0]\n\t"
    "v_pk_add_f32 v[222:223], v[218:219], 1.0 op_sel_hi:[1,0]\n\t"
    "v_pk_add_f32 v[216:217], v[216:217], 1.0 op_sel_hi:[1,0] neg_lo:[1,0] neg_hi:[1,0]\n\t"
    "v_pk_add_f32 v[218:219], v[218:219], 1.0 op_sel_hi:[1,0] neg_lo:[1,0] neg_hi:[1,0]\n\t"
    "v_pk_fma_f32 v[220:221], v[220:221], v[210:211], v[220:221]\n\t"
    "v_pk_fma_f32 v[222:223], v[222:223], v[214:215], v[222:223]\n\t"
    "v_rcp_f32 v220, v220\n\t"
    "v_rcp_f32 v221, v221\n\t"
    "v_rcp_f32 v222, v222\n\t"
    "v_rcp_f32 v223, v223\n\t"
    "v_pk_mul_f32 v[216:217], v[216:217], v[220:221]\n\t"
    "v_pk_mul_f32 v[218:219], v[218:219], v[222:223]\n\t"
    "v_mfma_f32_16x16x32_bf16 v[20:23], v[96:99], v[148:151], v[20:23]\n\t"
    "v_bfi_b32 v216, s73, v216, v208\n\t"
    "v_bfi_b32 v217, s73, v217, v209\n\t"
    "v_bfi_b32 v218, s73, v218, v212\n\t"
    "v_mfma_f32_16x16x32_bf16 v[24:27], v[96:99], v[152:155], v[24:27]\n\t"
    "v_bfi_b32 v219, s73, v219, v213\n\t"
    "v_cvt_pk_bf16_f32 v42, v216, v218\n\t"
    "v_cvt_pk_bf16_f32 v46, v217, v219\n\t"
    "v_mfma_f32_16x16x32_bf16 v[28:31], v[96:99], v[156:159], v[28:31]\n\t"
    "v_accvgpr_read_b32 v224, a200\n\t"
    "v_accvgpr_read_b32 v225, a201\n\t"
    "s_waitcnt lgkmcnt(1)\n\t"
    "v_mfma_f32_16x16x32_bf16 v[16:19], v[100:103], v[160:163], v[16:19]\n\t"
    "v_accvgpr_read_b32 v226, a202\n\t"
    "v_accvgpr_read_b32 v227, a203\n\t"
    "v_mfma_f32_16x16x32_bf16 v[20:23], v[100:103], v[164:167], v[20:23]\n\t"
    "v_accvgpr_read_b32 v228, a232\n\t"
    "v_accvgpr_read_b32 v229, a233\n\t"
    "v_accvgpr_read_b32 v230, a234\n\t"
    "v_accvgpr_read_b32 v231, a235\n\t"
    "v_permlane32_swap_b32 v224, v226\n\t"
    "v_permlane32_swap_b32 v225, v227\n\t"
    "v_permlane32_swap_b32 v228, v230\n\t"
    "v_permlane32_swap_b32 v229, v231\n\t"
    "v_pk_mul_f32 v[224:225], v[224:225], s[74:75]\n\t"
    "v_pk_mul_f32 v[226:227], v[226:227], s[76:77]\n\t"
    "v_pk_mul_f32 v[228:229], v[228:229], s[74:75]\n\t"
    "v_pk_mul_f32 v[230:231], v[230:231], s[76:77]\n\t"
    "v_mfma_f32_16x16x32_bf16 v[24:27], v[100:103], v[168:171], v[24:27]\n\t"
    "v_exp_f32_e64 v232, -|v224|\n\t"
    "v_mfma_f32_16x16x32_bf16 v[28:31], v[100:103], v[172:175], v[28:31]\n\t"
    "v_exp_f32_e64 v233, -|v225|\n\t"
    "s_waitcnt lgkmcnt(0)\n\t"
    "v_mfma_f32_16x16x32_bf16 v[16:19], v[104:107], v[176:179], v[16:19]\n\t"
    "v_exp_f32_e64 v234, -|v228|\n\t"
    "v_mfma_f32_16x16x32_bf16 v[20:23], v[104:107], v[180:183], v[20:23]\n\t"
    "v_exp_f32_e64 v235, -|v229|\n\t"
    "v_mfma_f32_16x16x32_bf16 v[24:27], v[104:107], v[184:187], v[24:27]\n\t"
    "v_exp_f32 v226, v226\n\t"
    "v_mfma_f32_16x16x32_bf16 v[28:31], v[104:107], v[188:191], v[28:31]\n\t"
    "v_exp_f32 v227, v227\n\t"
    "v_exp_f32 v230, v230\n\t"
    "v_exp_f32 v231, v231\n\t"
    "v_pk_add_f32 v[236:237], v[232:233], 1.0 op_sel_hi:[1,0]\n\t"
    "v_pk_add_f32 v[238:239], v[234:235], 1.0 op_sel_hi:[1,0]\n\t"
    "v_pk_add_f32 v[232:233], v[232:233], 1.0 op_sel_hi:[1,0] neg_lo:[1,0] neg_hi:[1,0]\n\t"
    "v_pk_add_f32 v[234:235], v[234:235], 1.0 op_sel_hi:[1,0] neg_lo:[1,0] neg_hi:[1,0]\n\t"
    "v_pk_fma_f32 v[236:237], v[236:237], v[226:227], v[236:237]\n\t"
    "v_pk_fma_f32 v[238:239], v[238:239], v[230:231], v[238:239]\n\t"
    "v_rcp_f32 v236, v236\n\t"
    "v_rcp_f32 v237, v237\n\t"
    "v_rcp_f32 v238, v238\n\t"
    "v_rcp_f32 v239, v239\n\t"
    "v_pk_mul_f32 v[232:233], v[232:233], v[236:237]\n\t"
    "v_pk_mul_f32 v[234:235], v[234:235], v[238:239]\n\t"
    "v_mfma_f32_16x16x32_bf16 v[16:19], v[108:111], v[192:195], v[16:19]\n\t"
    "v_bfi_b32 v232, s73, v232, v224\n\t"
    "v_bfi_b32 v233, s73, v233, v225\n\t"
    "v_bfi_b32 v234, s73, v234, v228\n\t"
    "v_bfi_b32 v235, s73, v235, v229\n\t"
    "v_cvt_pk_bf16_f32 v43, v232, v234\n\t"
    "v_cvt_pk_bf16_f32 v47, v233, v235\n\t"
    "ds_write_b128 %[vzw0], v[40:43] offset:4096\n\t"
    "ds_write_b128 %[vzw1], v[44:47] offset:4096\n\t"
    "v_mfma_f32_16x16x32_bf16 v[20:23], v[108:111], v[196:199], v[20:23]\n\t"
    "v_accvgpr_read_b32 v208, a12\n\t"
    "v_accvgpr_read_b32 v209, a13\n\t"
    "v_mfma_f32_16x16x32_bf16 v[24:27], v[108:111], v[200:203], v[24:27]\n\t"
    "v_accvgpr_read_b32 v210, a14\n\t"
    "v_accvgpr_read_b32 v211, a15\n\t"
    "v_mfma_f32_16x16x32_bf16 v[28:31], v[108:111], v[204:207], v[28:31]\n\t"
    "v_accvgpr_read_b32 v212, a44\n\t"
    "v_accvgpr_read_b32 v213, a45\n\t"
    "v_accvgpr_read_b32 v214, a46\n\t"
    "v_accvgpr_read_b32 v215, a47\n\t"
    "v_permlane32_swap_b32 v208, v210\n\t"
    "v_permlane32_swap_b32 v209, v211\n\t"
    "v_permlane32_swap_b32 v212, v214\n\t"
    "v_permlane32_swap_b32 v213, v215\n\t"
    "v_pk_mul_f32 v[208:209], v[208:209], s[74:75]\n\t"
    "v_pk_mul_f32 v[210:211], v[210:211], s[76:77]\n\t"
    "v_pk_mul_f32 v[212:213], v[212:213], s[74:75]\n\t"
    "v_pk_mul_f32 v[214:215], v[214:215], s[76:77]\n\t"
    "v_exp_f32_e64 v216, -|v208|\n\t"
    "v_exp_f32_e64 v217, -|v209|\n\t"
    "s_xor_b32 s64, %[mx], 1024\n\t"
    "v_cvt_pk_bf16_f32 v32, v16, v20\n\t"
    "v_cvt_pk_bf16_f32 v33, v24, v28\n\t"
    "buffer_store_dwordx2 v[32:33], %[vout], s[56:59], s64 offen sc1\n\t"
    "v_exp_f32_e64 v218, -|v212|\n\t"
    "v_exp_f32_e64 v219, -|v213|\n\t"
    "v_cvt_pk_bf16_f32 v34, v17, v21\n\t"
    "v_cvt_pk_bf16_f32 v35, v25, v29\n\t"
    "buffer_store_dwordx2 v[34:35], %[vout], s[56:59], s64 offen offset:512 sc1\n\t"
    "v_exp_f32 v210, v210\n\t"
    "v_exp_f32 v211, v211\n\t"
    "v_cvt_pk_bf16_f32 v36, v18, v22\n\t"
    "v_cvt_pk_bf16_f32 v37, v26, v30\n\t"
    "s_xor_b32 s64, %[mx], 9216\n\t"
    "buffer_store_dwordx2 v[36:37], %[vout], s[56:59], s64 offen sc1\n\t"
    "v_exp_f32 v214, v214\n\t"
    "v_cvt_pk_bf16_f32 v38, v19, v23\n\t"
    "v_cvt_pk_bf16_f32 v39, v27, v31\n\t"
    "buffer_store_dwordx2 v[38:39], %[vout], s[56:59], s64 offen offset:512 sc1\n\t"
    "v_exp_f32 v215, v215\n\t"
    "s_waitcnt lgkmcnt(0)\n\t"
    "s_barrier\n\t"
    "ds_read_b128 v[80:83], %[vzr0] offset:4096\n\t"
    "v_pk_add_f32 v[220:221], v[216:217], 1.0 op_sel_hi:[1,0]\n\t"
    "v_pk_add_f32 v[222:223], v[218:219], 1.0 op_sel_hi:[1,0]\n\t"
    "v_pk_add_f32 v[216:217], v[216:217], 1.0 op_sel_hi:[1,0] neg_lo:[1,0] neg_hi:[1,0]\n\t"
    "ds_read_b128 v[84:87], %[vzr1] offset:4096\n\t"
    "v_pk_add_f32 v[218:219], v[218:219], 1.0 op_sel_hi:[1,0] neg_lo:[1,0] neg_hi:[1,0]\n\t"
    "v_pk_fma_f32 v[220:221], v[220:221], v[210:211], v[220:221]\n\t"
    "v_pk_fma_f32 v[222:223], v[222:223], v[214:215], v[222:223]\n\t"
    "ds_read_b128 v[88:91], %[vzr0] offset:20480\n\t"
    "v_rcp_f32 v220, v220\n\t"
    "v_rcp_f32 v221, v221\n\t"
    "ds_read_b128 v[92:95], %[vzr1] offset:20480\n\t"
    "v_rcp_f32 v222, v222\n\t"
    "v_rcp_f32 v223, v223\n\t"
    "v_pk_mul_f32 v[216:217], v[216:217], v[220:221]\n\t"
    "v_pk_mul_f32 v[218:219], v[218:219], v[222:223]\n\t"
    "ds_read_b128 v[96:99], %[vzr0] offset:36864\n\t"
    "v_bfi_b32 v216, s73, v216, v208\n\t"
    "v_bfi_b32 v217, s73, v217, v209\n\t"
    "v_bfi_b32 v218, s73, v218, v212\n\t"
    "ds_read_b128 v[100:103], %[vzr1] offset:36864\n\t"
    "v_bfi_b32 v219, s73, v219, v213\n\t"
    "v_cvt_pk_bf16_f32 v40, v216, v218\n\t"
    "v_cvt_pk_bf16_f32 v44, v217, v219\n\t"
    "v_accvgpr_read_b32 v224, a76\n\t"
    "ds_read_b128 v[104:107], %[vzr0] offset:53248\n\t"
    "v_accvgpr_read_b32 v225, a77\n\t"
    "v_accvgpr_read_b32 v226, a78\n\t"
    "v_accvgpr_read_b32 v227, a79\n\t"
    "v_accvgpr_read_b32 v228, a108\n\t"
    "ds_read_b128 v[108:111], %[vzr1] offset:53248\n\t"
    "v_accvgpr_read_b32 v229, a109\n\t"
    "v_accvgpr_read_b32 v230, a110\n\t"
    "v_accvgpr_read_b32 v231, a111\n\t"
    "v_permlane32_swap_b32 v224, v226\n\t"
    "v_permlane32_swap_b32 v225, v227\n\t"
    "v_permlane32_swap_b32 v228, v230\n\t"
    "v_permlane32_swap_b32 v229, v231\n\t"
    "v_pk_mul_f32 v[224:225], v[224:225], s[74:75]\n\t"
    "v_pk_mul_f32 v[226:227], v[226:227], s[76:77]\n\t"
    "v_pk_mul_f32 v[228:229], v[228:229], s[74:75]\n\t"
    "v_pk_mul_f32 v[230:231], v[230:231], s[76:77]\n\t"
    "s_waitcnt lgkmcnt(7)\n\t"
    "v_mfma_f32_16x16x32_bf16 v[16:19], v[80:83], v[48:51], 0\n\t"
    "v_exp_f32_e64 v232, -|v224|\n\t"
    "v_mfma_f32_16x16x32_bf16 v[20:23], v[80:83], v[52:55], 0\n\t"
    "v_exp_f32_e64 v233, -|v225|\n\t"
    "v_mfma_f32_16x16x32_bf16 v[24:27], v[80:83], v[56:59], 0\n\t"
    "v_exp_f32_e64 v234, -|v228|\n\t"
    "v_mfma_f32_16x16x32_bf16 v[28:31], v[80:83], v[60:63], 0\n\t"
    "v_exp_f32_e64 v235, -|v229|\n\t"
    "s_waitcnt lgkmcnt(5)\n\t"
    "v_mfma_f32_16x16x32_bf16 v[16:19], v[84:87], v[64:67], v[16:19]\n\t"
    "v_exp_f32 v226, v226\n\t"
    "v_mfma_f32_16x16x32_bf16 v[20:23], v[84:87], v[68:71], v[20:23]\n\t"
    "v_exp_f32 v227, v227\n\t"
    "v_exp_f32 v230, v230\n\t"
    "v_exp_f32 v231, v231\n\t"
    "v_pk_add_f32 v[236:237], v[232:233], 1.0 op_sel_hi:[1,0]\n\t"
    "v_pk_add_f32 v[238:239], v[234:235], 1.0 op_sel_hi:[1,0]\n\t"
    "v_pk_add_f32 v[232:233], v[232:233], 1.0 op_sel_hi:[1,0] neg_lo:[1,0] neg_hi:[1,0]\n\t"
    "v_pk_add_f32 v[234:235], v[234:235], 1.0 op_sel_hi:[1,0] neg_lo:[1,0] neg_hi:[1,0]\n\t"
    "v_pk_fma_f32 v[236:237], v[236:237], v[226:227], v[236:237]\n\t"
    "v_pk_fma_f32 v[238:239], v[238:239], v[230:231], v[238:239]\n\t"
    "v_rcp_f32 v236, v236\n\t"
    "v_rcp_f32 v237, v237\n\t"
    "v_rcp_f32 v238, v238\n\t"
    "v_rcp_f32 v239, v239\n\t"
    "v_pk_mul_f32 v[232:233], v[232:233], v[236:237]\n\t"
    "v_pk_mul_f32 v[234:235], v[234:235], v[238:239]\n\t"
    "v_mfma_f32_16x16x32_bf16 v[24:27], v[84:87], v[72:75], v[24:27]\n\t"
    "v_bfi_b32 v232, s73, v232, v224\n\t"
    "v_bfi_b32 v233, s73, v233, v225\n\t"
    "v_bfi_b32 v234, s73, v234, v228\n\t"
    "v_mfma_f32_16x16x32_bf16 v[28:31], v[84:87], v[76:79], v[28:31]\n\t"
    "v_bfi_b32 v235, s73, v235, v229\n\t"
    "v_cvt_pk_bf16_f32 v41, v232, v234\n\t"
    "v_cvt_pk_bf16_f32 v45, v233, v235\n\t"
    "s_waitcnt lgkmcnt(4)\n\t"
    "v_mfma_f32_16x16x32_bf16 v[16:19], v[88:91], v[112:115], v[16:19]\n\t"
    "v_accvgpr_read_b32 v208, a140\n\t"
    "v_accvgpr_read_b32 v209, a141\n\t"
    "v_mfma_f32_16x16x32_bf16 v[20:23], v[88:91], v[116:119], v[20:23]\n\t"
    "v_accvgpr_read_b32 v210, a142\n\t"
    "v_accvgpr_read_b32 v211, a143\n\t"
    "v_mfma_f32_16x16x32_bf16 v[24:27], v[88:91], v[120:123], v[24:27]\n\t"
    "v_accvgpr_read_b32 v212, a172\n\t"
    "v_accvgpr_read_b32 v213, a173\n\t"
    "v_accvgpr_read_b32 v214, a174\n\t"
    "v_accvgpr_read_b32 v215, a175\n\t"
    "v_permlane32_swap_b32 v208, v210\n\t"
    "v_permlane32_swap_b32 v209, v211\n\t"
    "v_permlane32_swap_b32 v212, v214\n\t"
    "v_permlane32_swap_b32 v213, v215\n\t"
    "v_pk_mul_f32 v[208:209], v[208:209], s[74:75]\n\t"
    "v_pk_mul_f32 v[210:211], v[210:211], s[76:77]\n\t"
    "v_pk_mul_f32 v[212:213], v[212:213], s[74:75]\n\t"
    "v_pk_mul_f32 v[214:215], v[214:215], s[76:77]\n\t"
    "v_mfma_f32_16x16x32_bf16 v[28:31], v[88:91], v[124:127], v[28:31]\n\t"
    "v_exp_f32_e64 v216, -|v208|\n\t"
    "s_waitcnt lgkmcnt(3)\n\t"
    "v_mfma_f32_16x16x32_bf16 v[16:19], v[92:95], v[128:131], v[16:19]\n\t"
    "v_exp_f32_e64 v217, -|v209|\n\t"
    "v_mfma_f32_16x16x32_bf16 v[20:23], v[92:95], v[132:135], v[20:23]\n\t"
    "v_exp_f32_e64 v218, -|v212|\n\t"
    "v_mfma_f32_16x16x32_bf16 v[24:27], v[92:95], v[136:139], v[24:27]\n\t"
    "v_exp_f32_e64 v219, -|v213|\n\t"
    "v_mfma_f32_16x16x32_bf16 v[28:31], v[92:95], v[140:143], v[28:31]\n\t"
    "v_exp_f32 v210, v210\n\t"
    "s_waitcnt lgkmcnt(2)\n\t"
    "v_mfma_f32_16x16x32_bf16 v[16:19], v[96:99], v[144:147], v[16:19]\n\t"
    "v_exp_f32 v211, v211\n\t"
    "v_exp_f32 v214, v214\n\t"
    "v_exp_f32 v215, v215\n\t"
    "v_pk_add_f32 v[220:221], v[216:217], 1.0 op_sel_hi:[1,0]\n\t"
    "v_pk_add_f32 v[222:223], v[218:219], 1.0 op_sel_hi:[1,0]\n\t"
    "v_pk_add_f32 v[216:217], v[216:217], 1.0 op_sel_hi:[1,0] neg_lo:[1,0] neg_hi:[1,0]\n\t"
    "v_pk_add_f32 v[218:219], v[218:219], 1.0 op_sel_hi:[1,0] neg_lo:[1,0] neg_hi:[1,0]\n\t"
    "v_pk_fma_f32 v[220:221], v[220:221], v[210:211], v[220:221]\n\t"
    "v_pk_fma_f32 v[222:223], v[222:223], v[214:215], v[222:223]\n\t"
    "v_rcp_f32 v220, v220\n\t"
    "v_rcp_f32 v221, v221\n\t"
    "v_rcp_f32 v222, v222\n\t"
    "v_rcp_f32 v223, v223\n\t"
    "v_pk_mul_f32 v[216:217], v[216:217], v[220:221]\n\t"
    "v_pk_mul_f32 v[218:219], v[218:219], v[222:223]\n\t"
    "v_mfma_f32_16x16x32_bf16 v[20:23], v[96:99], v[148:151], v[20:23]\n\t"
    "v_bfi_b32 v216, s73, v216, v208\n\t"
    "v_bfi_b32 v217, s73, v217, v209\n\t"
    "v_bfi_b32 v218, s73, v218, v212\n\t"
    "v_mfma_f32_16x16x32_bf16 v[24:27], v[96:99], v[152:155], v[24:27]\n\t"
    "v_bfi_b32 v219, s73, v219, v213\n\t"
    "v_cvt_pk_bf16_f32 v42, v216, v218\n\t"
    "v_cvt_pk_bf16_f32 v46, v217, v219\n\t"
    "v_mfma_f32_16x16x32_bf16 v[28:31], v[96:99], v[156:159], v[28:31]\n\t"
    "v_accvgpr_read_b32 v224, a204\n\t"
    "v_accvgpr_read_b32 v225, a205\n\t"
    "s_waitcnt lgkmcnt(1)\n\t"
    "v_mfma_f32_16x16x32_bf16 v[16:19], v[100:103], v[160:163], v[16:19]\n\t"
    "v_accvgpr_read_b32 v226, a206\n\t"
    "v_accvgpr_read_b32 v227, a207\n\t"
    "v_mfma_f32_16x16x32_bf16 v[20:23], v[100:103], v[164:167], v[20:23]\n\t"
    "v_accvgpr_read_b32 v228, a236\n\t"
    "v_accvgpr_read_b32 v229, a237\n\t"
    "v_accvgpr_read_b32 v230, a238\n\t"
    "v_accvgpr_read_b32 v231, a239\n\t"
    "v_permlane32_swap_b32 v224, v226\n\t"
    "v_permlane32_swap_b32 v225, v227\n\t"
    "v_permlane32_swap_b32 v228, v230\n\t"
    "v_permlane32_swap_b32 v229, v231\n\t"
    "v_pk_mul_f32 v[224:225], v[224:225], s[74:75]\n\t"
    "v_pk_mul_f32 v[226:227], v[226:227], s[76:77]\n\t"
    "v_pk_mul_f32 v[228:229], v[228:229], s[74:75]\n\t"
    "v_pk_mul_f32 v[230:231], v[230:231], s[76:77]\n\t"
    "v_mfma_f32_16x16x32_bf16 v[24:27], v[100:103], v[168:171], v[24:27]\n\t"
    "v_exp_f32_e64 v232, -|v224|\n\t"
    "v_mfma_f32_16x16x32_bf16 v[28:31], v[100:103], v[172:175], v[28:31]\n\t"
    "v_exp_f32_e64 v233, -|v225|\n\t"
    "s_waitcnt lgkmcnt(0)\n\t"
    "v_mfma_f32_16x16x32_bf16 v[16:19], v[104:107], v[176:179], v[16:19]\n\t"
    "v_exp_f32_e64 v234, -|v228|\n\t"
    "v_mfma_f32_16x16x32_bf16 v[20:23], v[104:107], v[180:183], v[20:23]\n\t"
    "v_exp_f32_e64 v235, -|v229|\n\t"
    "v_mfma_f32_16x16x32_bf16 v[24:27], v[104:107], v[184:187], v[24:27]\n\t"
    "v_exp_f32 v226, v226\n\t"
    "v_mfma_f32_16x16x32_bf16 v[28:31], v[104:107], v[188:191], v[28:31]\n\t"
    "v_exp_f32 v227, v227\n\t"
    "v_exp_f32 v230, v230\n\t"
    "v_exp_f32 v231, v231\n\t"
    "v_pk_add_f32 v[236:237], v[232:233], 1.0 op_sel_hi:[1,0]\n\t"
    "v_pk_add_f32 v[238:239], v[234:235], 1.0 op_sel_hi:[1,0]\n\t"
    "v_pk_add_f32 v[232:233], v[232:233], 1.0 op_sel_hi:[1,0] neg_lo:[1,0] neg_hi:[1,0]\n\t"
    "v_pk_add_f32 v[234:235], v[234:235], 1.0 op_sel_hi:[1,0] neg_lo:[1,0] neg_hi:[1,0]\n\t"
    "v_pk_fma_f32 v[236:237], v[236:237], v[226:227], v[236:237]\n\t"
    "v_pk_fma_f32 v[238:239], v[238:239], v[230:231], v[238:239]\n\t"
    "v_rcp_f32 v236, v236\n\t"
    "v_rcp_f32 v237, v237\n\t"
    "v_rcp_f32 v238, v238\n\t"
    "v_rcp_f32 v239, v239\n\t"
    "v_pk_mul_f32 v[232:233], v[232:233], v[236:237]\n\t"
    "v_pk_mul_f32 v[234:235], v[234:235], v[238:239]\n\t"
    "v_mfma_f32_16x16x32_bf16 v[16:19], v[108:111], v[192:195], v[16:19]\n\t"
    "v_bfi_b32 v232, s73, v232, v224\n\t"
    "v_bfi_b32 v233, s73, v233, v225\n\t"
    "v_bfi_b32 v234, s73, v234, v228\n\t"
    "v_bfi_b32 v235, s73, v235, v229\n\t"
    "v_cvt_pk_bf16_f32 v43, v232, v234\n\t"
    "v_cvt_pk_bf16_f32 v47, v233, v235\n\t"
    "ds_write_b128 %[vzw0], v[40:43] offset:6144\n\t"
    "ds_write_b128 %[vzw1], v[44:47] offset:6144\n\t"
    "v_mfma_f32_16x16x32_bf16 v[20:23], v[108:111], v[196:199], v[20:23]\n\t"
    "v_accvgpr_read_b32 v208, a16\n\t"
    "v_accvgpr_read_b32 v209, a17\n\t"
    "v_mfma_f32_16x16x32_bf16 v[24:27], v[108:111], v[200:203], v[24:27]\n\t"
    "v_accvgpr_read_b32 v210, a18\n\t"
    "v_accvgpr_read_b32 v211, a19\n\t"
    "v_mfma_f32_16x16x32_bf16 v[28:31], v[108:111], v[204:207], v[28:31]\n\t"
    "v_accvgpr_read_b32 v212, a48\n\t"
    "v_accvgpr_read_b32 v213, a49\n\t"
    "v_accvgpr_read_b32 v214, a50\n\t"
    "v_accvgpr_read_b32 v215, a51\n\t"
    "v_permlane32_swap_b32 v208, v210\n\t"
    "v_permlane32_swap_b32 v209, v211\n\t"
    "v_permlane32_swap_b32 v212, v214\n\t"
    "v_permlane32_swap_b32 v213, v215\n\t"
    "v_pk_mul_f32 v[208:209], v[208:209], s[74:75]\n\t"
    "v_pk_mul_f32 v[210:211], v[210:211], s[76:77]\n\t"
    "v_pk_mul_f32 v[212:213], v[212:213], s[74:75]\n\t"
    "v_pk_mul_f32 v[214:215], v[214:215], s[76:77]\n\t"
    "v_exp_f32_e64 v216, -|v208|\n\t"
    "v_exp_f32_e64 v217, -|v209|\n\t"
    "s_xor_b32 s64, %[mx], 2048\n\t"
    "v_cvt_pk_bf16_f32 v32, v16, v20\n\t"
    "v_cvt_pk_bf16_f32 v33, v24, v28\n\t"
    "buffer_store_dwordx2 v[32:33], %[vout], s[56:59], s64 offen sc1\n\t"
    "v_exp_f32_e64 v218, -|v212|\n\t"
    "v_exp_f32_e64 v219, -|v213|\n\t"
    "v_cvt_pk_bf16_f32 v34, v17, v21\n\t"
    "v_cvt_pk_bf16_f32 v35, v25, v29\n\t"
    "buffer_store_dwordx2 v[34:35], %[vout], s[56:59], s64 offen offset:512 sc1\n\t"
    "v_exp_f32 v210, v210\n\t"
    "v_exp_f32 v211, v211\n\t"
    "v_cvt_pk_bf16_f32 v36, v18, v22\n\t"
    "v_cvt_pk_bf16_f32 v37, v26, v30\n\t"
    "s_xor_b32 s64, %[mx], 10240\n\t"
    "buffer_store_dwordx2 v[36:37], %[vout], s[56:59], s64 offen sc1\n\t"
    "v_exp_f32 v214, v214\n\t"
    "v_cvt_pk_bf16_f32 v38, v19, v23\n\t"
    "v_cvt_pk_bf16_f32 v39, v27, v31\n\t"
    "buffer_store_dwordx2 v[38:39], %[vout], s[56:59], s64 offen offset:512 sc1\n\t"
    "v_exp_f32 v215, v215\n\t"
    "s_waitcnt lgkmcnt(0)\n\t"
    "s_barrier\n\t"
    "ds_read_b128 v[80:83], %[vzr0] offset:6144\n\t"
    "v_pk_add_f32 v[220:221], v[216:217], 1.0 op_sel_hi:[1,0]\n\t"
    "v_pk_add_f32 v[222:223], v[218:219], 1.0 op_sel_hi:[1,0]\n\t"
    "v_pk_add_f32 v[216:217], v[216:217], 1.0 op_sel_hi:[1,0] neg_lo:[1,0] neg_hi:[1,0]\n\t"
    "ds_read_b128 v[84:87], %[vzr1] offset:6144\n\t"
    "v_pk_add_f32 v[218:219], v[218:219], 1.0 op_sel_hi:[1,0] neg_lo:[1,0] neg_hi:[1,0]\n\t"
    "v_pk_fma_f32 v[220:221], v[220:221], v[210:211], v[220:221]\n\t"
    "v_pk_fma_f32 v[222:223], v[222:223], v[214:215], v[222:223]\n\t"
    "ds_read_b128 v[88:91], %[vzr0] offset:22528\n\t"
    "v_rcp_f32 v220, v220\n\t"
    "v_rcp_f32 v221, v221\n\t"
    "ds_read_b128 v[92:95], %[vzr1] offset:22528\n\t"
    "v_rcp_f32 v222, v222\n\t"
    "v_rcp_f32 v223, v223\n\t"
    "v_pk_mul_f32 v[216:217], v[216:217], v[220:221]\n\t"
    "v_pk_mul_f32 v[218:219], v[218:219], v[222:223]\n\t"
    "ds_read_b128 v[96:99], %[vzr0] offset:38912\n\t"
    "v_bfi_b32 v216, s73, v216, v208\n\t"
    "v_bfi_b32 v217, s73, v217, v209\n\t"
    "v_bfi_b32 v218, s73, v218, v212\n\t"
    "ds_read_b128 v[100:103], %[vzr1] offset:38912\n\t"
    "v_bfi_b32 v219, s73, v219, v213\n\t"
    "v_cvt_pk_bf16_f32 v40, v216, v218\n\t"
    "v_cvt_pk_bf16_f32 v44, v217, v219\n\t"
    "v_accvgpr_read_b32 v224, a80\n\t"
    "ds_read_b128 v[104:107], %[vzr0] offset:55296\n\t"
    "v_accvgpr_read_b32 v225, a81\n\t"
    "v_accvgpr_read_b32 v226, a82\n\t"
    "v_accvgpr_read_b32 v227, a83\n\t"
    "v_accvgpr_read_b32 v228, a112\n\t"
    "ds_read_b128 v[108:111], %[vzr1] offset:55296\n\t"
    "v_accvgpr_read_b32 v229, a113\n\t"
    "v_accvgpr_read_b32 v230, a114\n\t"
    "v_accvgpr_read_b32 v231, a115\n\t"
    "v_permlane32_swap_b32 v224, v226\n\t"
    "v_permlane32_swap_b32 v225, v227\n\t"
    "v_permlane32_swap_b32 v228, v230\n\t"
    "v_permlane32_swap_b32 v229, v231\n\t"
    "v_pk_mul_f32 v[224:225], v[224:225], s[74:75]\n\t"
    "v_pk_mul_f32 v[226:227], v[226:227], s[76:77]\n\t"
    "v_pk_mul_f32 v[228:229], v[228:229], s[74:75]\n\t"
    "v_pk_mul_f32 v[230:231], v[230:231], s[76:77]\n\t"
    "s_waitcnt lgkmcnt(7)\n\t"
    "v_mfma_f32_16x16x32_bf16 v[16:19], v[80:83], v[48:51], 0\n\t"
    "v_exp_f32_e64 v232, -|v224|\n\t"
    "v_mfma_f32_16x16x32_bf16 v[20:23], v[80:83], v[52:55], 0\n\t"
    "v_exp_f32_e64 v233, -|v225|\n\t"
    "v_mfma_f32_16x16x32_bf16 v[24:27], v[80:83], v[56:59], 0\n\t"
    "v_exp_f32_e64 v234, -|v228|\n\t"
    "v_mfma_f32_16x16x32_bf16 v[28:31], v[80:83], v[60:63], 0\n\t"
    "v_exp_f32_e64 v235, -|v229|\n\t"
    "s_waitcnt lgkmcnt(5)\n\t"
    "v_mfma_f32_16x16x32_bf16 v[16:19], v[84:87], v[64:67], v[16:19]\n\t"
    "v_exp_f32 v226, v226\n\t"
    "v_mfma_f32_16x16x32_bf16 v[20:23], v[84:87], v[68:71], v[20:23]\n\t"
    "v_exp_f32 v227, v227\n\t"
    "v_exp_f32 v230, v230\n\t"
    "v_exp_f32 v231, v231\n\t"
    "v_pk_add_f32 v[236:237], v[232:233], 1.0 op_sel_hi:[1,0]\n\t"
    "v_pk_add_f32 v[238:239], v[234:235], 1.0 op_sel_hi:[1,0]\n\t"
    "v_pk_add_f32 v[232:233], v[232:233], 1.0 op_sel_hi:[1,0] neg_lo:[1,0] neg_hi:[1,0]\n\t"
    "v_pk_add_f32 v[234:235], v[234:235], 1.0 op_sel_hi:[1,0] neg_lo:[1,0] neg_hi:[1,0]\n\t"
    "v_pk_fma_f32 v[236:237], v[236:237], v[226:227], v[236:237]\n\t"
    "v_pk_fma_f32 v[238:239], v[238:239], v[230:231], v[238:239]\n\t"
    "v_rcp_f32 v236, v236\n\t"
    "v_rcp_f32 v237, v237\n\t"
    "v_rcp_f32 v238, v238\n\t"
    "v_rcp_f32 v239, v239\n\t"
    "v_pk_mul_f32 v[232:233], v[232:233], v[236:237]\n\t"
    "v_pk_mul_f32 v[234:235], v[234:235], v[238:239]\n\t"
    "v_mfma_f32_16x16x32_bf16 v[24:27], v[84:87], v[72:75], v[24:27]\n\t"
    "v_bfi_b32 v232, s73, v232, v224\n\t"
    "v_bfi_b32 v233, s73, v233, v225\n\t"
    "v_bfi_b32 v234, s73, v234, v228\n\t"
    "v_mfma_f32_16x16x32_bf16 v[28:31], v[84:87], v[76:79], v[28:31]\n\t"
    "v_bfi_b32 v235, s73, v235, v229\n\t"
    "v_cvt_pk_bf16_f32 v41, v232, v234\n\t"
    "v_cvt_pk_bf16_f32 v45, v233, v235\n\t"
    "s_waitcnt lgkmcnt(4)\n\t"
    "v_mfma_f32_16x16x32_bf16 v[16:19], v[88:91], v[112:115], v[16:19]\n\t"
    "v_accvgpr_read_b32 v208, a144\n\t"
    "v_accvgpr_read_b32 v209, a145\n\t"
    "v_mfma_f32_16x16x32_bf16 v[20:23], v[88:91], v[116:119], v[20:23]\n\t"
    "v_accvgpr_read_b32 v210, a146\n\t"
    "v_accvgpr_read_b32 v211, a147\n\t"
    "v_mfma_f32_16x16x32_bf16 v[24:27], v[88:91], v[120:123], v[24:27]\n\t"
    "v_accvgpr_read_b32 v212, a176\n\t"
    "v_accvgpr_read_b32 v213, a177\n\t"
    "v_accvgpr_read_b32 v214, a178\n\t"
    "v_accvgpr_read_b32 v215, a179\n\t"
    "v_permlane32_swap_b32 v208, v210\n\t"
    "v_permlane32_swap_b32 v209, v211\n\t"
    "v_permlane32_swap_b32 v212, v214\n\t"
    "v_permlane32_swap_b32 v213, v215\n\t"
    "v_pk_mul_f32 v[208:209], v[208:209], s[74:75]\n\t"
    "v_pk_mul_f32 v[210:211], v[210:211], s[76:77]\n\t"
    "v_pk_mul_f32 v[212:213], v[212:213], s[74:75]\n\t"
    "v_pk_mul_f32 v[214:215], v[214:215], s[76:77]\n\t"
    "v_mfma_f32_16x16x32_bf16 v[28:31], v[88:91], v[124:127], v[28:31]\n\t"
    "v_exp_f32_e64 v216, -|v208|\n\t"
    "s_waitcnt lgkmcnt(3)\n\t"
    "v_mfma_f32_16x16x32_bf16 v[16:19], v[92:95], v[128:131], v[16:19]\n\t"
    "v_exp_f32_e64 v217, -|v209|\n\t"
    "v_mfma_f32_16x16x32_bf16 v[20:23], v[92:95], v[132:135], v[20:23]\n\t"
    "v_exp_f32_e64 v218, -|v212|\n\t"
    "v_mfma_f32_16x16x32_bf16 v[24:27], v[92:95], v[136:139], v[24:27]\n\t"
    "v_exp_f32_e64 v219, -|v213|\n\t"
    "v_mfma_f32_16x16x32_bf16 v[28:31], v[92:95], v[140:143], v[28:31]\n\t"
    "v_exp_f32 v210, v210\n\t"
    "s_waitcnt lgkmcnt(2)\n\t"
    "v_mfma_f32_16x16x32_bf16 v[16:19], v[96:99], v[144:147], v[16:19]\n\t"
    "v_exp_f32 v211, v211\n\t"
    "v_exp_f32 v214, v214\n\t"
    "v_exp_f32 v215, v215\n\t"
    "v_pk_add_f32 v[220:221], v[216:217], 1.0 op_sel_hi:[1,0]\n\t"
    "v_pk_add_f32 v[222:223], v[218:219], 1.0 op_sel_hi:[1,0]\n\t"
    "v_pk_add_f32 v[216:217], v[216:217], 1.0 op_sel_hi:[1,0] neg_lo:[1,0] neg_hi:[1,0]\n\t"
    "v_pk_add_f32 v[218:219], v[218:219], 1.0 op_sel_hi:[1,0] neg_lo:[1,0] neg_hi:[1,0]\n\t"
    "v_pk_fma_f32 v[220:221], v[220:221], v[210:211], v[220:221]\n\t"
    "v_pk_fma_f32 v[222:223], v[222:223], v[214:215], v[222:223]\n\t"
    "v_rcp_f32 v220, v220\n\t"
    "v_rcp_f32 v221, v221\n\t"
    "v_rcp_f32 v222, v222\n\t"
    "v_rcp_f32 v223, v223\n\t"
    "v_pk_mul_f32 v[216:217], v[216:217], v[220:221]\n\t"
    "v_pk_mul_f32 v[218:219], v[218:219], v[222:223]\n\t"
    "v_mfma_f32_16x16x32_bf16 v[20:23], v[96:99], v[148:151], v[20:23]\n\t"
    "v_bfi_b32 v216, s73, v216, v208\n\t"
    "v_bfi_b32 v217, s73, v217, v209\n\t"
    "v_bfi_b32 v218, s73, v218, v212\n\t"
    "v_mfma_f32_16x16x32_bf16 v[24:27], v[96:99], v[152:155], v[24:27]\n\t"
    "v_bfi_b32 v219, s73, v219, v213\n\t"
    "v_cvt_pk_bf16_f32 v42, v216, v218\n\t"
    "v_cvt_pk_bf16_f32 v46, v217, v219\n\t"
    "v_mfma_f32_16x16x32_bf16 v[28:31], v[96:99], v[156:159], v[28:31]\n\t"
    "v_accvgpr_read_b32 v224, a208\n\t"
    "v_accvgpr_read_b32 v225, a209\n\t"
    "s_waitcnt lgkmcnt(1)\n\t"
    "v_mfma_f32_16x16x32_bf16 v[16:19], v[100:103], v[160:163], v[16:19]\n\t"
    "v_accvgpr_read_b32 v226, a210\n\t"
    "v_accvgpr_read_b32 v227, a211\n\t"
    "v_mfma_f32_16x16x32_bf16 v[20:23], v[100:103], v[164:167], v[20:23]\n\t"
    "v_accvgpr_read_b32 v228, a240\n\t"
    "v_accvgpr_read_b32 v229, a241\n\t"
    "v_accvgpr_read_b32 v230, a242\n\t"
    "v_accvgpr_read_b32 v231, a243\n\t"
    "v_permlane32_swap_b32 v224, v226\n\t"
    "v_permlane32_swap_b32 v225, v227\n\t"
    "v_permlane32_swap_b32 v228, v230\n\t"
    "v_permlane32_swap_b32 v229, v231\n\t"
    "v_pk_mul_f32 v[224:225], v[224:225], s[74:75]\n\t"
    "v_pk_mul_f32 v[226:227], v[226:227], s[76:77]\n\t"
    "v_pk_mul_f32 v[228:229], v[228:229], s[74:75]\n\t"
    "v_pk_mul_f32 v[230:231], v[230:231], s[76:77]\n\t"
    "v_mfma_f32_16x16x32_bf16 v[24:27], v[100:103], v[168:171], v[24:27]\n\t"
    "v_exp_f32_e64 v232, -|v224|\n\t"
    "v_mfma_f32_16x16x32_bf16 v[28:31], v[100:103], v[172:175], v[28:31]\n\t"
    "v_exp_f32_e64 v233, -|v225|\n\t"
    "s_waitcnt lgkmcnt(0)\n\t"
    "v_mfma_f32_16x16x32_bf16 v[16:19], v[104:107], v[176:179], v[16:19]\n\t"
    "v_exp_f32_e64 v234, -|v228|\n\t"
    "v_mfma_f32_16x16x32_bf16 v[20:23], v[104:107], v[180:183], v[20:23]\n\t"
    "v_exp_f32_e64 v235, -|v229|\n\t"
    "v_mfma_f32_16x16x32_bf16 v[24:27], v[104:107], v[184:187], v[24:27]\n\t"
    "v_exp_f32 v226, v226\n\t"
    "v_mfma_f32_16x16x32_bf16 v[28:31], v[104:107], v[188:191], v[28:31]\n\t"
    "v_exp_f32 v227, v227\n\t"
    "v_exp_f32 v230, v230\n\t"
    "v_exp_f32 v231, v231\n\t"
    "v_pk_add_f32 v[236:237], v[232:233], 1.0 op_sel_hi:[1,0]\n\t"
    "v_pk_add_f32 v[238:239], v[234:235], 1.0 op_sel_hi:[1,0]\n\t"
    "v_pk_add_f32 v[232:233], v[232:233], 1.0 op_sel_hi:[1,0] neg_lo:[1,0] neg_hi:[1,0]\n\t"
    "v_pk_add_f32 v[234:235], v[234:235], 1.0 op_sel_hi:[1,0] neg_lo:[1,0] neg_hi:[1,0]\n\t"
    "v_pk_fma_f32 v[236:237], v[236:237], v[226:227], v[236:237]\n\t"
    "v_pk_fma_f32 v[238:239], v[238:239], v[230:231], v[238:239]\n\t"
    "v_rcp_f32 v236, v236\n\t"
    "v_rcp_f32 v237, v237\n\t"
    "v_rcp_f32 v238, v238\n\t"
    "v_rcp_f32 v239, v239\n\t"
    "v_pk_mul_f32 v[232:233], v[232:233], v[236:237]\n\t"
    "v_pk_mul_f32 v[234:235], v[234:235], v[238:239]\n\t"
    "v_mfma_f32_16x16x32_bf16 v[16:19], v[108:111], v[192:195], v[16:19]\n\t"
    "v_bfi_b32 v232, s73, v232, v224\n\t"
    "v_bfi_b32 v233, s73, v233, v225\n\t"
    "v_bfi_b32 v234, s73, v234, v228\n\t"
    "v_bfi_b32 v235, s73, v235, v229\n\t"
    "v_cvt_pk_bf16_f32 v43, v232, v234\n\t"
    "v_cvt_pk_bf16_f32 v47, v233, v235\n\t"
    "ds_write_b128 %[vzw0], v[40:43] offset:8192\n\t"
    "ds_write_b128 %[vzw1], v[44:47] offset:8192\n\t"
    "v_mfma_f32_16x16x32_bf16 v[20:23], v[108:111], v[196:199], v[20:23]\n\t"
    "v_accvgpr_read_b32 v208, a20\n\t"
    "v_accvgpr_read_b32 v209, a21\n\t"
    "v_mfma_f32_16x16x32_bf16 v[24:27], v[108:111], v[200:203], v[24:27]\n\t"
    "v_accvgpr_read_b32 v210, a22\n\t"
    "v_accvgpr_read_b32 v211, a23\n\t"
    "v_mfma_f32_16x16x32_bf16 v[28:31], v[108:111], v[204:207], v[28:31]\n\t"
    "v_accvgpr_read_b32 v212, a52\n\t"
    "v_accvgpr_read_b32 v213, a53\n\t"
    "v_accvgpr_read_b32 v214, a54\n\t"
    "v_accvgpr_read_b32 v215, a55\n\t"
    "v_permlane32_swap_b32 v208, v210\n\t"
    "v_permlane32_swap_b32 v209, v211\n\t"
    "v_permlane32_swap_b32 v212, v214\n\t"
    "v_permlane32_swap_b32 v213, v215\n\t"
    "v_pk_mul_f32 v[208:209], v[208:209], s[74:75]\n\t"
    "v_pk_mul_f32 v[210:211], v[210:211], s[76:77]\n\t"
    "v_pk_mul_f32 v[212:213], v[212:213], s[74:75]\n\t"
    "v_pk_mul_f32 v[214:215], v[214:215], s[76:77]\n\t"
    "v_exp_f32_e64 v216, -|v208|\n\t"
    "v_exp_f32_e64 v217, -|v209|\n\t"
    "s_xor_b32 s64, %[mx], 3072\n\t"
    "v_cvt_pk_bf16_f32 v32, v16, v20\n\t"
    "v_cvt_pk_bf16_f32 v33, v24, v28\n\t"
    "buffer_store_dwordx2 v[32:33], %[vout], s[56:59], s64 offen sc1\n\t"
    "v_exp_f32_e64 v218, -|v212|\n\t"
    "v_exp_f32_e64 v219, -|v213|\n\t"
    "v_cvt_pk_bf16_f32 v34, v17, v21\n\t"
    "v_cvt_pk_bf16_f32 v35, v25, v29\n\t"
    "buffer_store_dwordx2 v[34:35], %[vout], s[56:59], s64 offen offset:512 sc1\n\t"
    "v_exp_f32 v210, v210\n\t"
    "v_exp_f32 v211, v211\n\t"
    "v_cvt_pk_bf16_f32 v36, v18, v22\n\t"
    "v_cvt_pk_bf16_f32 v37, v26, v30\n\t"
    "s_xor_b32 s64, %[mx], 11264\n\t"
    "buffer_store_dwordx2 v[36:37], %[vout], s[56:59], s64 offen sc1\n\t"
    "v_exp_f32 v214, v214\n\t"
    "v_cvt_pk_bf16_f32 v38, v19, v23\n\t"
    "v_cvt_pk_bf16_f32 v39, v27, v31\n\t"
    "buffer_store_dwordx2 v[38:39], %[vout], s[56:59], s64 offen offset:512 sc1\n\t"
    "v_exp_f32 v215, v215\n\t"
    "s_waitcnt lgkmcnt(0)\n\t"
    "s_barrier\n\t"
    "ds_read_b128 v[80:83], %[vzr0] offset:8192\n\t"
    "v_pk_add_f32 v[220:221], v[216:217], 1.0 op_sel_hi:[1,0]\n\t"
    "v_pk_add_f32 v[222:223], v[218:219], 1.0 op_sel_hi:[1,0]\n\t"
    "v_pk_add_f32 v[216:217], v[216:217], 1.0 op_sel_hi:[1,0] neg_lo:[1,0] neg_hi:[1,0]\n\t"
    "ds_read_b128 v[84:87], %[vzr1] offset:8192\n\t"
    "v_pk_add_f32 v[218:219], v[218:219], 1.0 op_sel_hi:[1,0] neg_lo:[1,0] neg_hi:[1,0]\n\t"
    "v_pk_fma_f32 v[220:221], v[220:221], v[210:211], v[220:221]\n\t"
    "v_pk_fma_f32 v[222:223], v[222:223], v[214:215], v[222:223]\n\t"
    "ds_read_b128 v[88:91], %[vzr0] offset:24576\n\t"
    "v_rcp_f32 v220, v220\n\t"
    "v_rcp_f32 v221, v221\n\t"
    "ds_read_b128 v[92:95], %[vzr1] offset:24576\n\t"
    "v_rcp_f32 v222, v222\n\t"
    "v_rcp_f32 v223, v223\n\t"
    "v_pk_mul_f32 v[216:217], v[216:217], v[220:221]\n\t"
    "v_pk_mul_f32 v[218:219], v[218:219], v[222:223]\n\t"
    "ds_read_b128 v[96:99], %[vzr0] offset:40960\n\t"
    "v_bfi_b32 v216, s73, v216, v208\n\t"
    "v_bfi_b32 v217, s73, v217, v209\n\t"
    "v_bfi_b32 v218, s73, v218, v212\n\t"
    "ds_read_b128 v[100:103], %[vzr1] offset:40960\n\t"
    "v_bfi_b32 v219, s73, v219, v213\n\t"
    "v_cvt_pk_bf16_f32 v40, v216, v218\n\t"
    "v_cvt_pk_bf16_f32 v44, v217, v219\n\t"
    "v_accvgpr_read_b32 v224, a84\n\t"
    "ds_read_b128 v[104:107], %[vzr0] offset:57344\n\t"
    "v_accvgpr_read_b32 v225, a85\n\t"
    "v_accvgpr_read_b32 v226, a86\n\t"
    "v_accvgpr_read_b32 v227, a87\n\t"
    "v_accvgpr_read_b32 v228, a116\n\t"
    "ds_read_b128 v[108:111], %[vzr1] offset:57344\n\t"
    "v_accvgpr_read_b32 v229, a117\n\t"
    "v_accvgpr_read_b32 v230, a118\n\t"
    "v_accvgpr_read_b32 v231, a119\n\t"
    "v_permlane32_swap_b32 v224, v226\n\t"
    "v_permlane32_swap_b32 v225, v227\n\t"
    "v_permlane32_swap_b32 v228, v230\n\t"
    "v_permlane32_swap_b32 v229, v231\n\t"
    "v_pk_mul_f32 v[224:225], v[224:225], s[74:75]\n\t"
    "v_pk_mul_f32 v[226:227], v[226:227], s[76:77]\n\t"
    "v_pk_mul_f32 v[228:229], v[228:229], s[74:75]\n\t"
    "v_pk_mul_f32 v[230:231], v[230:231], s[76:77]\n\t"
    "s_waitcnt lgkmcnt(7)\n\t"
    "v_mfma_f32_16x16x32_bf16 v[16:19], v[80:83], v[48:51], 0\n\t"
    "v_exp_f32_e64 v232, -|v224|\n\t"
    "v_mfma_f32_16x16x32_bf16 v[20:23], v[80:83], v[52:55], 0\n\t"
    "v_exp_f32_e64 v233, -|v225|\n\t"
    "v_mfma_f32_16x16x32_bf16 v[24:27], v[80:83], v[56:59], 0\n\t"
    "v_exp_f32_e64 v234, -|v228|\n\t"
    "v_mfma_f32_16x16x32_bf16 v[28:31], v[80:83], v[60:63], 0\n\t"
    "v_exp_f32_e64 v235, -|v229|\n\t"
    "s_waitcnt lgkmcnt(5)\n\t"
    "v_mfma_f32_16x16x32_bf16 v[16:19], v[84:87], v[64:67], v[16:19]\n\t"
    "v_exp_f32 v226, v226\n\t"
    "v_mfma_f32_16x16x32_bf16 v[20:23], v[84:87], v[68:71], v[20:23]\n\t"
    "v_exp_f32 v227, v227\n\t"
    "v_exp_f32 v230, v230\n\t"
    "v_exp_f32 v231, v231\n\t"
    "v_pk_add_f32 v[236:237], v[232:233], 1.0 op_sel_hi:[1,0]\n\t"
    "v_pk_add_f32 v[238:239], v[234:235], 1.0 op_sel_hi:[1,0]\n\t"
    "v_pk_add_f32 v[232:233], v[232:233], 1.0 op_sel_hi:[1,0] neg_lo:[1,0] neg_hi:[1,0]\n\t"
    "v_pk_add_f32 v[234:235], v[234:235], 1.0 op_sel_hi:[1,0] neg_lo:[1,0] neg_hi:[1,0]\n\t"
    "v_pk_fma_f32 v[236:237], v[236:237], v[226:227], v[236:237]\n\t"
    "v_pk_fma_f32 v[238:239], v[238:239], v[230:231], v[238:239]\n\t"
    "v_rcp_f32 v236, v236\n\t"
    "v_rcp_f32 v237, v237\n\t"
    "v_rcp_f32 v238, v238\n\t"
    "v_rcp_f32 v239, v239\n\t"
    "v_pk_mul_f32 v[232:233], v[232:233], v[236:237]\n\t"
    "v_pk_mul_f32 v[234:235], v[234:235], v[238:239]\n\t"
    "v_mfma_f32_16x16x32_bf16 v[24:27], v[84:87], v[72:75], v[24:27]\n\t"
    "v_bfi_b32 v232, s73, v232, v224\n\t"
    "v_bfi_b32 v233, s73, v233, v225\n\t"
    "v_bfi_b32 v234, s73, v234, v228\n\t"
    "v_mfma_f32_16x16x32_bf16 v[28:31], v[84:87], v[76:79], v[28:31]\n\t"
    "v_bfi_b32 v235, s73, v235, v229\n\t"
    "v_cvt_pk_bf16_f32 v41, v232, v234\n\t"
    "v_cvt_pk_bf16_f32 v45, v233, v235\n\t"
    "s_waitcnt lgkmcnt(4)\n\t"
    "v_mfma_f32_16x16x32_bf16 v[16:19], v[88:91], v[112:115], v[16:19]\n\t"
    "v_accvgpr_read_b32 v208, a148\n\t"
    "v_accvgpr_read_b32 v209, a149\n\t"
    "v_mfma_f32_16x16x32_bf16 v[20:23], v[88:91], v[116:119], v[20:23]\n\t"
    "v_accvgpr_read_b32 v210, a150\n\t"
    "v_accvgpr_read_b32 v211, a151\n\t"
    "v_mfma_f32_16x16x32_bf16 v[24:27], v[88:91], v[120:123], v[24:27]\n\t"
    "v_accvgpr_read_b32 v212, a180\n\t"
    "v_accvgpr_read_b32 v213, a181\n\t"
    "v_accvgpr_read_b32 v214, a182\n\t"
    "v_accvgpr_read_b32 v215, a183\n\t"
    "v_permlane32_swap_b32 v208, v210\n\t"
    "v_permlane32_swap_b32 v209, v211\n\t"
    "v_permlane32_swap_b32 v212, v214\n\t"
    "v_permlane32_swap_b32 v213, v215\n\t"
    "v_pk_mul_f32 v[208:209], v[208:209], s[74:75]\n\t"
    "v_pk_mul_f32 v[210:211], v[210:211], s[76:77]\n\t"
    "v_pk_mul_f32 v[212:213], v[212:213], s[74:75]\n\t"
    "v_pk_mul_f32 v[214:215], v[214:215], s[76:77]\n\t"
    "v_mfma_f32_16x16x32_bf16 v[28:31], v[88:91], v[124:127], v[28:31]\n\t"
    "v_exp_f32_e64 v216, -|v208|\n\t"
    "s_waitcnt lgkmcnt(3)\n\t"
    "v_mfma_f32_16x16x32_bf16 v[16:19], v[92:95], v[128:131], v[16:19]\n\t"
    "v_exp_f32_e64 v217, -|v209|\n\t"
    "v_mfma_f32_16x16x32_bf16 v[20:23], v[92:95], v[132:135], v[20:23]\n\t"
    "v_exp_f32_e64 v218, -|v212|\n\t"
    "v_mfma_f32_16x16x32_bf16 v[24:27], v[92:95], v[136:139], v[24:27]\n\t"
    "v_exp_f32_e64 v219, -|v213|\n\t"
    "v_mfma_f32_16x16x32_bf16 v[28:31], v[92:95], v[140:143], v[28:31]\n\t"
    "v_exp_f32 v210, v210\n\t"
    "s_waitcnt lgkmcnt(2)\n\t"
    "v_mfma_f32_16x16x32_bf16 v[16:19], v[96:99], v[144:147], v[16:19]\n\t"
    "v_exp_f32 v211, v211\n\t"
    "v_exp_f32 v214, v214\n\t"
    "v_exp_f32 v215, v215\n\t"
    "v_pk_add_f32 v[220:221], v[216:217], 1.0 op_sel_hi:[1,0]\n\t"
    "v_pk_add_f32 v[222:223], v[218:219], 1.0 op_sel_hi:[1,0]\n\t"
    "v_pk_add_f32 v[216:217], v[216:217], 1.0 op_sel_hi:[1,0] neg_lo:[1,0] neg_hi:[1,0]\n\t"
    "v_pk_add_f32 v[218:219], v[218:219], 1.0 op_sel_hi:[1,0] neg_lo:[1,0] neg_hi:[1,0]\n\t"
    "v_pk_fma_f32 v[220:221], v[220:221], v[210:211], v[220:221]\n\t"
    "v_pk_fma_f32 v[222:223], v[222:223], v[214:215], v[222:223]\n\t"
    "v_rcp_f32 v220, v220\n\t"
    "v_rcp_f32 v221, v221\n\t"
    "v_rcp_f32 v222, v222\n\t"
    "v_rcp_f32 v223, v223\n\t"
    "v_pk_mul_f32 v[216:217], v[216:217], v[220:221]\n\t"
    "v_pk_mul_f32 v[218:219], v[218:219], v[222:223]\n\t"
    "v_mfma_f32_16x16x32_bf16 v[20:23], v[96:99], v[148:151], v[20:23]\n\t"
    "v_bfi_b32 v216, s73, v216, v208\n\t"
    "v_bfi_b32 v217, s73, v217, v209\n\t"
    "v_bfi_b32 v218, s73, v218, v212\n\t"
    "v_mfma_f32_16x16x32_bf16 v[24:27], v[96:99], v[152:155], v[24:27]\n\t"
    "v_bfi_b32 v219, s73, v219, v213\n\t"
    "v_cvt_pk_bf16_f32 v42, v216, v218\n\t"
    "v_cvt_pk_bf16_f32 v46, v217, v219\n\t"
    "v_mfma_f32_16x16x32_bf16 v[28:31], v[96:99], v[156:159], v[28:31]\n\t"
    "v_accvgpr_read_b32 v224, a212\n\t"
    "v_accvgpr_read_b32 v225, a213\n\t"
    "s_waitcnt lgkmcnt(1)\n\t"
    "v_mfma_f32_16x16x32_bf16 v[16:19], v[100:103], v[160:163], v[16:19]\n\t"
    "v_accvgpr_read_b32 v226, a214\n\t"
    "v_accvgpr_read_b32 v227, a215\n\t"
    "v_mfma_f32_16x16x32_bf16 v[20:23], v[100:103], v[164:167], v[20:23]\n\t"
    "v_accvgpr_read_b32 v228, a244\n\t"
    "v_accvgpr_read_b32 v229, a245\n\t"
    "v_accvgpr_read_b32 v230, a246\n\t"
    "v_accvgpr_read_b32 v231, a247\n\t"
    "v_permlane32_swap_b32 v224, v226\n\t"
    "v_permlane32_swap_b32 v225, v227\n\t"
    "v_permlane32_swap_b32 v228, v230\n\t"
    "v_permlane32_swap_b32 v229, v231\n\t"
    "v_pk_mul_f32 v[224:225], v[224:225], s[74:75]\n\t"
    "v_pk_mul_f32 v[226:227], v[226:227], s[76:77]\n\t"
    "v_pk_mul_f32 v[228:229], v[228:229], s[74:75]\n\t"
    "v_pk_mul_f32 v[230:231], v[230:231], s[76:77]\n\t"
    "v_mfma_f32_16x16x32_bf16 v[24:27], v[100:103], v[168:171], v[24:27]\n\t"
    "v_exp_f32_e64 v232, -|v224|\n\t"
    "v_mfma_f32_16x16x32_bf16 v[28:31], v[100:103], v[172:175], v[28:31]\n\t"
    "v_exp_f32_e64 v233, -|v225|\n\t"
    "s_waitcnt lgkmcnt(0)\n\t"
    "v_mfma_f32_16x16x32_bf16 v[16:19], v[104:107], v[176:179], v[16:19]\n\t"
    "v_exp_f32_e64 v234, -|v228|\n\t"
    "v_mfma_f32_16x16x32_bf16 v[20:23], v[104:107], v[180:183], v[20:23]\n\t"
    "v_exp_f32_e64 v235, -|v229|\n\t"
    "v_mfma_f32_16x16x32_bf16 v[24:27], v[104:107], v[184:187], v[24:27]\n\t"
    "v_exp_f32 v226, v226\n\t"
    "v_mfma_f32_16x16x32_bf16 v[28:31], v[104:107], v[188:191], v[28:31]\n\t"
    "v_exp_f32 v227, v227\n\t"
    "v_exp_f32 v230, v230\n\t"
    "v_exp_f32 v231, v231\n\t"
    "v_pk_add_f32 v[236:237], v[232:233], 1.0 op_sel_hi:[1,0]\n\t"
    "v_pk_add_f32 v[238:239], v[234:235], 1.0 op_sel_hi:[1,0]\n\t"
    "v_pk_add_f32 v[232:233], v[232:233], 1.0 op_sel_hi:[1,0] neg_lo:[1,0] neg_hi:[1,0]\n\t"
    "v_pk_add_f32 v[234:235], v[234:235], 1.0 op_sel_hi:[1,0] neg_lo:[1,0] neg_hi:[1,0]\n\t"
    "v_pk_fma_f32 v[236:237], v[236:237], v[226:227], v[236:237]\n\t"
    "v_pk_fma_f32 v[238:239], v[238:239], v[230:231], v[238:239]\n\t"
    "v_rcp_f32 v236, v236\n\t"
    "v_rcp_f32 v237, v237\n\t"
    "v_rcp_f32 v238, v238\n\t"
    "v_rcp_f32 v239, v239\n\t"
    "v_pk_mul_f32 v[232:233], v[232:233], v[236:237]\n\t"
    "v_pk_mul_f32 v[234:235], v[234:235], v[238:239]\n\t"
    "v_mfma_f32_16x16x32_bf16 v[16:19], v[108:111], v[192:195], v[16:19]\n\t"
    "v_bfi_b32 v232, s73, v232, v224\n\t"
    "v_bfi_b32 v233, s73, v233, v225\n\t"
    "v_bfi_b32 v234, s73, v234, v228\n\t"
    "v_bfi_b32 v235, s73, v235, v229\n\t"
    "v_cvt_pk_bf16_f32 v43, v232, v234\n\t"
    "v_cvt_pk_bf16_f32 v47, v233, v235\n\t"
    "ds_write_b128 %[vzw0], v[40:43] offset:10240\n\t"
    "ds_write_b128 %[vzw1], v[44:47] offset:10240\n\t"
    "v_mfma_f32_16x16x32_bf16 v[20:23], v[108:111], v[196:199], v[20:23]\n\t"
    "v_accvgpr_read_b32 v208, a24\n\t"
    "v_accvgpr_read_b32 v209, a25\n\t"
    "v_mfma_f32_16x16x32_bf16 v[24:27], v[108:111], v[200:203], v[24:27]\n\t"
    "v_accvgpr_read_b32 v210, a26\n\t"
    "v_accvgpr_read_b32 v211, a27\n\t"
    "v_mfma_f32_16x16x32_bf16 v[28:31], v[108:111], v[204:207], v[28:31]\n\t"
    "v_accvgpr_read_b32 v212, a56\n\t"
    "v_accvgpr_read_b32 v213, a57\n\t"
    "v_accvgpr_read_b32 v214, a58\n\t"
    "v_accvgpr_read_b32 v215, a59\n\t"
    "v_permlane32_swap_b32 v208, v210\n\t"
    "v_permlane32_swap_b32 v209, v211\n\t"
    "v_permlane32_swap_b32 v212, v214\n\t"
    "v_permlane32_swap_b32 v213, v215\n\t"
    "v_pk_mul_f32 v[208:209], v[208:209], s[74:75]\n\t"
    "v_pk_mul_f32 v[210:211], v[210:211], s[76:77]\n\t"
    "v_pk_mul_f32 v[212:213], v[212:213], s[74:75]\n\t"
    "v_pk_mul_f32 v[214:215], v[214:215], s[76:77]\n\t"
    "v_exp_f32_e64 v216, -|v208|\n\t"
    "v_exp_f32_e64 v217, -|v209|\n\t"
    "s_xor_b32 s64, %[mx], 4096\n\t"
    "v_cvt_pk_bf16_f32 v32, v16, v20\n\t"
    "v_cvt_pk_bf16_f32 v33, v24, v28\n\t"
    "buffer_store_dwordx2 v[32:33], %[vout], s[56:59], s64 offen sc1\n\t"
    "v_exp_f32_e64 v218, -|v212|\n\t"
    "v_exp_f32_e64 v219, -|v213|\n\t"
    "v_cvt_pk_bf16_f32 v34, v17, v21\n\t"
    "v_cvt_pk_bf16_f32 v35, v25, v29\n\t"
    "buffer_store_dwordx2 v[34:35], %[vout], s[56:59], s64 offen offset:512 sc1\n\t"
    "v_exp_f32 v210, v210\n\t"
    "v_exp_f32 v211, v211\n\t"
    "v_cvt_pk_bf16_f32 v36, v18, v22\n\t"
    "v_cvt_pk_bf16_f32 v37, v26, v30\n\t"
    "s_xor_b32 s64, %[mx], 12288\n\t"
    "buffer_store_dwordx2 v[36:37], %[vout], s[56:59], s64 offen sc1\n\t"
    "v_exp_f32 v214, v214\n\t"
    "v_cvt_pk_bf16_f32 v38, v19, v23\n\t"
    "v_cvt_pk_bf16_f32 v39, v27, v31\n\t"
    "buffer_store_dwordx2 v[38:39], %[vout], s[56:59], s64 offen offset:512 sc1\n\t"
    "v_exp_f32 v215, v215\n\t"
    "s_waitcnt lgkmcnt(0)\n\t"
    "s_barrier\n\t"
    "ds_read_b128 v[80:83], %[vzr0] offset:10240\n\t"
    "v_pk_add_f32 v[220:221], v[216:217], 1.0 op_sel_hi:[1,0]\n\t"
    "v_pk_add_f32 v[222:223], v[218:219], 1.0 op_sel_hi:[1,0]\n\t"
    "v_pk_add_f32 v[216:217], v[216:217], 1.0 op_sel_hi:[1,0] neg_lo:[1,0] neg_hi:[1,0]\n\t"
    "ds_read_b128 v[84:87], %[vzr1] offset:10240\n\t"
    "v_pk_add_f32 v[218:219], v[218:219], 1.0 op_sel_hi:[1,0] neg_lo:[1,0] neg_hi:[1,0]\n\t"
    "v_pk_fma_f32 v[220:221], v[220:221], v[210:211], v[220:221]\n\t"
    "v_pk_fma_f32 v[222:223], v[222:223], v[214:215], v[222:223]\n\t"
    "ds_read_b128 v[88:91], %[vzr0] offset:26624\n\t"
    "v_rcp_f32 v220, v220\n\t"
    "v_rcp_f32 v221, v221\n\t"
    "ds_read_b128 v[92:95], %[vzr1] offset:26624\n\t"
    "v_rcp_f32 v222, v222\n\t"
    "v_rcp_f32 v223, v223\n\t"
    "v_pk_mul_f32 v[216:217], v[216:217], v[220:221]\n\t"
    "v_pk_mul_f32 v[218:219], v[218:219], v[222:223]\n\t"
    "ds_read_b128 v[96:99], %[vzr0] offset:43008\n\t"
    "v_bfi_b32 v216, s73, v216, v208\n\t"
    "v_bfi_b32 v217, s73, v217, v209\n\t"
    "v_bfi_b32 v218, s73, v218, v212\n\t"
    "ds_read_b128 v[100:103], %[vzr1] offset:43008\n\t"
    "v_bfi_b32 v219, s73, v219, v213\n\t"
    "v_cvt_pk_bf16_f32 v40, v216, v218\n\t"
    "v_cvt_pk_bf16_f32 v44, v217, v219\n\t"
    "v_accvgpr_read_b32 v224, a88\n\t"
    "ds_read_b128 v[104:107], %[vzr0] offset:59392\n\t"
    "v_accvgpr_read_b32 v225, a89\n\t"
    "v_accvgpr_read_b32 v226, a90\n\t"
    "v_accvgpr_read_b32 v227, a91\n\t"
    "v_accvgpr_read_b32 v228, a120\n\t"
    "ds_read_b128 v[108:111], %[vzr1] offset:59392\n\t"
    "v_accvgpr_read_b32 v229, a121\n\t"
    "v_accvgpr_read_b32 v230, a122\n\t"
    "v_accvgpr_read_b32 v231, a123\n\t"
    "v_permlane32_swap_b32 v224, v226\n\t"
    "v_permlane32_swap_b32 v225, v227\n\t"
    "v_permlane32_swap_b32 v228, v230\n\t"
    "v_permlane32_swap_b32 v229, v231\n\t"
    "v_pk_mul_f32 v[224:225], v[224:225], s[74:75]\n\t"
    "v_pk_mul_f32 v[226:227], v[226:227], s[76:77]\n\t"
    "v_pk_mul_f32 v[228:229], v[228:229], s[74:75]\n\t"
    "v_pk_mul_f32 v[230:231], v[230:231], s[76:77]\n\t"
    "s_waitcnt lgkmcnt(7)\n\t"
    "v_mfma_f32_16x16x32_bf16 v[16:19], v[80:83], v[48:51], 0\n\t"
    "v_exp_f32_e64 v232, -|v224|\n\t"
    "v_mfma_f32_16x16x32_bf16 v[20:23], v[80:83], v[52:55], 0\n\t"
    "v_exp_f32_e64 v233, -|v225|\n\t"
    "v_mfma_f32_16x16x32_bf16 v[24:27], v[80:83], v[56:59], 0\n\t"
    "v_exp_f32_e64 v234, -|v228|\n\t"
    "v_mfma_f32_16x16x32_bf16 v[28:31], v[80:83], v[60:63], 0\n\t"
    "v_exp_f32_e64 v235, -|v229|\n\t"
    "s_waitcnt lgkmcnt(5)\n\t"
    "v_mfma_f32_16x16x32_bf16 v[16:19], v[84:87], v[64:67], v[16:19]\n\t"
    "v_exp_f32 v226, v226\n\t"
    "v_mfma_f32_16x16x32_bf16 v[20:23], v[84:87], v[68:71], v[20:23]\n\t"
    "v_exp_f32 v227, v227\n\t"
    "v_exp_f32 v230, v230\n\t"
    "v_exp_f32 v231, v231\n\t"
    "v_pk_add_f32 v[236:237], v[232:233], 1.0 op_sel_hi:[1,0]\n\t"
    "v_pk_add_f32 v[238:239], v[234:235], 1.0 op_sel_hi:[1,0]\n\t"
    "v_pk_add_f32 v[232:233], v[232:233], 1.0 op_sel_hi:[1,0] neg_lo:[1,0] neg_hi:[1,0]\n\t"
    "v_pk_add_f32 v[234:235], v[234:235], 1.0 op_sel_hi:[1,0] neg_lo:[1,0] neg_hi:[1,0]\n\t"
    "v_pk_fma_f32 v[236:237], v[236:237], v[226:227], v[236:237]\n\t"
    "v_pk_fma_f32 v[238:239], v[238:239], v[230:231], v[238:239]\n\t"
    "v_rcp_f32 v236, v236\n\t"
    "v_rcp_f32 v237, v237\n\t"
    "v_rcp_f32 v238, v238\n\t"
    "v_rcp_f32 v239, v239\n\t"
    "v_pk_mul_f32 v[232:233], v[232:233], v[236:237]\n\t"
    "v_pk_mul_f32 v[234:235], v[234:235], v[238:239]\n\t"
    "v_mfma_f32_16x16x32_bf16 v[24:27], v[84:87], v[72:75], v[24:27]\n\t"
    "v_bfi_b32 v232, s73, v232, v224\n\t"
    "v_bfi_b32 v233, s73, v233, v225\n\t"
    "v_bfi_b32 v234, s73, v234, v228\n\t"
    "v_mfma_f32_16x16x32_bf16 v[28:31], v[84:87], v[76:79], v[28:31]\n\t"
    "v_bfi_b32 v235, s73, v235, v229\n\t"
    "v_cvt_pk_bf16_f32 v41, v232, v234\n\t"
    "v_cvt_pk_bf16_f32 v45, v233, v235\n\t"
    "s_waitcnt lgkmcnt(4)\n\t"
    "v_mfma_f32_16x16x32_bf16 v[16:19], v[88:91], v[112:115], v[16:19]\n\t"
    "v_accvgpr_read_b32 v208, a152\n\t"
    "v_accvgpr_read_b32 v209, a153\n\t"
    "v_mfma_f32_16x16x32_bf16 v[20:23], v[88:91], v[116:119], v[20:23]\n\t"
    "v_accvgpr_read_b32 v210, a154\n\t"
    "v_accvgpr_read_b32 v211, a155\n\t"
    "v_mfma_f32_16x16x32_bf16 v[24:27], v[88:91], v[120:123], v[24:27]\n\t"
    "v_accvgpr_read_b32 v212, a184\n\t"
    "v_accvgpr_read_b32 v213, a185\n\t"
    "v_accvgpr_read_b32 v214, a186\n\t"
    "v_accvgpr_read_b32 v215, a187\n\t"
    "v_permlane32_swap_b32 v208, v210\n\t"
    "v_permlane32_swap_b32 v209, v211\n\t"
    "v_permlane32_swap_b32 v212, v214\n\t"
    "v_permlane32_swap_b32 v213, v215\n\t"
    "v_pk_mul_f32 v[208:209], v[208:209], s[74:75]\n\t"
    "v_pk_mul_f32 v[210:211], v[210:211], s[76:77]\n\t"
    "v_pk_mul_f32 v[212:213], v[212:213], s[74:75]\n\t"
    "v_pk_mul_f32 v[214:215], v[214:215], s[76:77]\n\t"
    "v_mfma_f32_16x16x32_bf16 v[28:31], v[88:91], v[124:127], v[28:31]\n\t"
    "v_exp_f32_e64 v216, -|v208|\n\t"
    "s_waitcnt lgkmcnt(3)\n\t"
    "v_mfma_f32_16x16x32_bf16 v[16:19], v[92:95], v[128:131], v[16:19]\n\t"
    "v_exp_f32_e64 v217, -|v209|\n\t"
    "v_mfma_f32_16x16x32_bf16 v[20:23], v[92:95], v[132:135], v[20:23]\n\t"
    "v_exp_f32_e64 v218, -|v212|\n\t"
    "v_mfma_f32_16x16x32_bf16 v[24:27], v[92:95], v[136:139], v[24:27]\n\t"
    "v_exp_f32_e64 v219, -|v213|\n\t"
    "v_mfma_f32_16x16x32_bf16 v[28:31], v[92:95], v[140:143], v[28:31]\n\t"
    "v_exp_f32 v210, v210\n\t"
    "s_waitcnt lgkmcnt(2)\n\t"
    "v_mfma_f32_16x16x32_bf16 v[16:19], v[96:99], v[144:147], v[16:19]\n\t"
    "v_exp_f32 v211, v211\n\t"
    "v_exp_f32 v214, v214\n\t"
    "v_exp_f32 v215, v215\n\t"
    "v_pk_add_f32 v[220:221], v[216:217], 1.0 op_sel_hi:[1,0]\n\t"
    "v_pk_add_f32 v[222:223], v[218:219], 1.0 op_sel_hi:[1,0]\n\t"
    "v_pk_add_f32 v[216:217], v[216:217], 1.0 op_sel_hi:[1,0] neg_lo:[1,0] neg_hi:[1,0]\n\t"
    "v_pk_add_f32 v[218:219], v[218:219], 1.0 op_sel_hi:[1,0] neg_lo:[1,0] neg_hi:[1,0]\n\t"
    "v_pk_fma_f32 v[220:221], v[220:221], v[210:211], v[220:221]\n\t"
    "v_pk_fma_f32 v[222:223], v[222:223], v[214:215], v[222:223]\n\t"
    "v_rcp_f32 v220, v220\n\t"
    "v_rcp_f32 v221, v221\n\t"
    "v_rcp_f32 v222, v222\n\t"
    "v_rcp_f32 v223, v223\n\t"
    "v_pk_mul_f32 v[216:217], v[216:217], v[220:221]\n\t"
    "v_pk_mul_f32 v[218:219], v[218:219], v[222:223]\n\t"
    "v_mfma_f32_16x16x32_bf16 v[20:23], v[96:99], v[148:151], v[20:23]\n\t"
    "v_bfi_b32 v216, s73, v216, v208\n\t"
    "v_bfi_b32 v217, s73, v217, v209\n\t"
    "v_bfi_b32 v218, s73, v218, v212\n\t"
    "v_mfma_f32_16x16x32_bf16 v[24:27], v[96:99], v[152:155], v[24:27]\n\t"
    "v_bfi_b32 v219, s73, v219, v213\n\t"
    "v_cvt_pk_bf16_f32 v42, v216, v218\n\t"
    "v_cvt_pk_bf16_f32 v46, v217, v219\n\t"
    "v_mfma_f32_16x16x32_bf16 v[28:31], v[96:99], v[156:159], v[28:31]\n\t"
    "v_accvgpr_read_b32 v224, a216\n\t"
    "v_accvgpr_read_b32 v225, a217\n\t"
    "s_waitcnt lgkmcnt(1)\n\t"
    "v_mfma_f32_16x16x32_bf16 v[16:19], v[100:103], v[160:163], v[16:19]\n\t"
    "v_accvgpr_read_b32 v226, a218\n\t"
    "v_accvgpr_read_b32 v227, a219\n\t"
    "v_mfma_f32_16x16x32_bf16 v[20:23], v[100:103], v[164:167], v[20:23]\n\t"
    "v_accvgpr_read_b32 v228, a248\n\t"
    "v_accvgpr_read_b32 v229, a249\n\t"
    "v_accvgpr_read_b32 v230, a250\n\t"
    "v_accvgpr_read_b32 v231, a251\n\t"
    "v_permlane32_swap_b32 v224, v226\n\t"
    "v_permlane32_swap_b32 v225, v227\n\t"
    "v_permlane32_swap_b32 v228, v230\n\t"
    "v_permlane32_swap_b32 v229, v231\n\t"
    "v_pk_mul_f32 v[224:225], v[224:225], s[74:75]\n\t"
    "v_pk_mul_f32 v[226:227], v[226:227], s[76:77]\n\t"
    "v_pk_mul_f32 v[228:229], v[228:229], s[74:75]\n\t"
    "v_pk_mul_f32 v[230:231], v[230:231], s[76:77]\n\t"
    "v_mfma_f32_16x16x32_bf16 v[24:27], v[100:103], v[168:171], v[24:27]\n\t"
    "v_exp_f32_e64 v232, -|v224|\n\t"
    "v_mfma_f32_16x16x32_bf16 v[28:31], v[100:103], v[172:175], v[28:31]\n\t"
    "v_exp_f32_e64 v233, -|v225|\n\t"
    "s_waitcnt lgkmcnt(0)\n\t"
    "v_mfma_f32_16x16x32_bf16 v[16:19], v[104:107], v[176:179], v[16:19]\n\t"
    "v_exp_f32_e64 v234, -|v228|\n\t"
    "v_mfma_f32_16x16x32_bf16 v[20:23], v[104:107], v[180:183], v[20:23]\n\t"
    "v_exp_f32_e64 v235, -|v229|\n\t"
    "v_mfma_f32_16x16x32_bf16 v[24:27], v[104:107], v[184:187], v[24:27]\n\t"
    "v_exp_f32 v226, v226\n\t"
    "v_mfma_f32_16x16x32_bf16 v[28:31], v[104:107], v[188:191], v[28:31]\n\t"
    "v_exp_f32 v227, v227\n\t"
    "v_exp_f32 v230, v230\n\t"
    "v_exp_f32 v231, v231\n\t"
    "v_pk_add_f32 v[236:237], v[232:233], 1.0 op_sel_hi:[1,0]\n\t"
    "v_pk_add_f32 v[238:239], v[234:235], 1.0 op_sel_hi:[1,0]\n\t"
    "v_pk_add_f32 v[232:233], v[232:233], 1.0 op_sel_hi:[1,0] neg_lo:[1,0] neg_hi:[1,0]\n\t"
    "v_pk_add_f32 v[234:235], v[234:235], 1.0 op_sel_hi:[1,0] neg_lo:[1,0] neg_hi:[1,0]\n\t"
    "v_pk_fma_f32 v[236:237], v[236:237], v[226:227], v[236:237]\n\t"
    "v_pk_fma_f32 v[238:239], v[238:239], v[230:231], v[238:239]\n\t"
    "v_rcp_f32 v236, v236\n\t"
    "v_rcp_f32 v237, v237\n\t"
    "v_rcp_f32 v238, v238\n\t"
    "v_rcp_f32 v239, v239\n\t"
    "v_pk_mul_f32 v[232:233], v[232:233], v[236:237]\n\t"
    "v_pk_mul_f32 v[234:235], v[234:235], v[238:239]\n\t"
    "v_mfma_f32_16x16x32_bf16 v[16:19], v[108:111], v[192:195], v[16:19]\n\t"
    "v_bfi_b32 v232, s73, v232, v224\n\t"
    "v_bfi_b32 v233, s73, v233, v225\n\t"
    "v_bfi_b32 v234, s73, v234, v228\n\t"
    "v_bfi_b32 v235, s73, v235, v229\n\t"
    "v_cvt_pk_bf16_f32 v43, v232, v234\n\t"
    "v_cvt_pk_bf16_f32 v47, v233, v235\n\t"
    "ds_write_b128 %[vzw0], v[40:43] offset:12288\n\t"
    "ds_write_b128 %[vzw1], v[44:47] offset:12288\n\t"
    "v_mfma_f32_16x16x32_bf16 v[20:23], v[108:111], v[196:199], v[20:23]\n\t"
    "v_accvgpr_read_b32 v208, a28\n\t"
    "v_accvgpr_read_b32 v209, a29\n\t"
    "v_mfma_f32_16x16x32_bf16 v[24:27], v[108:111], v[200:203], v[24:27]\n\t"
    "v_accvgpr_read_b32 v210, a30\n\t"
    "v_accvgpr_read_b32 v211, a31\n\t"
    "v_mfma_f32_16x16x32_bf16 v[28:31], v[108:111], v[204:207], v[28:31]\n\t"
    "v_accvgpr_read_b32 v212, a60\n\t"
    "v_accvgpr_read_b32 v213, a61\n\t"
    "v_accvgpr_read_b32 v214, a62\n\t"
    "v_accvgpr_read_b32 v215, a63\n\t"
    "v_permlane32_swap_b32 v208, v210\n\t"
    "v_permlane32_swap_b32 v209, v211\n\t"
    "v_permlane32_swap_b32 v212, v214\n\t"
    "v_permlane32_swap_b32 v213, v215\n\t"
    "v_pk_mul_f32 v[208:209], v[208:209], s[74:75]\n\t"
    "v_pk_mul_f32 v[210:211], v[210:211], s[76:77]\n\t"
    "v_pk_mul_f32 v[212:213], v[212:213], s[74:75]\n\t"
    "v_pk_mul_f32 v[214:215], v[214:215], s[76:77]\n\t"
    "v_exp_f32_e64 v216, -|v208|\n\t"
    "v_exp_f32_e64 v217, -|v209|\n\t"
    "s_xor_b32 s64, %[mx], 5120\n\t"
    "v_cvt_pk_bf16_f32 v32, v16, v20\n\t"
    "v_cvt_pk_bf16_f32 v33, v24, v28\n\t"
    "buffer_store_dwordx2 v[32:33], %[vout], s[56:59], s64 offen sc1\n\t"
    "v_exp_f32_e64 v218, -|v212|\n\t"
    "v_exp_f32_e64 v219, -|v213|\n\t"
    "v_cvt_pk_bf16_f32 v34, v17, v21\n\t"
    "v_cvt_pk_bf16_f32 v35, v25, v29\n\t"
    "buffer_store_dwordx2 v[34:35], %[vout], s[56:59], s64 offen offset:512 sc1\n\t"
    "v_exp_f32 v210, v210\n\t"
    "v_exp_f32 v211, v211\n\t"
    "v_cvt_pk_bf16_f32 v36, v18, v22\n\t"
    "v_cvt_pk_bf16_f32 v37, v26, v30\n\t"
    "s_xor_b32 s64, %[mx], 13312\n\t"
    "buffer_store_dwordx2 v[36:37], %[vout], s[56:59], s64 offen sc1\n\t"
    "v_exp_f32 v214, v214\n\t"
    "v_cvt_pk_bf16_f32 v38, v19, v23\n\t"
    "v_cvt_pk_bf16_f32 v39, v27, v31\n\t"
    "buffer_store_dwordx2 v[38:39], %[vout], s[56:59], s64 offen offset:512 sc1\n\t"
    "v_exp_f32 v215, v215\n\t"
    "s_waitcnt lgkmcnt(0)\n\t"
    "s_barrier\n\t"
    "ds_read_b128 v[80:83], %[vzr0] offset:12288\n\t"
    "v_pk_add_f32 v[220:221], v[216:217], 1.0 op_sel_hi:[1,0]\n\t"
    "v_pk_add_f32 v[222:223], v[218:219], 1.0 op_sel_hi:[1,0]\n\t"
    "v_pk_add_f32 v[216:217], v[216:217], 1.0 op_sel_hi:[1,0] neg_lo:[1,0] neg_hi:[1,0]\n\t"
    "ds_read_b128 v[84:87], %[vzr1] offset:12288\n\t"
    "v_pk_add_f32 v[218:219], v[218:219], 1.0 op_sel_hi:[1,0] neg_lo:[1,0] neg_hi:[1,0]\n\t"
    "v_pk_fma_f32 v[220:221], v[220:221], v[210:211], v[220:221]\n\t"
    "v_pk_fma_f32 v[222:223], v[222:223], v[214:215], v[222:223]\n\t"
    "ds_read_b128 v[88:91], %[vzr0] offset:28672\n\t"
    "v_rcp_f32 v220, v220\n\t"
    "v_rcp_f32 v221, v221\n\t"
    "ds_read_b128 v[92:95], %[vzr1] offset:28672\n\t"
    "v_rcp_f32 v222, v222\n\t"
    "v_rcp_f32 v223, v223\n\t"
    "v_pk_mul_f32 v[216:217], v[216:217], v[220:221]\n\t"
    "v_pk_mul_f32 v[218:219], v[218:219], v[222:223]\n\t"
    "ds_read_b128 v[96:99], %[vzr0] offset:45056\n\t"
    "v_bfi_b32 v216, s73, v216, v208\n\t"
    "v_bfi_b32 v217, s73, v217, v209\n\t"
    "v_bfi_b32 v218, s73, v218, v212\n\t"
    "ds_read_b128 v[100:103], %[vzr1] offset:45056\n\t"
    "v_bfi_b32 v219, s73, v219, v213\n\t"
    "v_cvt_pk_bf16_f32 v40, v216, v218\n\t"
    "v_cvt_pk_bf16_f32 v44, v217, v219\n\t"
    "v_accvgpr_read_b32 v224, a92\n\t"
    "ds_read_b128 v[104:107], %[vzr0] offset:61440\n\t"
    "v_accvgpr_read_b32 v225, a93\n\t"
    "v_accvgpr_read_b32 v226, a94\n\t"
    "v_accvgpr_read_b32 v227, a95\n\t"
    "v_accvgpr_read_b32 v228, a124\n\t"
    "ds_read_b128 v[108:111], %[vzr1] offset:61440\n\t"
    "v_accvgpr_read_b32 v229, a125\n\t"
    "v_accvgpr_read_b32 v230, a126\n\t"
    "v_accvgpr_read_b32 v231, a127\n\t"
    "v_permlane32_swap_b32 v224, v226\n\t"
    "v_permlane32_swap_b32 v225, v227\n\t"
    "v_permlane32_swap_b32 v228, v230\n\t"
    "v_permlane32_swap_b32 v229, v231\n\t"
    "v_pk_mul_f32 v[224:225], v[224:225], s[74:75]\n\t"
    "v_pk_mul_f32 v[226:227], v[226:227], s[76:77]\n\t"
    "v_pk_mul_f32 v[228:229], v[228:229], s[74:75]\n\t"
    "v_pk_mul_f32 v[230:231], v[230:231], s[76:77]\n\t"
    "s_waitcnt lgkmcnt(7)\n\t"
    "v_mfma_f32_16x16x32_bf16 v[16:19], v[80:83], v[48:51], 0\n\t"
    "v_exp_f32_e64 v232, -|v224|\n\t"
    "v_mfma_f32_16x16x32_bf16 v[20:23], v[80:83], v[52:55], 0\n\t"
    "v_exp_f32_e64 v233, -|v225|\n\t"
    "v_mfma_f32_16x16x32_bf16 v[24:27], v[80:83], v[56:59], 0\n\t"
    "v_exp_f32_e64 v234, -|v228|\n\t"
    "v_mfma_f32_16x16x32_bf16 v[28:31], v[80:83], v[60:63], 0\n\t"
    "v_exp_f32_e64 v235, -|v229|\n\t"
    "s_waitcnt lgkmcnt(5)\n\t"
    "v_mfma_f32_16x16x32_bf16 v[16:19], v[84:87], v[64:67], v[16:19]\n\t"
    "v_exp_f32 v226, v226\n\t"
    "v_mfma_f32_16x16x32_bf16 v[20:23], v[84:87], v[68:71], v[20:23]\n\t"
    "v_exp_f32 v227, v227\n\t"
    "v_exp_f32 v230, v230\n\t"
    "v_exp_f32 v231, v231\n\t"
    "v_pk_add_f32 v[236:237], v[232:233], 1.0 op_sel_hi:[1,0]\n\t"
    "v_pk_add_f32 v[238:239], v[234:235], 1.0 op_sel_hi:[1,0]\n\t"
    "v_pk_add_f32 v[232:233], v[232:233], 1.0 op_sel_hi:[1,0] neg_lo:[1,0] neg_hi:[1,0]\n\t"
    "v_pk_add_f32 v[234:235], v[234:235], 1.0 op_sel_hi:[1,0] neg_lo:[1,0] neg_hi:[1,0]\n\t"
    "v_pk_fma_f32 v[236:237], v[236:237], v[226:227], v[236:237]\n\t"
    "v_pk_fma_f32 v[238:239], v[238:239], v[230:231], v[238:239]\n\t"
    "v_rcp_f32 v236, v236\n\t"
    "v_rcp_f32 v237, v237\n\t"
    "v_rcp_f32 v238, v238\n\t"
    "v_rcp_f32 v239, v239\n\t"
    "v_pk_mul_f32 v[232:233], v[232:233], v[236:237]\n\t"
    "v_pk_mul_f32 v[234:235], v[234:235], v[238:239]\n\t"
    "v_mfma_f32_16x16x32_bf16 v[24:27], v[84:87], v[72:75], v[24:27]\n\t"
    "v_bfi_b32 v232, s73, v232, v224\n\t"
    "v_bfi_b32 v233, s73, v233, v225\n\t"
    "v_bfi_b32 v234, s73, v234, v228\n\t"
    "v_mfma_f32_16x16x32_bf16 v[28:31], v[84:87], v[76:79], v[28:31]\n\t"
    "v_bfi_b32 v235, s73, v235, v229\n\t"
    "v_cvt_pk_bf16_f32 v41, v232, v234\n\t"
    "v_cvt_pk_bf16_f32 v45, v233, v235\n\t"
    "s_waitcnt lgkmcnt(4)\n\t"
    "v_mfma_f32_16x16x32_bf16 v[16:19], v[88:91], v[112:115], v[16:19]\n\t"
    "v_accvgpr_read_b32 v208, a156\n\t"
    "v_accvgpr_read_b32 v209, a157\n\t"
    "v_mfma_f32_16x16x32_bf16 v[20:23], v[88:91], v[116:119], v[20:23]\n\t"
    "v_accvgpr_read_b32 v210, a158\n\t"
    "v_accvgpr_read_b32 v211, a159\n\t"
    "v_mfma_f32_16x16x32_bf16 v[24:27], v[88:91], v[120:123], v[24:27]\n\t"
    "v_accvgpr_read_b32 v212, a188\n\t"
    "v_accvgpr_read_b32 v213, a189\n\t"
    "v_accvgpr_read_b32 v214, a190\n\t"
    "v_accvgpr_read_b32 v215, a191\n\t"
    "v_permlane32_swap_b32 v208, v210\n\t"
    "v_permlane32_swap_b32 v209, v211\n\t"
    "v_permlane32_swap_b32 v212, v214\n\t"
    "v_permlane32_swap_b32 v213, v215\n\t"
    "v_pk_mul_f32 v[208:209], v[208:209], s[74:75]\n\t"
    "v_pk_mul_f32 v[210:211], v[210:211], s[76:77]\n\t"
    "v_pk_mul_f32 v[212:213], v[212:213], s[74:75]\n\t"
    "v_pk_mul_f32 v[214:215], v[214:215], s[76:77]\n\t"
    "v_mfma_f32_16x16x32_bf16 v[28:31], v[88:91], v[124:127], v[28:31]\n\t"
    "v_exp_f32_e64 v216, -|v208|\n\t"
    "s_waitcnt lgkmcnt(3)\n\t"
    "v_mfma_f32_16x16x32_bf16 v[16:19], v[92:95], v[128:131], v[16:19]\n\t"
    "v_exp_f32_e64 v217, -|v209|\n\t"
    "v_mfma_f32_16x16x32_bf16 v[20:23], v[92:95], v[132:135], v[20:23]\n\t"
    "v_exp_f32_e64 v218, -|v212|\n\t"
    "v_mfma_f32_16x16x32_bf16 v[24:27], v[92:95], v[136:139], v[24:27]\n\t"
    "v_exp_f32_e64 v219, -|v213|\n\t"
    "v_mfma_f32_16x16x32_bf16 v[28:31], v[92:95], v[140:143], v[28:31]\n\t"
    "v_exp_f32 v210, v210\n\t"
    "s_waitcnt lgkmcnt(2)\n\t"
    "v_mfma_f32_16x16x32_bf16 v[16:19], v[96:99], v[144:147], v[16:19]\n\t"
    "v_exp_f32 v211, v211\n\t"
    "v_exp_f32 v214, v214\n\t"
    "v_exp_f32 v215, v215\n\t"
    "v_pk_add_f32 v[220:221], v[216:217], 1.0 op_sel_hi:[1,0]\n\t"
    "v_pk_add_f32 v[222:223], v[218:219], 1.0 op_sel_hi:[1,0]\n\t"
    "v_pk_add_f32 v[216:217], v[216:217], 1.0 op_sel_hi:[1,0] neg_lo:[1,0] neg_hi:[1,0]\n\t"
    "v_pk_add_f32 v[218:219], v[218:219], 1.0 op_sel_hi:[1,0] neg_lo:[1,0] neg_hi:[1,0]\n\t"
    "v_pk_fma_f32 v[220:221], v[220:221], v[210:211], v[220:221]\n\t"
    "v_pk_fma_f32 v[222:223], v[222:223], v[214:215], v[222:223]\n\t"
    "v_rcp_f32 v220, v220\n\t"
    "v_rcp_f32 v221, v221\n\t"
    "v_rcp_f32 v222, v222\n\t"
    "v_rcp_f32 v223, v223\n\t"
    "v_pk_mul_f32 v[216:217], v[216:217], v[220:221]\n\t"
    "v_pk_mul_f32 v[218:219], v[218:219], v[222:223]\n\t"
    "v_mfma_f32_16x16x32_bf16 v[20:23], v[96:99], v[148:151], v[20:23]\n\t"
    "v_bfi_b32 v216, s73, v216, v208\n\t"
    "v_bfi_b32 v217, s73, v217, v209\n\t"
    "v_bfi_b32 v218, s73, v218, v212\n\t"
    "v_mfma_f32_16x16x32_bf16 v[24:27], v[96:99], v[152:155], v[24:27]\n\t"
    "v_bfi_b32 v219, s73, v219, v213\n\t"
    "v_cvt_pk_bf16_f32 v42, v216, v218\n\t"
    "v_cvt_pk_bf16_f32 v46, v217, v219\n\t"
    "v_mfma_f32_16x16x32_bf16 v[28:31], v[96:99], v[156:159], v[28:31]\n\t"
    "v_accvgpr_read_b32 v224, a220\n\t"
    "v_accvgpr_read_b32 v225, a221\n\t"
    "s_waitcnt lgkmcnt(1)\n\t"
    "v_mfma_f32_16x16x32_bf16 v[16:19], v[100:103], v[160:163], v[16:19]\n\t"
    "v_accvgpr_read_b32 v226, a222\n\t"
    "v_accvgpr_read_b32 v227, a223\n\t"
    "v_mfma_f32_16x16x32_bf16 v[20:23], v[100:103], v[164:167], v[20:23]\n\t"
    "v_accvgpr_read_b32 v228, a252\n\t"
    "v_accvgpr_read_b32 v229, a253\n\t"
    "v_accvgpr_read_b32 v230, a254\n\t"
    "v_accvgpr_read_b32 v231, a255\n\t"
    "v_permlane32_swap_b32 v224, v226\n\t"
    "v_permlane32_swap_b32 v225, v227\n\t"
    "v_permlane32_swap_b32 v228, v230\n\t"
    "v_permlane32_swap_b32 v229, v231\n\t"
    "v_pk_mul_f32 v[224:225], v[224:225], s[74:75]\n\t"
    "v_pk_mul_f32 v[226:227], v[226:227], s[76:77]\n\t"
    "v_pk_mul_f32 v[228:229], v[228:229], s[74:75]\n\t"
    "v_pk_mul_f32 v[230:231], v[230:231], s[76:77]\n\t"
    "v_mfma_f32_16x16x32_bf16 v[24:27], v[100:103], v[168:171], v[24:27]\n\t"
    "v_exp_f32_e64 v232, -|v224|\n\t"
    "v_mfma_f32_16x16x32_bf16 v[28:31], v[100:103], v[172:175], v[28:31]\n\t"
    "v_exp_f32_e64 v233, -|v225|\n\t"
    "s_waitcnt lgkmcnt(0)\n\t"
    "v_mfma_f32_16x16x32_bf16 v[16:19], v[104:107], v[176:179], v[16:19]\n\t"
    "v_exp_f32_e64 v234, -|v228|\n\t"
    "v_mfma_f32_16x16x32_bf16 v[20:23], v[104:107], v[180:183], v[20:23]\n\t"
    "v_exp_f32_e64 v235, -|v229|\n\t"
    "v_mfma_f32_16x16x32_bf16 v[24:27], v[104:107], v[184:187], v[24:27]\n\t"
    "v_exp_f32 v226, v226\n\t"
    "v_mfma_f32_16x16x32_bf16 v[28:31], v[104:107], v[188:191], v[28:31]\n\t"
    "v_exp_f32 v227, v227\n\t"
    "v_exp_f32 v230, v230\n\t"
    "v_exp_f32 v231, v231\n\t"
    "v_pk_add_f32 v[236:237], v[232:233], 1.0 op_sel_hi:[1,0]\n\t"
    "v_pk_add_f32 v[238:239], v[234:235], 1.0 op_sel_hi:[1,0]\n\t"
    "v_pk_add_f32 v[232:233], v[232:233], 1.0 op_sel_hi:[1,0] neg_lo:[1,0] neg_hi:[1,0]\n\t"
    "v_pk_add_f32 v[234:235], v[234:235], 1.0 op_sel_hi:[1,0] neg_lo:[1,0] neg_hi:[1,0]\n\t"
    "v_pk_fma_f32 v[236:237], v[236:237], v[226:227], v[236:237]\n\t"
    "v_pk_fma_f32 v[238:239], v[238:239], v[230:231], v[238:239]\n\t"
    "v_rcp_f32 v236, v236\n\t"
    "v_rcp_f32 v237, v237\n\t"
    "v_rcp_f32 v238, v238\n\t"
    "v_rcp_f32 v239, v239\n\t"
    "v_pk_mul_f32 v[232:233], v[232:233], v[236:237]\n\t"
    "v_pk_mul_f32 v[234:235], v[234:235], v[238:239]\n\t"
    "v_mfma_f32_16x16x32_bf16 v[16:19], v[108:111], v[192:195], v[16:19]\n\t"
    "v_bfi_b32 v232, s73, v232, v224\n\t"
    "v_bfi_b32 v233, s73, v233, v225\n\t"
    "v_bfi_b32 v234, s73, v234, v228\n\t"
    "v_bfi_b32 v235, s73, v235, v229\n\t"
    "v_cvt_pk_bf16_f32 v43, v232, v234\n\t"
    "v_cvt_pk_bf16_f32 v47, v233, v235\n\t"
    "ds_write_b128 %[vzw0], v[40:43] offset:14336\n\t"
    "ds_write_b128 %[vzw1], v[44:47] offset:14336\n\t"
    "v_mfma_f32_16x16x32_bf16 v[20:23], v[108:111], v[196:199], v[20:23]\n\t"
    "v_mfma_f32_16x16x32_bf16 v[24:27], v[108:111], v[200:203], v[24:27]\n\t"
    "v_mfma_f32_16x16x32_bf16 v[28:31], v[108:111], v[204:207], v[28:31]\n\t"
    "s_waitcnt lgkmcnt(0)\n\t"
    "s_barrier\n\t"
    "ds_read_b128 v[80:83], %[vzr0] offset:14336\n\t"
    "ds_read_b128 v[84:87], %[vzr1] offset:14336\n\t"
    "ds_read_b128 v[88:91], %[vzr0] offset:30720\n\t"
    "ds_read_b128 v[92:95], %[vzr1] offset:30720\n\t"
    "ds_read_b128 v[96:99], %[vzr0] offset:47104\n\t"
    "ds_read_b128 v[100:103], %[vzr1] offset:47104\n\t"
    "ds_read_b128 v[104:107], %[vzr0] offset:63488\n\t"
    "ds_read_b128 v[108:111], %[vzr1] offset:63488\n\t"
    "s_xor_b32 s64, %[mx], 6144\n\t"
    "v_cvt_pk_bf16_f32 v32, v16, v20\n\t"
    "v_cvt_pk_bf16_f32 v33, v24, v28\n\t"
    "buffer_store_dwordx2 v[32:33], %[vout], s[56:59], s64 offen sc1\n\t"
    "v_cvt_pk_bf16_f32 v34, v17, v21\n\t"
    "v_cvt_pk_bf16_f32 v35, v25, v29\n\t"
    "buffer_store_dwordx2 v[34:35], %[vout], s[56:59], s64 offen offset:512 sc1\n\t"
    "v_cvt_pk_bf16_f32 v36, v18, v22\n\t"
    "v_cvt_pk_bf16_f32 v37, v26, v30\n\t"
    "s_xor_b32 s64, %[mx], 14336\n\t"
    "buffer_store_dwordx2 v[36:37], %[vout], s[56:59], s64 offen sc1\n\t"
    "v_cvt_pk_bf16_f32 v38, v19, v23\n\t"
    "v_cvt_pk_bf16_f32 v39, v27, v31\n\t"
    "buffer_store_dwordx2 v[38:39], %[vout], s[56:59], s64 offen offset:512 sc1\n\t"
    "s_waitcnt lgkmcnt(7)\n\t"
    "v_mfma_f32_16x16x32_bf16 v[16:19], v[80:83], v[48:51], 0\n\t"
    "v_mfma_f32_16x16x32_bf16 v[20:23], v[80:83], v[52:55], 0\n\t"
    "v_mfma_f32_16x16x32_bf16 v[24:27], v[80:83], v[56:59], 0\n\t"
    "v_mfma_f32_16x16x32_bf16 v[28:31], v[80:83], v[60:63], 0\n\t"
    "s_waitcnt lgkmcnt(6)\n\t"
    "v_mfma_f32_16x16x32_bf16 v[16:19], v[84:87], v[64:67], v[16:19]\n\t"
    "v_mfma_f32_16x16x32_bf16 v[20:23], v[84:87], v[68:71], v[20:23]\n\t"
    "v_mfma_f32_16x16x32_bf16 v[24:27], v[84:87], v[72:75], v[24:27]\n\t"
    "v_mfma_f32_16x16x32_bf16 v[28:31], v[84:87], v[76:79], v[28:31]\n\t"
    "s_waitcnt lgkmcnt(5)\n\t"
    "v_mfma_f32_16x16x32_bf16 v[16:19], v[88:91], v[112:115], v[16:19]\n\t"
    "v_mfma_f32_16x16x32_bf16 v[20:23], v[88:91], v[116:119], v[20:23]\n\t"
    "v_mfma_f32_16x16x32_bf16 v[24:27], v[88:91], v[120:123], v[24:27]\n\t"
    "v_mfma_f32_16x16x32_bf16 v[28:31], v[88:91], v[124:127], v[28:31]\n\t"
    "s_waitcnt lgkmcnt(4)\n\t"
    "v_mfma_f32_16x16x32_bf16 v[16:19], v[92:95], v[128:131], v[16:19]\n\t"
    "v_mfma_f32_16x16x32_bf16 v[20:23], v[92:95], v[132:135], v[20:23]\n\t"
    "v_mfma_f32_16x16x32_bf16 v[24:27], v[92:95], v[136:139], v[24:27]\n\t"
    "v_mfma_f32_16x16x32_bf16 v[28:31], v[92:95], v[140:143], v[28:31]\n\t"
    "s_waitcnt lgkmcnt(3)\n\t"
    "v_mfma_f32_16x16x32_bf16 v[16:19], v[96:99], v[144:147], v[16:19]\n\t"
    "v_mfma_f32_16x16x32_bf16 v[20:23], v[96:99], v[148:151], v[20:23]\n\t"
    "v_mfma_f32_16x16x32_bf16 v[24:27], v[96:99], v[152:155], v[24:27]\n\t"
    "v_mfma_f32_16x16x32_bf16 v[28:31], v[96:99], v[156:159], v[28:31]\n\t"
    "s_waitcnt lgkmcnt(2)\n\t"
    "v_mfma_f32_16x16x32_bf16 v[16:19], v[100:103], v[160:163], v[16:19]\n\t"
    "v_mfma_f32_16x16x32_bf16 v[20:23], v[100:103], v[164:167], v[20:23]\n\t"
    "v_mfma_f32_16x16x32_bf16 v[24:27], v[100:103], v[168:171], v[24:27]\n\t"
    "v_mfma_f32_16x16x32_bf16 v[28:31], v[100:103], v[172:175], v[28:31]\n\t"
    "s_waitcnt lgkmcnt(1)\n\t"
    "v_mfma_f32_16x16x32_bf16 v[16:19], v[104:107], v[176:179], v[16:19]\n\t"
    "v_mfma_f32_16x16x32_bf16 v[20:23], v[104:107], v[180:183], v[20:23]\n\t"
    "v_mfma_f32_16x16x32_bf16 v[24:27], v[104:107], v[184:187], v[24:27]\n\t"
    "v_mfma_f32_16x16x32_bf16 v[28:31], v[104:107], v[188:191], v[28:31]\n\t"
    "s_waitcnt lgkmcnt(0)\n\t"
    "v_mfma_f32_16x16x32_bf16 v[16:19], v[108:111], v[192:195], v[16:19]\n\t"
    "v_mfma_f32_16x16x32_bf16 v[20:23], v[108:111], v[196:199], v[20:23]\n\t"
    "v_mfma_f32_16x16x32_bf16 v[24:27], v[108:111], v[200:203], v[24:27]\n\t"
    "v_mfma_f32_16x16x32_bf16 v[28:31], v[108:111], v[204:207], v[28:31]\n\t"
    "s_nop 7\n\t"
    "s_xor_b32 s64, %[mx], 7168\n\t"
    "v_cvt_pk_bf16_f32 v32, v16, v20\n\t"
    "v_cvt_pk_bf16_f32 v33, v24, v28\n\t"
    "buffer_store_dwordx2 v[32:33], %[vout], s[56:59], s64 offen sc1\n\t"
    "v_cvt_pk_bf16_f32 v34, v17, v21\n\t"
    "v_cvt_pk_bf16_f32 v35, v25, v29\n\t"
    "buffer_store_dwordx2 v[34:35], %[vout], s[56:59], s64 offen offset:512 sc1\n\t"
    "v_cvt_pk_bf16_f32 v36, v18, v22\n\t"
    "v_cvt_pk_bf16_f32 v37, v26, v30\n\t"
    "s_xor_b32 s64, %[mx], 15360\n\t"
    "buffer_store_dwordx2 v[36:37], %[vout], s[56:59], s64 offen sc1\n\t"
    "v_cvt_pk_bf16_f32 v38, v19, v23\n\t"
    "v_cvt_pk_bf16_f32 v39, v27, v31\n\t"
    "buffer_store_dwordx2 v[38:39], %[vout], s[56:59], s64 offen offset:512 sc1\n\t"
    "CEND_%=:\n\t"
      :
      : [px] "s"(x), [pw] "s"(w), [po] "s"(wo), [pout] "s"(out), [xo] "s"(xo), [vxo] "v"(vxo),
        [vxh] "v"(vxh), [vwo] "v"(vwo), [vxa] "v"(vxa), [vxb] "v"(vxb),
        [vwo2] "v"(vwo2), [vzw0] "v"(vzw0), [vzw1] "v"(vzw1), [vzr0] "v"(vzr0), [vzr1] "v"(vzr1),
        [vout] "v"(vout),  [sw] "s"(wv), [sgid] "s"(gid), [slds] "s"(slds), [tb] "s"(tb), [gx] "s"(gx), [gs] "s"(gs), [gxh] "s"(gxh), [mx] "s"(mx)
      :
      "v4", "v5", "v6", "v7", "v8", "v9", "v10", "v11", "v12", "v13", "v14", "v15", "v16", "v17", "v18", "v19",
      "v20", "v21", "v22", "v23", "v24", "v25", "v26", "v27", "v28", "v29", "v30", "v31", "v32", "v33", "v34", "v35",
      "v36", "v37", "v38", "v39", "v40", "v41", "v42", "v43", "v44", "v45", "v46", "v47", "v48", "v49", "v50", "v51",
      "v52", "v53", "v54", "v55", "v56", "v57", "v58", "v59", "v60", "v61", "v62", "v63", "v64", "v65", "v66", "v67",
      "v68", "v69", "v70", "v71", "v72", "v73", "v74", "v75", "v76", "v77", "v78", "v79", "v80", "v81", "v82", "v83",
      "v84", "v85", "v86", "v87", "v88", "v89", "v90", "v91", "v92", "v93", "v94", "v95", "v96", "v97", "v98", "v99",
      "v100", "v101", "v102", "v103", "v104", "v105", "v106", "v107", "v108", "v109", "v110", "v111", "v112", "v113", "v114", "v115",
      "v116", "v117", "v118", "v119", "v120", "v121", "v122", "v123", "v124", "v125", "v126", "v127", "v128", "v129", "v130", "v131",
      "v132", "v133", "v134", "v135", "v136", "v137", "v138", "v139", "v140", "v141", "v142", "v143", "v144", "v145", "v146", "v147",
      "v148", "v149", "v150", "v151", "v152", "v153", "v154", "v155", "v156", "v157", "v158", "v159", "v160", "v161", "v162", "v163",
      "v164", "v165", "v166", "v167", "v168", "v169", "v170", "v171", "v172", "v173", "v174", "v175", "v176", "v177", "v178", "v179",
      "v180", "v181", "v182", "v183", "v184", "v185", "v186", "v187", "v188", "v189", "v190", "v191", "v192", "v193", "v194", "v195",
      "v196", "v197", "v198", "v199", "v200", "v201", "v202", "v203", "v204", "v205", "v206", "v207", "v208", "v209", "v210", "v211",
      "v212", "v213", "v214", "v215", "v216", "v217", "v218", "v219", "v220", "v221", "v222", "v223", "v224", "v225", "v226", "v227",
      "v228", "v229", "v230", "v231", "v232", "v233", "v234", "v235", "v236", "v237", "v238", "v239", "a0", "a1", "a2", "a3",
      "a4", "a5", "a6", "a7", "a8", "a9", "a10", "a11", "a12", "a13", "a14", "a15", "a16", "a17", "a18", "a19",
      "a20", "a21", "a22", "a23", "a24", "a25", "a26", "a27", "a28", "a29", "a30", "a31", "a32", "a33", "a34", "a35",
      "a36", "a37", "a38", "a39", "a40", "a41", "a42", "a43", "a44", "a45", "a46", "a47", "a48", "a49", "a50", "a51",
      "a52", "a53", "a54", "a55", "a56", "a57", "a58", "a59", "a60", "a61", "a62", "a63", "a64", "a65", "a66", "a67",
      "a68", "a69", "a70", "a71", "a72", "a73", "a74", "a75", "a76", "a77", "a78", "a79", "a80", "a81", "a82", "a83",
      "a84", "a85", "a86", "a87", "a88", "a89", "a90", "a91", "a92", "a93", "a94", "a95", "a96", "a97", "a98", "a99",
      "a100", "a101", "a102", "a103", "a104", "a105", "a106", "a107", "a108", "a109", "a110", "a111", "a112", "a113", "a114", "a115",
      "a116", "a117", "a118", "a119", "a120", "a121", "a122", "a123", "a124", "a125", "a126", "a127", "a128", "a129", "a130", "a131",
      "a132", "a133", "a134", "a135", "a136", "a137", "a138", "a139", "a140", "a141", "a142", "a143", "a144", "a145", "a146", "a147",
      "a148", "a149", "a150", "a151", "a152", "a153", "a154", "a155", "a156", "a157", "a158", "a159", "a160", "a161", "a162", "a163",
      "a164", "a165", "a166", "a167", "a168", "a169", "a170", "a171", "a172", "a173", "a174", "a175", "a176", "a177", "a178", "a179",
      "a180", "a181", "a182", "a183", "a184", "a185", "a186", "a187", "a188", "a189", "a190", "a191", "a192", "a193", "a194", "a195",
      "a196", "a197", "a198", "a199", "a200", "a201", "a202", "a203", "a204", "a205", "a206", "a207", "a208", "a209", "a210", "a211",
      "a212", "a213", "a214", "a215", "a216", "a217", "a218", "a219", "a220", "a221", "a222", "a223", "a224", "a225", "a226", "a227",
      "a228", "a229", "a230", "a231", "a232", "a233", "a234", "a235", "a236", "a237", "a238", "a239", "a240", "a241", "a242", "a243",
      "a244", "a245", "a246", "a247", "a248", "a249", "a250", "a251", "a252", "a253", "a254", "a255", "s60", "s61", "s62", "s63",
      "s64", "s65", "s66", "s67", "s68", "s69", "s70", "s71", "s72", "s73", "s74", "s75", "s76", "s77", "s78", "s79",
      "s80", "s81", "s82", "s83", "s84", "s85", "s86", "s87", "s88", "s89", "s56", "s57", "s58", "s59", "s90", "s91",
      "s92", "s93", "s94", "s95", "s96", "s97", "s98", "s99", "m0", "scc", "memory"

  );
}

extern "C" void kernel_launch(void* const* d_in, const int* in_sizes, int n_in, void* d_out, int out_size,
                              void* d_ws, size_t ws_size, hipStream_t stream) {
  const unsigned short* x = (const unsigned short*)d_in[0];
  const unsigned short* w = (const unsigned short*)d_in[1];
  const unsigned short* wo = (const unsigned short*)d_in[2];
  unsigned short* out = (unsigned short*)d_out;
  wavenet_fused<<<2 * TILES_PER_B, 256, 0, stream>>>(x, w, wo, out);
}
```
